```python
import torch

def _softmax(x, axis=-1):
    return torch.softmax(x, dim=axis)

def _gelu(x):
    return torch.nn.functional.gelu(x, approximate='tanh')

def _silu(x):
    return torch.nn.functional.silu(x)

def _sigmoid(x):
    return torch.sigmoid(x)

def _layernorm(x, axis=-1, eps=1e-05):
    m = x.mean(dim=axis, keepdim=True)
    v = x.var(dim=axis, keepdim=True, unbiased=False)
    return (x - m) / torch.sqrt(v + eps)

def _scatter_add(tgt, idx, src, axis=0):
    return tgt.clone().index_add_(axis, idx, src)

def _segment_sum(src, seg, n):
    out = torch.zeros((n,) + src.shape[1:], dtype=src.dtype)
    return out.index_add_(0, seg, src)

def _pad(x, pw):
    flat = tuple((v for pair in reversed(pw) for v in pair))
    return torch.nn.functional.pad(x, flat)
kernel_name = 'sequential_preLN__memory_aug_recurrent_mem_tr__swiglu_ffn__prompt'

def setup_inputs():

    _w = lambda a: (lambda t: t.to(torch.bfloat16) if t.is_floating_point() else t)(torch.as_tensor(a))

    def _A_setup():
        B, S, Dm = (1, 2048, 1024)
        H, Dh = (32, 128)
        M, seg = (8, 32)
        seg = min(seg, S)
        S = S // seg * seg
        return {'x': _w((lambda *s: torch.randn(*s, dtype=torch.bfloat16) * 0.05)(B, S, Dm)), 'mem0': _w((lambda *s: torch.randn(*s, dtype=torch.bfloat16) * 0.2)(B, M, Dm)), 'Wq': _w((lambda *s: torch.randn(*s, dtype=torch.bfloat16) * 1.6)(Dm, H, Dh) * Dm ** (-0.5)), 'Wk': _w((lambda *s: torch.randn(*s, dtype=torch.bfloat16) * 1.6)(Dm, H, Dh) * Dm ** (-0.5)), 'Wv': _w((lambda *s: torch.randn(*s, dtype=torch.bfloat16) * 0.2)(Dm, H, Dh) * Dm ** (-0.5)), 'Wo': _w((lambda *s: torch.randn(*s, dtype=torch.bfloat16) * 0.2)(H, Dh, Dm) * (H * Dh) ** (-0.5)), 'seg_len': seg}
    _A_in = _A_setup()

    def _B_setup():
        B, S, D, F = (1, 2048, 1024, 3584)
        return {'x': _w((lambda *s: torch.randn(*s, dtype=torch.bfloat16) * 0.05)(B, S, D)), 'w_gate': _w((lambda *s: torch.randn(*s, dtype=torch.bfloat16) * 0.05)(D, F)), 'w_up': _w((lambda *s: torch.randn(*s, dtype=torch.bfloat16) * 0.05)(D, F)), 'w_down': _w((lambda *s: torch.randn(*s, dtype=torch.bfloat16) * 0.05)(F, D))}
    _B_in = _B_setup()
    return {'A_x': _A_in['x'], 'A_mem0': _A_in['mem0'], 'A_Wq': _A_in['Wq'], 'A_Wk': _A_in['Wk'], 'A_Wv': _A_in['Wv'], 'A_Wo': _A_in['Wo'], 'A_seg_len': _A_in['seg_len'], 'B_x': _B_in['x'], 'B_w_gate': _B_in['w_gate'], 'B_w_up': _B_in['w_up'], 'B_w_down': _B_in['w_down']}

def reference(A_x, A_mem0, A_Wq, A_Wk, A_Wv, A_Wo, A_seg_len, B_x, B_w_gate, B_w_up, B_w_down):

    def _A_ref(x, mem0, Wq, Wk, Wv, Wo, seg_len):
        B, S, Dm = x.shape
        H, Dh = (Wq.shape[1], Wq.shape[2])
        M = mem0.shape[1]
        scale = Dh ** (-0.5)
        n_seg = S // seg_len
        L = M + seg_len
        idx = torch.arange(L)
        mask = (idx[:, None] < M) | (idx[:, None] >= idx[None, :])
        mem = mem0
        outs = []
        for s in range(n_seg):
            xs = x[:, s * seg_len:(s + 1) * seg_len, :].clone()
            h = torch.concatenate([mem, xs], axis=1)
            q = torch.einsum('btd,dhe->bhte', h, Wq)
            k = torch.einsum('btd,dhe->bhte', h, Wk)
            v = torch.einsum('btd,dhe->bhte', h, Wv)
            scores = torch.einsum('bhqe,bhke->bhqk', q, k) * scale
            scores = torch.where(mask[None, None], scores, -1000000000.0)
            p = _softmax(scores, axis=-1)
            o = torch.einsum('bhqk,bhke->bhqe', p, v)
            o = torch.einsum('bhte,hed->btd', o, Wo)
            mem = o[:, :M, :].clone()
            outs.append(o[:, M:, :])
        return torch.concatenate(outs, axis=1)

    def _B_ref(x, w_gate, w_up, w_down):
        g = _silu(x @ w_gate)
        u = x @ w_up
        return g * u @ w_down
    x = A_x
    h = x + _A_ref(_layernorm(x, axis=-1), A_mem0, A_Wq, A_Wk, A_Wv, A_Wo, A_seg_len)
    h2 = h + _B_ref(_layernorm(h, axis=-1), B_w_gate, B_w_up, B_w_down)
    return h2
```

```cpp
#include <hip/hip_runtime.h>
#include <hip/hip_bf16.h>
typedef unsigned short u16;
typedef short bf16x8 __attribute__((ext_vector_type(8)));
typedef float f32x4 __attribute__((ext_vector_type(4)));
__device__ __forceinline__ float bf2f(u16 x) { return __uint_as_float((unsigned)x << 16); }
__device__ __forceinline__ u16 f2bf(float f) { unsigned u = __float_as_uint(f); u += 0x7fffu + ((u >> 16) & 1u); return (u16)(u >> 16); }
constexpr int S = 2048, D = 1024, H = 32, DH = 128, HD = 4096, M = 8, SEG = 32, NSEG = 64, F = 3584;

__global__ void __launch_bounds__(256) ln_k(const u16* x, u16* y) {
  int r = blockIdx.x, t = threadIdx.x; __shared__ float red[8];
  float v[4]; float s = 0; for (int i = 0; i < 4; ++i) { v[i] = bf2f(x[r * D + t * 4 + i]); s += v[i]; }
  for (int o = 32; o; o >>= 1) s += __shfl_xor(s, o);
  if ((t & 63) == 0) red[t >> 6] = s; __syncthreads(); float m = (red[0] + red[1] + red[2] + red[3]) / D;
  float q = 0; for (int i = 0; i < 4; ++i) q += (v[i] - m) * (v[i] - m);
  for (int o = 32; o; o >>= 1) q += __shfl_xor(q, o);
  if ((t & 63) == 0) red[4 + (t >> 6)] = q; __syncthreads(); float var = (red[4] + red[5] + red[6] + red[7]) / D;
  float rs = 1.0f / sqrtf(var + 1e-5f);
  for (int i = 0; i < 4; ++i) y[r * D + t * 4 + i] = f2bf((v[i] - m) * rs);
}
template <int EPI>
__global__ void __launch_bounds__(256) gemm_k(const u16* A, int lda, const u16* B, const u16* B2, int ldb, u16* C, int ldc, const u16* R, int Mr, int K) {
  __shared__ u16 As[64][40], Bs[64][40], Bs2[64][40];
  int bm = blockIdx.y * 64, bn = blockIdx.x * 64, t = threadIdx.x, w = t >> 6, l = t & 63;
  f32x4 acc[4] = {}, acc2[4] = {};
  for (int k0 = 0; k0 < K; k0 += 32) {
    { int r = t >> 2, c = (t & 3) * 8; for (int i = 0; i < 8; ++i) As[r][c + i] = (bm + r < Mr) ? A[(long)(bm + r) * lda + k0 + c + i] : 0; }
    { int kk = t >> 3, c = (t & 7) * 8; for (int i = 0; i < 8; ++i) { Bs[c + i][kk] = B[(long)(k0 + kk) * ldb + bn + c + i]; if (EPI == 2) Bs2[c + i][kk] = B2[(long)(k0 + kk) * ldb + bn + c + i]; } }
    __syncthreads();
    bf16x8 a; for (int i = 0; i < 8; ++i) a[i] = As[w * 16 + (l & 15)][(l >> 4) * 8 + i];
    for (int n = 0; n < 4; ++n) { bf16x8 b; for (int i = 0; i < 8; ++i) b[i] = Bs[n * 16 + (l & 15)][(l >> 4) * 8 + i];
      acc[n] = __builtin_amdgcn_mfma_f32_16x16x32_bf16(a, b, acc[n], 0, 0, 0);
      if (EPI == 2) { for (int i = 0; i < 8; ++i) b[i] = Bs2[n * 16 + (l & 15)][(l >> 4) * 8 + i]; acc2[n] = __builtin_amdgcn_mfma_f32_16x16x32_bf16(a, b, acc2[n], 0, 0, 0); } }
    __syncthreads();
  }
  for (int n = 0; n < 4; ++n) for (int j = 0; j < 4; ++j) { int r = bm + w * 16 + (l >> 4) * 4 + j, c = bn + n * 16 + (l & 15); if (r >= Mr) continue;
    float v = acc[n][j];
    if (EPI == 1) v = bf2f(R[(long)r * ldc + c]) + bf2f(f2bf(v));
    if (EPI == 2) { float g = v; v = g / (1.0f + __expf(-g)) * acc2[n][j]; }
    C[(long)r * ldc + c] = f2bf(v); }
}
__global__ void __launch_bounds__(256) attn_k(const u16* Q, const u16* Kt, const u16* Vt, const u16* QM, const u16* KM, const u16* VM, u16* O, u16* OM, int seg0, int which) {
  int h = blockIdx.x, s = seg0 + blockIdx.y, t = threadIdx.x; __shared__ float ks[40][129], vs[40][129], qs[32][129], p[32][41];
  for (int i = t; i < 40 * 128; i += 256) { int j = i >> 7, e = i & 127;
    ks[j][e] = bf2f(j < 8 ? KM[((long)s * 8 + j) * HD + h * 128 + e] : Kt[((long)s * 32 + j - 8) * HD + h * 128 + e]);
    vs[j][e] = bf2f(j < 8 ? VM[((long)s * 8 + j) * HD + h * 128 + e] : Vt[((long)s * 32 + j - 8) * HD + h * 128 + e]); }
  int nq = which ? 32 : 8;
  for (int i = t; i < nq * 128; i += 256) { int j = i >> 7, e = i & 127; qs[j][e] = bf2f(which ? Q[((long)s * 32 + j) * HD + h * 128 + e] : QM[(long)j * HD + h * 128 + e]); }
  __syncthreads();
  for (int i = t; i < nq * 40; i += 256) { int q = i / 40, j = i % 40; float a = 0; for (int e = 0; e < 128; ++e) a += qs[q][e] * ks[j][e];
    a *= 0.08838834764831845f; if (which && j > q + 8) a = -1e9f; p[q][j] = a; }
  __syncthreads();
  if (t < nq) { float m = -3e38f; for (int j = 0; j < 40; ++j) m = fmaxf(m, p[t][j]); float sum = 0; for (int j = 0; j < 40; ++j) { float e = __expf(p[t][j] - m); p[t][j] = e; sum += e; }
    for (int j = 0; j < 40; ++j) p[t][j] = bf2f(f2bf(p[t][j] / sum)); }
  __syncthreads();
  for (int i = t; i < nq * 128; i += 256) { int q = i >> 7, e = i & 127; float a = 0; for (int j = 0; j < 40; ++j) a += p[q][j] * vs[j][e];
    if (which) O[((long)s * 32 + q) * HD + h * 128 + e] = f2bf(a); else OM[(long)q * HD + h * 128 + e] = f2bf(a); }
}
extern "C" void kernel_launch(void* const* d_in, const int* in_sizes, int n_in, void* d_out, int out_size, void* d_ws, size_t ws_size, hipStream_t st) {
  const u16 *x = (const u16*)d_in[0], *mem0 = (const u16*)d_in[1], *Wq = (const u16*)d_in[2], *Wk = (const u16*)d_in[3], *Wv = (const u16*)d_in[4], *Wo = (const u16*)d_in[5];
  const u16 *Wg = (const u16*)d_in[8], *Wu = (const u16*)d_in[9], *Wd = (const u16*)d_in[10];
  u16* out = (u16*)d_out; u16* w = (u16*)d_ws;
  u16* xn = w; w += S * D; u16* Q = w; w += (long)S * HD; u16* K = w; w += (long)S * HD; u16* V = w; w += (long)S * HD; u16* O = w; w += (long)S * HD;
  u16* h = w; w += S * D; u16* hn = w; w += S * D; u16* act = w; w += (long)S * F;
  u16* MEM = w; w += (NSEG + 1) * M * D; u16* QM = w; w += M * HD; u16* KM = w; w += (long)NSEG * M * HD; u16* VM = w; w += (long)NSEG * M * HD; u16* OM = w; w += M * HD;
  ln_k<<<S, 256, 0, st>>>(x, xn);
  gemm_k<0><<<dim3(HD / 64, S / 64), 256, 0, st>>>(xn, D, Wq, nullptr, HD, Q, HD, nullptr, S, D);
  gemm_k<0><<<dim3(HD / 64, S / 64), 256, 0, st>>>(xn, D, Wk, nullptr, HD, K, HD, nullptr, S, D);
  gemm_k<0><<<dim3(HD / 64, S / 64), 256, 0, st>>>(xn, D, Wv, nullptr, HD, V, HD, nullptr, S, D);
  hipMemcpyAsync(MEM, mem0, M * D * 2, hipMemcpyDeviceToDevice, st);
  for (int s = 0; s < NSEG; ++s) {
    const u16* m = MEM + (long)s * M * D;
    gemm_k<0><<<dim3(HD / 64, 1), 256, 0, st>>>(m, D, Wk, nullptr, HD, KM + (long)s * M * HD, HD, nullptr, M, D);
    gemm_k<0><<<dim3(HD / 64, 1), 256, 0, st>>>(m, D, Wv, nullptr, HD, VM + (long)s * M * HD, HD, nullptr, M, D);
    if (s == NSEG - 1) break;
    gemm_k<0><<<dim3(HD / 64, 1), 256, 0, st>>>(m, D, Wq, nullptr, HD, QM, HD, nullptr, M, D);
    attn_k<<<dim3(H, 1), 256, 0, st>>>(Q, K, V, QM, KM, VM, O, OM, s, 0);
    gemm_k<0><<<dim3(D / 64, 1), 256, 0, st>>>(OM, HD, Wo, nullptr, D, MEM + (long)(s + 1) * M * D, D, nullptr, M, HD);
  }
  attn_k<<<dim3(H, NSEG), 256, 0, st>>>(Q, K, V, QM, KM, VM, O, OM, 0, 1);
  gemm_k<1><<<dim3(D / 64, S / 64), 256, 0, st>>>(O, HD, Wo, nullptr, D, h, D, x, S, HD);
  ln_k<<<S, 256, 0, st>>>(h, hn);
  gemm_k<2><<<dim3(F / 64, S / 64), 256, 0, st>>>(hn, D, Wg, Wu, F, act, F, nullptr, S, D);
  gemm_k<1><<<dim3(D / 64, S / 64), 256, 0, st>>>(act, F, Wd, nullptr, D, out, D, h, S, F);
}
```

```cpp
#include <hip/hip_runtime.h>
#include <hip/hip_bf16.h>
typedef unsigned short u16;
#define RFL(x) ((unsigned)__builtin_amdgcn_readfirstlane((int)(x)))
__device__ __forceinline__ unsigned lane_id() { unsigned l; asm volatile("v_mbcnt_lo_u32_b32 %0, -1, 0\n v_mbcnt_hi_u32_b32 %0, -1, %0" : "=v"(l)); return l; }
struct GP { unsigned kind, aoff, lda, bn, ldb, nk, o0, o1; unsigned long long bp; };
constexpr unsigned XN = 94371840u;
constexpr unsigned QB = 4194304u;
constexpr unsigned KB = 20971520u;
constexpr unsigned VB = 37748736u;
constexpr unsigned OATT = 54525952u;
constexpr unsigned HB = 71303168u;
constexpr unsigned HN = 75497472u;
constexpr unsigned ACT = 79691776u;
constexpr unsigned MEMB = 98566144u;
constexpr unsigned OB = 100663296u;
constexpr unsigned VMB = 105906176u;
constexpr unsigned KMB = 111149056u;
constexpr unsigned CNT = 115343360u;
constexpr unsigned DBG = 116391936u;
constexpr unsigned PART = 117440512u;
constexpr unsigned PART2 = 188743680u;

__device__ __forceinline__ void gemm_L(char* ws, int wave, const GP& P) {
  const unsigned kind = RFL(P.kind), aoff = RFL(P.aoff), lda = RFL(P.lda), bn = RFL(P.bn), ldb = RFL(P.ldb), nk = RFL(P.nk), o0 = RFL(P.o0), o1 = RFL(P.o1);
  const unsigned long long bp = ((unsigned long long)RFL((unsigned)(P.bp >> 32)) << 32) | RFL((unsigned)P.bp);
  const unsigned l = lane_id(), w = wave, wm = w >> 1, wn = w & 1;
  const unsigned i16 = l & 15, g = l >> 4, q = (l >> 2) & 3, p = l & 3, r = l / 16;
  const unsigned c0 = (l & 7) ^ g, va0 = 2048 + 4 * w * lda + (l >> 3) * 64 + (c0 >> 2) * 1024 + (c0 & 3) * 16;
  const unsigned vb0 = (32 * wn + r) * ldb + (((l % 16) ^ (2 * (r & 3))) * 16);
  const unsigned vx0 = wm * 16384 + i16 * 128 + ((g ^ (i16 >> 1)) * 16);
  const unsigned vw0 = (8 * g + q) * 256 + (((4 * wn) ^ (q | ((g & 1) << 2))) * 32) + 8 * p;
  const unsigned gp = ((g & 1) << 1) | (g >> 1);
  const unsigned vst = kind == 0 ? gp * 256 + i16 * 16 : kind == 1 ? l * 16 : i16 * 64 + gp * 16;
  asm volatile(
"s_mov_b64 s[36:37], %[ws]\n"
"s_and_b32 s37, s37, 0xffff\n"
"s_mov_b32 s38, 0xf000000\n"
"s_mov_b32 s39, 0x20000\n"
"s_mov_b64 s[40:41], %[bp]\n"
"s_and_b32 s41, s41, 0xffff\n"
"s_mov_b32 s42, %[bn]\n"
"s_mov_b32 s43, 0x20000\n"
"s_mov_b64 s[56:57], %[ws]\n"
"s_and_b32 s57, s57, 0xffff\n"
"s_mov_b32 s58, 0xf000000\n"
"s_mov_b32 s59, 0x20000\n"
"s_sub_u32 s44, %[aoff], 0x800\n"
"s_mov_b32 s45, 0\n"
"s_mov_b32 s47, %[nk]\n"
"s_lshl_b32 s48, %[ldb], 6\n"
"s_mov_b32 s49, %[wave]\n"
"s_mul_i32 s60, s49, 8192\n"
"s_add_u32 s61, s60, 4096\n"
"s_mul_i32 s62, s49, 8192\n"
"s_add_u32 s62, s62, 32768\n"
"s_add_u32 s63, s62, 4096\n"
"v_mov_b32 v24, %[va0]\n"
"v_add_u32 v26, %[lda], v24\n"
"v_add_u32 v28, %[lda], v26\n"
"v_add_u32 v30, %[lda], v28\n"
"v_xor_b32 v25, 0x400, v24\n"
"v_add_u32 v25, 0x200, v25\n"
"v_xor_b32 v27, 0x400, v26\n"
"v_add_u32 v27, 0x200, v27\n"
"v_xor_b32 v29, 0x400, v28\n"
"v_add_u32 v29, 0x200, v29\n"
"v_xor_b32 v31, 0x400, v30\n"
"v_add_u32 v31, 0x200, v31\n"
"v_subrev_u32 v25, 1024, v25\n"
"v_subrev_u32 v26, 2048, v26\n"
"v_subrev_u32 v27, 3072, v27\n"
"v_subrev_u32 v29, 1024, v29\n"
"v_subrev_u32 v30, 2048, v30\n"
"v_subrev_u32 v31, 3072, v31\n"
"s_mul_i32 s55, %[ldb], 4\n"
"v_mov_b32 v32, %[vb0]\n"
"v_add_u32 v33, s55, v32\n"
"v_add_u32 v34, s55, v33\n"
"v_xor_b32 v34, 128, v34\n"
"v_add_u32 v35, s55, v34\n"
"v_add_u32 v36, s55, v35\n"
"v_xor_b32 v36, 128, v36\n"
"v_add_u32 v37, s55, v36\n"
"v_add_u32 v38, s55, v37\n"
"v_xor_b32 v38, 128, v38\n"
"v_add_u32 v39, s55, v38\n"
"v_subrev_u32 v33, 1024, v33\n"
"v_subrev_u32 v34, 2048, v34\n"
"v_subrev_u32 v35, 3072, v35\n"
"v_subrev_u32 v37, 1024, v37\n"
"v_subrev_u32 v38, 2048, v38\n"
"v_subrev_u32 v39, 3072, v39\n"
"v_mov_b32 v46, %[vx0]\n"
"v_xor_b32 v47, 64, %[vx0]\n"
"v_xor_b32 v48, 0, %[vw0]\n"
"v_xor_b32 v49, 32, %[vw0]\n"
"v_xor_b32 v50, 64, %[vw0]\n"
"v_xor_b32 v51, 96, %[vw0]\n"
"s_cmp_lg_u32 %[kind], 1\n"
"s_cbranch_scc1 SW_%=\n"
"s_mov_b32 s50, 0\n"
"s_mov_b32 s51, 0\n"
"s_add_u32 m0, s50, s60\n"
"s_nop 0\n"
"buffer_load_dwordx4 v24, s[36:39], s44 offen offset:0 sc1 lds\n"
"buffer_load_dwordx4 v25, s[36:39], s44 offen offset:1024 sc1 lds\n"
"buffer_load_dwordx4 v26, s[36:39], s44 offen offset:2048 sc1 lds\n"
"buffer_load_dwordx4 v27, s[36:39], s44 offen offset:3072 sc1 lds\n"
"s_add_u32 m0, s50, s61\n"
"s_nop 0\n"
"buffer_load_dwordx4 v28, s[36:39], s44 offen offset:0 sc1 lds\n"
"buffer_load_dwordx4 v29, s[36:39], s44 offen offset:1024 sc1 lds\n"
"buffer_load_dwordx4 v30, s[36:39], s44 offen offset:2048 sc1 lds\n"
"buffer_load_dwordx4 v31, s[36:39], s44 offen offset:3072 sc1 lds\n"
"s_add_u32 m0, s50, s62\n"
"s_nop 0\n"
"buffer_load_dwordx4 v32, s[40:43], s45 offen offset:0 lds\n"
"buffer_load_dwordx4 v33, s[40:43], s45 offen offset:1024 lds\n"
"buffer_load_dwordx4 v34, s[40:43], s45 offen offset:2048 lds\n"
"buffer_load_dwordx4 v35, s[40:43], s45 offen offset:3072 lds\n"
"s_add_u32 m0, s50, s63\n"
"s_nop 0\n"
"buffer_load_dwordx4 v36, s[40:43], s45 offen offset:0 lds\n"
"buffer_load_dwordx4 v37, s[40:43], s45 offen offset:1024 lds\n"
"buffer_load_dwordx4 v38, s[40:43], s45 offen offset:2048 lds\n"
"buffer_load_dwordx4 v39, s[40:43], s45 offen offset:3072 lds\n"
"s_add_u32 s44, s44, 2048\n"
"s_add_u32 s45, s45, s48\n"
"s_add_u32 s50, s50, 65536\n"
"s_and_b32 s50, s50, 131071\n"
"v_mov_b64 v[128:129], 0\n"
"v_mov_b64 v[130:131], 0\n"
"v_mov_b64 v[136:137], 0\n"
"v_mov_b64 v[138:139], 0\n"
"v_mov_b64 v[144:145], 0\n"
"v_mov_b64 v[146:147], 0\n"
"v_mov_b64 v[152:153], 0\n"
"v_mov_b64 v[154:155], 0\n"
"v_mov_b64 v[160:161], 0\n"
"v_mov_b64 v[162:163], 0\n"
"v_mov_b64 v[168:169], 0\n"
"v_mov_b64 v[170:171], 0\n"
"v_mov_b64 v[176:177], 0\n"
"v_mov_b64 v[178:179], 0\n"
"v_mov_b64 v[184:185], 0\n"
"v_mov_b64 v[186:187], 0\n"
"v_mov_b64 v[192:193], 0\n"
"v_mov_b64 v[194:195], 0\n"
"v_mov_b64 v[200:201], 0\n"
"v_mov_b64 v[202:203], 0\n"
"v_mov_b64 v[208:209], 0\n"
"v_mov_b64 v[210:211], 0\n"
"v_mov_b64 v[216:217], 0\n"
"v_mov_b64 v[218:219], 0\n"
"v_mov_b64 v[224:225], 0\n"
"v_mov_b64 v[226:227], 0\n"
"v_mov_b64 v[232:233], 0\n"
"v_mov_b64 v[234:235], 0\n"
"v_mov_b64 v[240:241], 0\n"
"v_mov_b64 v[242:243], 0\n"
"v_mov_b64 v[248:249], 0\n"
"v_mov_b64 v[250:251], 0\n"
"v_accvgpr_write_b32 a0, 0\n"
"v_accvgpr_write_b32 a1, 0\n"
"v_accvgpr_write_b32 a2, 0\n"
"v_accvgpr_write_b32 a3, 0\n"
"v_accvgpr_write_b32 a8, 0\n"
"v_accvgpr_write_b32 a9, 0\n"
"v_accvgpr_write_b32 a10, 0\n"
"v_accvgpr_write_b32 a11, 0\n"
"v_accvgpr_write_b32 a16, 0\n"
"v_accvgpr_write_b32 a17, 0\n"
"v_accvgpr_write_b32 a18, 0\n"
"v_accvgpr_write_b32 a19, 0\n"
"v_accvgpr_write_b32 a24, 0\n"
"v_accvgpr_write_b32 a25, 0\n"
"v_accvgpr_write_b32 a26, 0\n"
"v_accvgpr_write_b32 a27, 0\n"
"v_accvgpr_write_b32 a32, 0\n"
"v_accvgpr_write_b32 a33, 0\n"
"v_accvgpr_write_b32 a34, 0\n"
"v_accvgpr_write_b32 a35, 0\n"
"v_accvgpr_write_b32 a40, 0\n"
"v_accvgpr_write_b32 a41, 0\n"
"v_accvgpr_write_b32 a42, 0\n"
"v_accvgpr_write_b32 a43, 0\n"
"v_accvgpr_write_b32 a48, 0\n"
"v_accvgpr_write_b32 a49, 0\n"
"v_accvgpr_write_b32 a50, 0\n"
"v_accvgpr_write_b32 a51, 0\n"
"v_accvgpr_write_b32 a56, 0\n"
"v_accvgpr_write_b32 a57, 0\n"
"v_accvgpr_write_b32 a58, 0\n"
"v_accvgpr_write_b32 a59, 0\n"
"v_accvgpr_write_b32 a64, 0\n"
"v_accvgpr_write_b32 a65, 0\n"
"v_accvgpr_write_b32 a66, 0\n"
"v_accvgpr_write_b32 a67, 0\n"
"v_accvgpr_write_b32 a72, 0\n"
"v_accvgpr_write_b32 a73, 0\n"
"v_accvgpr_write_b32 a74, 0\n"
"v_accvgpr_write_b32 a75, 0\n"
"v_accvgpr_write_b32 a80, 0\n"
"v_accvgpr_write_b32 a81, 0\n"
"v_accvgpr_write_b32 a82, 0\n"
"v_accvgpr_write_b32 a83, 0\n"
"v_accvgpr_write_b32 a88, 0\n"
"v_accvgpr_write_b32 a89, 0\n"
"v_accvgpr_write_b32 a90, 0\n"
"v_accvgpr_write_b32 a91, 0\n"
"v_accvgpr_write_b32 a96, 0\n"
"v_accvgpr_write_b32 a97, 0\n"
"v_accvgpr_write_b32 a98, 0\n"
"v_accvgpr_write_b32 a99, 0\n"
"v_accvgpr_write_b32 a104, 0\n"
"v_accvgpr_write_b32 a105, 0\n"
"v_accvgpr_write_b32 a106, 0\n"
"v_accvgpr_write_b32 a107, 0\n"
"v_accvgpr_write_b32 a112, 0\n"
"v_accvgpr_write_b32 a113, 0\n"
"v_accvgpr_write_b32 a114, 0\n"
"v_accvgpr_write_b32 a115, 0\n"
"v_accvgpr_write_b32 a120, 0\n"
"v_accvgpr_write_b32 a121, 0\n"
"v_accvgpr_write_b32 a122, 0\n"
"v_accvgpr_write_b32 a123, 0\n"
"v_mov_b64 v[132:133], 0\n"
"v_mov_b64 v[134:135], 0\n"
"v_mov_b64 v[140:141], 0\n"
"v_mov_b64 v[142:143], 0\n"
"v_mov_b64 v[148:149], 0\n"
"v_mov_b64 v[150:151], 0\n"
"v_mov_b64 v[156:157], 0\n"
"v_mov_b64 v[158:159], 0\n"
"v_mov_b64 v[164:165], 0\n"
"v_mov_b64 v[166:167], 0\n"
"v_mov_b64 v[172:173], 0\n"
"v_mov_b64 v[174:175], 0\n"
"v_mov_b64 v[180:181], 0\n"
"v_mov_b64 v[182:183], 0\n"
"v_mov_b64 v[188:189], 0\n"
"v_mov_b64 v[190:191], 0\n"
"v_mov_b64 v[196:197], 0\n"
"v_mov_b64 v[198:199], 0\n"
"v_mov_b64 v[204:205], 0\n"
"v_mov_b64 v[206:207], 0\n"
"v_mov_b64 v[212:213], 0\n"
"v_mov_b64 v[214:215], 0\n"
"v_mov_b64 v[220:221], 0\n"
"v_mov_b64 v[222:223], 0\n"
"v_mov_b64 v[228:229], 0\n"
"v_mov_b64 v[230:231], 0\n"
"v_mov_b64 v[236:237], 0\n"
"v_mov_b64 v[238:239], 0\n"
"v_mov_b64 v[244:245], 0\n"
"v_mov_b64 v[246:247], 0\n"
"v_mov_b64 v[252:253], 0\n"
"v_mov_b64 v[254:255], 0\n"
"v_accvgpr_write_b32 a4, 0\n"
"v_accvgpr_write_b32 a5, 0\n"
"v_accvgpr_write_b32 a6, 0\n"
"v_accvgpr_write_b32 a7, 0\n"
"v_accvgpr_write_b32 a12, 0\n"
"v_accvgpr_write_b32 a13, 0\n"
"v_accvgpr_write_b32 a14, 0\n"
"v_accvgpr_write_b32 a15, 0\n"
"v_accvgpr_write_b32 a20, 0\n"
"v_accvgpr_write_b32 a21, 0\n"
"v_accvgpr_write_b32 a22, 0\n"
"v_accvgpr_write_b32 a23, 0\n"
"v_accvgpr_write_b32 a28, 0\n"
"v_accvgpr_write_b32 a29, 0\n"
"v_accvgpr_write_b32 a30, 0\n"
"v_accvgpr_write_b32 a31, 0\n"
"v_accvgpr_write_b32 a36, 0\n"
"v_accvgpr_write_b32 a37, 0\n"
"v_accvgpr_write_b32 a38, 0\n"
"v_accvgpr_write_b32 a39, 0\n"
"v_accvgpr_write_b32 a44, 0\n"
"v_accvgpr_write_b32 a45, 0\n"
"v_accvgpr_write_b32 a46, 0\n"
"v_accvgpr_write_b32 a47, 0\n"
"v_accvgpr_write_b32 a52, 0\n"
"v_accvgpr_write_b32 a53, 0\n"
"v_accvgpr_write_b32 a54, 0\n"
"v_accvgpr_write_b32 a55, 0\n"
"v_accvgpr_write_b32 a60, 0\n"
"v_accvgpr_write_b32 a61, 0\n"
"v_accvgpr_write_b32 a62, 0\n"
"v_accvgpr_write_b32 a63, 0\n"
"v_accvgpr_write_b32 a68, 0\n"
"v_accvgpr_write_b32 a69, 0\n"
"v_accvgpr_write_b32 a70, 0\n"
"v_accvgpr_write_b32 a71, 0\n"
"v_accvgpr_write_b32 a76, 0\n"
"v_accvgpr_write_b32 a77, 0\n"
"v_accvgpr_write_b32 a78, 0\n"
"v_accvgpr_write_b32 a79, 0\n"
"v_accvgpr_write_b32 a84, 0\n"
"v_accvgpr_write_b32 a85, 0\n"
"v_accvgpr_write_b32 a86, 0\n"
"v_accvgpr_write_b32 a87, 0\n"
"v_accvgpr_write_b32 a92, 0\n"
"v_accvgpr_write_b32 a93, 0\n"
"v_accvgpr_write_b32 a94, 0\n"
"v_accvgpr_write_b32 a95, 0\n"
"v_accvgpr_write_b32 a100, 0\n"
"v_accvgpr_write_b32 a101, 0\n"
"v_accvgpr_write_b32 a102, 0\n"
"v_accvgpr_write_b32 a103, 0\n"
"v_accvgpr_write_b32 a108, 0\n"
"v_accvgpr_write_b32 a109, 0\n"
"v_accvgpr_write_b32 a110, 0\n"
"v_accvgpr_write_b32 a111, 0\n"
"v_accvgpr_write_b32 a116, 0\n"
"v_accvgpr_write_b32 a117, 0\n"
"v_accvgpr_write_b32 a118, 0\n"
"v_accvgpr_write_b32 a119, 0\n"
"v_accvgpr_write_b32 a124, 0\n"
"v_accvgpr_write_b32 a125, 0\n"
"v_accvgpr_write_b32 a126, 0\n"
"v_accvgpr_write_b32 a127, 0\n"
"v_add_u32 v40, s51, v46\n"
"v_add_u32 v41, s51, v47\n"
"v_add_u32 v42, s51, v48\n"
"v_add_u32 v43, s51, v49\n"
"v_add_u32 v44, s51, v50\n"
"v_add_u32 v45, s51, v51\n"
"s_add_u32 m0, s50, s60\n"
"s_nop 0\n"
"buffer_load_dwordx4 v24, s[36:39], s44 offen offset:0 sc1 lds\n"
"buffer_load_dwordx4 v25, s[36:39], s44 offen offset:1024 sc1 lds\n"
"buffer_load_dwordx4 v26, s[36:39], s44 offen offset:2048 sc1 lds\n"
"buffer_load_dwordx4 v27, s[36:39], s44 offen offset:3072 sc1 lds\n"
"s_add_u32 m0, s50, s61\n"
"s_nop 0\n"
"buffer_load_dwordx4 v28, s[36:39], s44 offen offset:0 sc1 lds\n"
"buffer_load_dwordx4 v29, s[36:39], s44 offen offset:1024 sc1 lds\n"
"buffer_load_dwordx4 v30, s[36:39], s44 offen offset:2048 sc1 lds\n"
"buffer_load_dwordx4 v31, s[36:39], s44 offen offset:3072 sc1 lds\n"
"s_add_u32 m0, s50, s62\n"
"s_nop 0\n"
"buffer_load_dwordx4 v32, s[40:43], s45 offen offset:0 lds\n"
"buffer_load_dwordx4 v33, s[40:43], s45 offen offset:1024 lds\n"
"buffer_load_dwordx4 v34, s[40:43], s45 offen offset:2048 lds\n"
"buffer_load_dwordx4 v35, s[40:43], s45 offen offset:3072 lds\n"
"s_add_u32 m0, s50, s63\n"
"s_nop 0\n"
"buffer_load_dwordx4 v36, s[40:43], s45 offen offset:0 lds\n"
"buffer_load_dwordx4 v37, s[40:43], s45 offen offset:1024 lds\n"
"buffer_load_dwordx4 v38, s[40:43], s45 offen offset:2048 lds\n"
"buffer_load_dwordx4 v39, s[40:43], s45 offen offset:3072 lds\n"
"s_add_u32 s44, s44, 2048\n"
"s_add_u32 s45, s45, s48\n"
"s_add_u32 s50, s50, 65536\n"
"s_and_b32 s50, s50, 131071\n"
"s_waitcnt vmcnt(16)\n"
"s_barrier\n"
"ds_read_b64_tr_b16 a[160:161], v42 offset:32768\n"
"ds_read_b64_tr_b16 a[162:163], v42 offset:33792\n"
"ds_read_b128 a[128:131], v40 offset:0\n"
"ds_read_b128 a[132:135], v40 offset:2048\n"
"ds_read_b128 a[136:139], v40 offset:4096\n"
"ds_read_b128 a[140:143], v40 offset:6144\n"
"ds_read_b128 a[144:147], v40 offset:8192\n"
"ds_read_b128 a[148:151], v40 offset:10240\n"
"ds_read_b128 a[152:155], v40 offset:12288\n"
"ds_read_b128 a[156:159], v40 offset:14336\n"
"ds_read_b64_tr_b16 a[164:165], v43 offset:32768\n"
"ds_read_b64_tr_b16 a[166:167], v43 offset:33792\n"
"ds_read_b64_tr_b16 a[168:169], v44 offset:32768\n"
"ds_read_b64_tr_b16 a[170:171], v44 offset:33792\n"
"ds_read_b64_tr_b16 a[172:173], v45 offset:32768\n"
"ds_read_b64_tr_b16 a[174:175], v45 offset:33792\n"
"ds_read_b64_tr_b16 a[176:177], v42 offset:49152\n"
"ds_read_b64_tr_b16 a[178:179], v42 offset:50176\n"
"ds_read_b64_tr_b16 a[180:181], v43 offset:49152\n"
"ds_read_b64_tr_b16 a[182:183], v43 offset:50176\n"
"ds_read_b64_tr_b16 a[184:185], v44 offset:49152\n"
"ds_read_b64_tr_b16 a[186:187], v44 offset:50176\n"
"ds_read_b64_tr_b16 a[188:189], v45 offset:49152\n"
"ds_read_b64_tr_b16 a[190:191], v45 offset:50176\n"
"s_sub_u32 s46, s47, 2\n"
"s_waitcnt lgkmcnt(0)\n"
"KL_u_%=:\n"
"v_mfma_f32_16x16x32_bf16 v[128:131], a[128:131], a[160:163], v[128:131]\n"
"ds_read_b64_tr_b16 a[224:225], v42 offset:40960\n"
"v_mfma_f32_16x16x32_bf16 v[132:135], a[132:135], a[160:163], v[132:135]\n"
"ds_read_b64_tr_b16 a[226:227], v42 offset:41984\n"
"v_mfma_f32_16x16x32_bf16 v[136:139], a[136:139], a[160:163], v[136:139]\n"
"ds_read_b128 a[192:195], v41 offset:0\n"
"v_mfma_f32_16x16x32_bf16 v[140:143], a[140:143], a[160:163], v[140:143]\n"
"ds_read_b128 a[196:199], v41 offset:2048\n"
"v_mfma_f32_16x16x32_bf16 v[144:147], a[144:147], a[160:163], v[144:147]\n"
"ds_read_b128 a[200:203], v41 offset:4096\n"
"v_mfma_f32_16x16x32_bf16 v[148:151], a[148:151], a[160:163], v[148:151]\n"
"ds_read_b128 a[204:207], v41 offset:6144\n"
"v_mfma_f32_16x16x32_bf16 v[152:155], a[152:155], a[160:163], v[152:155]\n"
"ds_read_b128 a[208:211], v41 offset:8192\n"
"v_mfma_f32_16x16x32_bf16 v[156:159], a[156:159], a[160:163], v[156:159]\n"
"ds_read_b128 a[212:215], v41 offset:10240\n"
"v_mfma_f32_16x16x32_bf16 v[160:163], a[128:131], a[164:167], v[160:163]\n"
"ds_read_b128 a[216:219], v41 offset:12288\n"
"v_mfma_f32_16x16x32_bf16 v[164:167], a[132:135], a[164:167], v[164:167]\n"
"ds_read_b128 a[220:223], v41 offset:14336\n"
"v_mfma_f32_16x16x32_bf16 v[168:171], a[136:139], a[164:167], v[168:171]\n"
"ds_read_b64_tr_b16 a[228:229], v43 offset:40960\n"
"v_mfma_f32_16x16x32_bf16 v[172:175], a[140:143], a[164:167], v[172:175]\n"
"ds_read_b64_tr_b16 a[230:231], v43 offset:41984\n"
"v_mfma_f32_16x16x32_bf16 v[176:179], a[144:147], a[164:167], v[176:179]\n"
"ds_read_b64_tr_b16 a[232:233], v44 offset:40960\n"
"v_mfma_f32_16x16x32_bf16 v[180:183], a[148:151], a[164:167], v[180:183]\n"
"ds_read_b64_tr_b16 a[234:235], v44 offset:41984\n"
"v_mfma_f32_16x16x32_bf16 v[184:187], a[152:155], a[164:167], v[184:187]\n"
"ds_read_b64_tr_b16 a[236:237], v45 offset:40960\n"
"v_mfma_f32_16x16x32_bf16 v[188:191], a[156:159], a[164:167], v[188:191]\n"
"ds_read_b64_tr_b16 a[238:239], v45 offset:41984\n"
"v_mfma_f32_16x16x32_bf16 v[192:195], a[128:131], a[168:171], v[192:195]\n"
"ds_read_b64_tr_b16 a[240:241], v42 offset:57344\n"
"v_mfma_f32_16x16x32_bf16 v[196:199], a[132:135], a[168:171], v[196:199]\n"
"ds_read_b64_tr_b16 a[242:243], v42 offset:58368\n"
"v_mfma_f32_16x16x32_bf16 v[200:203], a[136:139], a[168:171], v[200:203]\n"
"ds_read_b64_tr_b16 a[244:245], v43 offset:57344\n"
"v_mfma_f32_16x16x32_bf16 v[204:207], a[140:143], a[168:171], v[204:207]\n"
"ds_read_b64_tr_b16 a[246:247], v43 offset:58368\n"
"v_mfma_f32_16x16x32_bf16 v[208:211], a[144:147], a[168:171], v[208:211]\n"
"ds_read_b64_tr_b16 a[248:249], v44 offset:57344\n"
"v_mfma_f32_16x16x32_bf16 v[212:215], a[148:151], a[168:171], v[212:215]\n"
"ds_read_b64_tr_b16 a[250:251], v44 offset:58368\n"
"v_mfma_f32_16x16x32_bf16 v[216:219], a[152:155], a[168:171], v[216:219]\n"
"ds_read_b64_tr_b16 a[252:253], v45 offset:57344\n"
"v_mfma_f32_16x16x32_bf16 v[220:223], a[156:159], a[168:171], v[220:223]\n"
"ds_read_b64_tr_b16 a[254:255], v45 offset:58368\n"
"v_mfma_f32_16x16x32_bf16 v[224:227], a[128:131], a[172:175], v[224:227]\n"
"v_mfma_f32_16x16x32_bf16 v[228:231], a[132:135], a[172:175], v[228:231]\n"
"v_mfma_f32_16x16x32_bf16 v[232:235], a[136:139], a[172:175], v[232:235]\n"
"v_mfma_f32_16x16x32_bf16 v[236:239], a[140:143], a[172:175], v[236:239]\n"
"s_add_u32 s51, s51, 65536\n"
"s_and_b32 s51, s51, 131071\n"
"v_mfma_f32_16x16x32_bf16 v[240:243], a[144:147], a[172:175], v[240:243]\n"
"v_mfma_f32_16x16x32_bf16 v[244:247], a[148:151], a[172:175], v[244:247]\n"
"s_waitcnt lgkmcnt(0)\n"
"s_barrier\n"
"v_mfma_f32_16x16x32_bf16 v[248:251], a[152:155], a[172:175], v[248:251]\n"
"s_add_u32 m0, s50, s60\n"
"v_mfma_f32_16x16x32_bf16 v[252:255], a[156:159], a[172:175], v[252:255]\n"
"buffer_load_dwordx4 v24, s[36:39], s44 offen offset:0 sc1 lds\n"
"v_mfma_f32_16x16x32_bf16 a[0:3], a[128:131], a[176:179], a[0:3]\n"
"v_mfma_f32_16x16x32_bf16 a[4:7], a[132:135], a[176:179], a[4:7]\n"
"v_mfma_f32_16x16x32_bf16 a[8:11], a[136:139], a[176:179], a[8:11]\n"
"v_mfma_f32_16x16x32_bf16 a[12:15], a[140:143], a[176:179], a[12:15]\n"
"buffer_load_dwordx4 v25, s[36:39], s44 offen offset:1024 sc1 lds\n"
"v_mfma_f32_16x16x32_bf16 a[16:19], a[144:147], a[176:179], a[16:19]\n"
"v_mfma_f32_16x16x32_bf16 a[20:23], a[148:151], a[176:179], a[20:23]\n"
"v_mfma_f32_16x16x32_bf16 a[24:27], a[152:155], a[176:179], a[24:27]\n"
"v_mfma_f32_16x16x32_bf16 a[28:31], a[156:159], a[176:179], a[28:31]\n"
"buffer_load_dwordx4 v26, s[36:39], s44 offen offset:2048 sc1 lds\n"
"v_mfma_f32_16x16x32_bf16 a[32:35], a[128:131], a[180:183], a[32:35]\n"
"v_mfma_f32_16x16x32_bf16 a[36:39], a[132:135], a[180:183], a[36:39]\n"
"v_mfma_f32_16x16x32_bf16 a[40:43], a[136:139], a[180:183], a[40:43]\n"
"v_mfma_f32_16x16x32_bf16 a[44:47], a[140:143], a[180:183], a[44:47]\n"
"buffer_load_dwordx4 v27, s[36:39], s44 offen offset:3072 sc1 lds\n"
"v_mfma_f32_16x16x32_bf16 a[48:51], a[144:147], a[180:183], a[48:51]\n"
"v_mfma_f32_16x16x32_bf16 a[52:55], a[148:151], a[180:183], a[52:55]\n"
"v_mfma_f32_16x16x32_bf16 a[56:59], a[152:155], a[180:183], a[56:59]\n"
"s_add_u32 m0, s50, s61\n"
"v_mfma_f32_16x16x32_bf16 a[60:63], a[156:159], a[180:183], a[60:63]\n"
"buffer_load_dwordx4 v28, s[36:39], s44 offen offset:0 sc1 lds\n"
"v_mfma_f32_16x16x32_bf16 a[64:67], a[128:131], a[184:187], a[64:67]\n"
"v_mfma_f32_16x16x32_bf16 a[68:71], a[132:135], a[184:187], a[68:71]\n"
"v_mfma_f32_16x16x32_bf16 a[72:75], a[136:139], a[184:187], a[72:75]\n"
"v_mfma_f32_16x16x32_bf16 a[76:79], a[140:143], a[184:187], a[76:79]\n"
"buffer_load_dwordx4 v29, s[36:39], s44 offen offset:1024 sc1 lds\n"
"v_mfma_f32_16x16x32_bf16 a[80:83], a[144:147], a[184:187], a[80:83]\n"
"v_mfma_f32_16x16x32_bf16 a[84:87], a[148:151], a[184:187], a[84:87]\n"
"v_mfma_f32_16x16x32_bf16 a[88:91], a[152:155], a[184:187], a[88:91]\n"
"v_mfma_f32_16x16x32_bf16 a[92:95], a[156:159], a[184:187], a[92:95]\n"
"buffer_load_dwordx4 v30, s[36:39], s44 offen offset:2048 sc1 lds\n"
"v_mfma_f32_16x16x32_bf16 a[96:99], a[128:131], a[188:191], a[96:99]\n"
"v_mfma_f32_16x16x32_bf16 a[100:103], a[132:135], a[188:191], a[100:103]\n"
"v_mfma_f32_16x16x32_bf16 a[104:107], a[136:139], a[188:191], a[104:107]\n"
"v_mfma_f32_16x16x32_bf16 a[108:111], a[140:143], a[188:191], a[108:111]\n"
"buffer_load_dwordx4 v31, s[36:39], s44 offen offset:3072 sc1 lds\n"
"v_mfma_f32_16x16x32_bf16 a[112:115], a[144:147], a[188:191], a[112:115]\n"
"v_mfma_f32_16x16x32_bf16 a[116:119], a[148:151], a[188:191], a[116:119]\n"
"v_mfma_f32_16x16x32_bf16 a[120:123], a[152:155], a[188:191], a[120:123]\n"
"s_add_u32 m0, s50, s62\n"
"v_mfma_f32_16x16x32_bf16 a[124:127], a[156:159], a[188:191], a[124:127]\n"
"buffer_load_dwordx4 v32, s[40:43], s45 offen offset:0 lds\n"
"v_mfma_f32_16x16x32_bf16 v[128:131], a[192:195], a[224:227], v[128:131]\n"
"v_mfma_f32_16x16x32_bf16 v[132:135], a[196:199], a[224:227], v[132:135]\n"
"v_mfma_f32_16x16x32_bf16 v[136:139], a[200:203], a[224:227], v[136:139]\n"
"v_mfma_f32_16x16x32_bf16 v[140:143], a[204:207], a[224:227], v[140:143]\n"
"buffer_load_dwordx4 v33, s[40:43], s45 offen offset:1024 lds\n"
"v_mfma_f32_16x16x32_bf16 v[144:147], a[208:211], a[224:227], v[144:147]\n"
"v_mfma_f32_16x16x32_bf16 v[148:151], a[212:215], a[224:227], v[148:151]\n"
"v_mfma_f32_16x16x32_bf16 v[152:155], a[216:219], a[224:227], v[152:155]\n"
"v_mfma_f32_16x16x32_bf16 v[156:159], a[220:223], a[224:227], v[156:159]\n"
"buffer_load_dwordx4 v34, s[40:43], s45 offen offset:2048 lds\n"
"v_mfma_f32_16x16x32_bf16 v[160:163], a[192:195], a[228:231], v[160:163]\n"
"v_mfma_f32_16x16x32_bf16 v[164:167], a[196:199], a[228:231], v[164:167]\n"
"v_mfma_f32_16x16x32_bf16 v[168:171], a[200:203], a[228:231], v[168:171]\n"
"v_add_u32 v40, s51, v46\n"
"v_mfma_f32_16x16x32_bf16 v[172:175], a[204:207], a[228:231], v[172:175]\n"
"buffer_load_dwordx4 v35, s[40:43], s45 offen offset:3072 lds\n"
"v_mfma_f32_16x16x32_bf16 v[176:179], a[208:211], a[228:231], v[176:179]\n"
"v_mfma_f32_16x16x32_bf16 v[180:183], a[212:215], a[228:231], v[180:183]\n"
"v_mfma_f32_16x16x32_bf16 v[184:187], a[216:219], a[228:231], v[184:187]\n"
"s_add_u32 m0, s50, s63\n"
"v_add_u32 v41, s51, v47\n"
"v_mfma_f32_16x16x32_bf16 v[188:191], a[220:223], a[228:231], v[188:191]\n"
"buffer_load_dwordx4 v36, s[40:43], s45 offen offset:0 lds\n"
"v_mfma_f32_16x16x32_bf16 v[192:195], a[192:195], a[232:235], v[192:195]\n"
"v_mfma_f32_16x16x32_bf16 v[196:199], a[196:199], a[232:235], v[196:199]\n"
"v_mfma_f32_16x16x32_bf16 v[200:203], a[200:203], a[232:235], v[200:203]\n"
"v_add_u32 v42, s51, v48\n"
"v_mfma_f32_16x16x32_bf16 v[204:207], a[204:207], a[232:235], v[204:207]\n"
"buffer_load_dwordx4 v37, s[40:43], s45 offen offset:1024 lds\n"
"v_mfma_f32_16x16x32_bf16 v[208:211], a[208:211], a[232:235], v[208:211]\n"
"v_mfma_f32_16x16x32_bf16 v[212:215], a[212:215], a[232:235], v[212:215]\n"
"v_mfma_f32_16x16x32_bf16 v[216:219], a[216:219], a[232:235], v[216:219]\n"
"v_add_u32 v43, s51, v49\n"
"v_mfma_f32_16x16x32_bf16 v[220:223], a[220:223], a[232:235], v[220:223]\n"
"buffer_load_dwordx4 v38, s[40:43], s45 offen offset:2048 lds\n"
"v_mfma_f32_16x16x32_bf16 v[224:227], a[192:195], a[236:239], v[224:227]\n"
"v_mfma_f32_16x16x32_bf16 v[228:231], a[196:199], a[236:239], v[228:231]\n"
"v_mfma_f32_16x16x32_bf16 v[232:235], a[200:203], a[236:239], v[232:235]\n"
"v_add_u32 v44, s51, v50\n"
"v_mfma_f32_16x16x32_bf16 v[236:239], a[204:207], a[236:239], v[236:239]\n"
"buffer_load_dwordx4 v39, s[40:43], s45 offen offset:3072 lds\n"
"v_mfma_f32_16x16x32_bf16 v[240:243], a[208:211], a[236:239], v[240:243]\n"
"v_mfma_f32_16x16x32_bf16 v[244:247], a[212:215], a[236:239], v[244:247]\n"
"v_mfma_f32_16x16x32_bf16 v[248:251], a[216:219], a[236:239], v[248:251]\n"
"v_add_u32 v45, s51, v51\n"
"v_mfma_f32_16x16x32_bf16 v[252:255], a[220:223], a[236:239], v[252:255]\n"
"v_mfma_f32_16x16x32_bf16 a[0:3], a[192:195], a[240:243], a[0:3]\n"
"v_mfma_f32_16x16x32_bf16 a[4:7], a[196:199], a[240:243], a[4:7]\n"
"s_waitcnt vmcnt(16)\n"
"s_barrier\n"
"v_mfma_f32_16x16x32_bf16 a[8:11], a[200:203], a[240:243], a[8:11]\n"
"ds_read_b64_tr_b16 a[160:161], v42 offset:32768\n"
"v_mfma_f32_16x16x32_bf16 a[12:15], a[204:207], a[240:243], a[12:15]\n"
"ds_read_b64_tr_b16 a[162:163], v42 offset:33792\n"
"v_mfma_f32_16x16x32_bf16 a[16:19], a[208:211], a[240:243], a[16:19]\n"
"ds_read_b128 a[128:131], v40 offset:0\n"
"v_mfma_f32_16x16x32_bf16 a[20:23], a[212:215], a[240:243], a[20:23]\n"
"ds_read_b128 a[132:135], v40 offset:2048\n"
"v_mfma_f32_16x16x32_bf16 a[24:27], a[216:219], a[240:243], a[24:27]\n"
"ds_read_b128 a[136:139], v40 offset:4096\n"
"v_mfma_f32_16x16x32_bf16 a[28:31], a[220:223], a[240:243], a[28:31]\n"
"ds_read_b128 a[140:143], v40 offset:6144\n"
"v_mfma_f32_16x16x32_bf16 a[32:35], a[192:195], a[244:247], a[32:35]\n"
"ds_read_b128 a[144:147], v40 offset:8192\n"
"v_mfma_f32_16x16x32_bf16 a[36:39], a[196:199], a[244:247], a[36:39]\n"
"ds_read_b128 a[148:151], v40 offset:10240\n"
"v_mfma_f32_16x16x32_bf16 a[40:43], a[200:203], a[244:247], a[40:43]\n"
"ds_read_b128 a[152:155], v40 offset:12288\n"
"v_mfma_f32_16x16x32_bf16 a[44:47], a[204:207], a[244:247], a[44:47]\n"
"ds_read_b128 a[156:159], v40 offset:14336\n"
"v_mfma_f32_16x16x32_bf16 a[48:51], a[208:211], a[244:247], a[48:51]\n"
"ds_read_b64_tr_b16 a[164:165], v43 offset:32768\n"
"v_mfma_f32_16x16x32_bf16 a[52:55], a[212:215], a[244:247], a[52:55]\n"
"ds_read_b64_tr_b16 a[166:167], v43 offset:33792\n"
"v_mfma_f32_16x16x32_bf16 a[56:59], a[216:219], a[244:247], a[56:59]\n"
"ds_read_b64_tr_b16 a[168:169], v44 offset:32768\n"
"v_mfma_f32_16x16x32_bf16 a[60:63], a[220:223], a[244:247], a[60:63]\n"
"ds_read_b64_tr_b16 a[170:171], v44 offset:33792\n"
"v_mfma_f32_16x16x32_bf16 a[64:67], a[192:195], a[248:251], a[64:67]\n"
"ds_read_b64_tr_b16 a[172:173], v45 offset:32768\n"
"v_mfma_f32_16x16x32_bf16 a[68:71], a[196:199], a[248:251], a[68:71]\n"
"ds_read_b64_tr_b16 a[174:175], v45 offset:33792\n"
"v_mfma_f32_16x16x32_bf16 a[72:75], a[200:203], a[248:251], a[72:75]\n"
"ds_read_b64_tr_b16 a[176:177], v42 offset:49152\n"
"v_mfma_f32_16x16x32_bf16 a[76:79], a[204:207], a[248:251], a[76:79]\n"
"ds_read_b64_tr_b16 a[178:179], v42 offset:50176\n"
"v_mfma_f32_16x16x32_bf16 a[80:83], a[208:211], a[248:251], a[80:83]\n"
"ds_read_b64_tr_b16 a[180:181], v43 offset:49152\n"
"v_mfma_f32_16x16x32_bf16 a[84:87], a[212:215], a[248:251], a[84:87]\n"
"ds_read_b64_tr_b16 a[182:183], v43 offset:50176\n"
"v_mfma_f32_16x16x32_bf16 a[88:91], a[216:219], a[248:251], a[88:91]\n"
"ds_read_b64_tr_b16 a[184:185], v44 offset:49152\n"
"v_mfma_f32_16x16x32_bf16 a[92:95], a[220:223], a[248:251], a[92:95]\n"
"ds_read_b64_tr_b16 a[186:187], v44 offset:50176\n"
"v_mfma_f32_16x16x32_bf16 a[96:99], a[192:195], a[252:255], a[96:99]\n"
"ds_read_b64_tr_b16 a[188:189], v45 offset:49152\n"
"v_mfma_f32_16x16x32_bf16 a[100:103], a[196:199], a[252:255], a[100:103]\n"
"ds_read_b64_tr_b16 a[190:191], v45 offset:50176\n"
"v_mfma_f32_16x16x32_bf16 a[104:107], a[200:203], a[252:255], a[104:107]\n"
"v_mfma_f32_16x16x32_bf16 a[108:111], a[204:207], a[252:255], a[108:111]\n"
"v_mfma_f32_16x16x32_bf16 a[112:115], a[208:211], a[252:255], a[112:115]\n"
"v_mfma_f32_16x16x32_bf16 a[116:119], a[212:215], a[252:255], a[116:119]\n"
"s_add_u32 s44, s44, 2048\n"
"s_add_u32 s45, s45, s48\n"
"s_add_u32 s50, s50, 65536\n"
"s_and_b32 s50, s50, 131071\n"
"v_mfma_f32_16x16x32_bf16 a[120:123], a[216:219], a[252:255], a[120:123]\n"
"v_mfma_f32_16x16x32_bf16 a[124:127], a[220:223], a[252:255], a[124:127]\n"
"s_sub_u32 s46, s46, 1\n"
"s_cmp_lg_u32 s46, 0\n"
"s_waitcnt lgkmcnt(0)\n"
"s_cbranch_scc1 KL_u_%=\n"
"v_mfma_f32_16x16x32_bf16 v[128:131], a[128:131], a[160:163], v[128:131]\n"
"ds_read_b64_tr_b16 a[224:225], v42 offset:40960\n"
"v_mfma_f32_16x16x32_bf16 v[132:135], a[132:135], a[160:163], v[132:135]\n"
"ds_read_b64_tr_b16 a[226:227], v42 offset:41984\n"
"v_mfma_f32_16x16x32_bf16 v[136:139], a[136:139], a[160:163], v[136:139]\n"
"ds_read_b128 a[192:195], v41 offset:0\n"
"v_mfma_f32_16x16x32_bf16 v[140:143], a[140:143], a[160:163], v[140:143]\n"
"ds_read_b128 a[196:199], v41 offset:2048\n"
"v_mfma_f32_16x16x32_bf16 v[144:147], a[144:147], a[160:163], v[144:147]\n"
"ds_read_b128 a[200:203], v41 offset:4096\n"
"v_mfma_f32_16x16x32_bf16 v[148:151], a[148:151], a[160:163], v[148:151]\n"
"ds_read_b128 a[204:207], v41 offset:6144\n"
"v_mfma_f32_16x16x32_bf16 v[152:155], a[152:155], a[160:163], v[152:155]\n"
"ds_read_b128 a[208:211], v41 offset:8192\n"
"v_mfma_f32_16x16x32_bf16 v[156:159], a[156:159], a[160:163], v[156:159]\n"
"ds_read_b128 a[212:215], v41 offset:10240\n"
"v_mfma_f32_16x16x32_bf16 v[160:163], a[128:131], a[164:167], v[160:163]\n"
"ds_read_b128 a[216:219], v41 offset:12288\n"
"v_mfma_f32_16x16x32_bf16 v[164:167], a[132:135], a[164:167], v[164:167]\n"
"ds_read_b128 a[220:223], v41 offset:14336\n"
"v_mfma_f32_16x16x32_bf16 v[168:171], a[136:139], a[164:167], v[168:171]\n"
"ds_read_b64_tr_b16 a[228:229], v43 offset:40960\n"
"v_mfma_f32_16x16x32_bf16 v[172:175], a[140:143], a[164:167], v[172:175]\n"
"ds_read_b64_tr_b16 a[230:231], v43 offset:41984\n"
"v_mfma_f32_16x16x32_bf16 v[176:179], a[144:147], a[164:167], v[176:179]\n"
"ds_read_b64_tr_b16 a[232:233], v44 offset:40960\n"
"v_mfma_f32_16x16x32_bf16 v[180:183], a[148:151], a[164:167], v[180:183]\n"
"ds_read_b64_tr_b16 a[234:235], v44 offset:41984\n"
"v_mfma_f32_16x16x32_bf16 v[184:187], a[152:155], a[164:167], v[184:187]\n"
"ds_read_b64_tr_b16 a[236:237], v45 offset:40960\n"
"v_mfma_f32_16x16x32_bf16 v[188:191], a[156:159], a[164:167], v[188:191]\n"
"ds_read_b64_tr_b16 a[238:239], v45 offset:41984\n"
"v_mfma_f32_16x16x32_bf16 v[192:195], a[128:131], a[168:171], v[192:195]\n"
"ds_read_b64_tr_b16 a[240:241], v42 offset:57344\n"
"v_mfma_f32_16x16x32_bf16 v[196:199], a[132:135], a[168:171], v[196:199]\n"
"ds_read_b64_tr_b16 a[242:243], v42 offset:58368\n"
"v_mfma_f32_16x16x32_bf16 v[200:203], a[136:139], a[168:171], v[200:203]\n"
"ds_read_b64_tr_b16 a[244:245], v43 offset:57344\n"
"v_mfma_f32_16x16x32_bf16 v[204:207], a[140:143], a[168:171], v[204:207]\n"
"ds_read_b64_tr_b16 a[246:247], v43 offset:58368\n"
"v_mfma_f32_16x16x32_bf16 v[208:211], a[144:147], a[168:171], v[208:211]\n"
"ds_read_b64_tr_b16 a[248:249], v44 offset:57344\n"
"v_mfma_f32_16x16x32_bf16 v[212:215], a[148:151], a[168:171], v[212:215]\n"
"ds_read_b64_tr_b16 a[250:251], v44 offset:58368\n"
"v_mfma_f32_16x16x32_bf16 v[216:219], a[152:155], a[168:171], v[216:219]\n"
"ds_read_b64_tr_b16 a[252:253], v45 offset:57344\n"
"v_mfma_f32_16x16x32_bf16 v[220:223], a[156:159], a[168:171], v[220:223]\n"
"ds_read_b64_tr_b16 a[254:255], v45 offset:58368\n"
"v_mfma_f32_16x16x32_bf16 v[224:227], a[128:131], a[172:175], v[224:227]\n"
"v_mfma_f32_16x16x32_bf16 v[228:231], a[132:135], a[172:175], v[228:231]\n"
"v_mfma_f32_16x16x32_bf16 v[232:235], a[136:139], a[172:175], v[232:235]\n"
"v_mfma_f32_16x16x32_bf16 v[236:239], a[140:143], a[172:175], v[236:239]\n"
"s_add_u32 s51, s51, 65536\n"
"s_and_b32 s51, s51, 131071\n"
"v_mfma_f32_16x16x32_bf16 v[240:243], a[144:147], a[172:175], v[240:243]\n"
"v_mfma_f32_16x16x32_bf16 v[244:247], a[148:151], a[172:175], v[244:247]\n"
"s_waitcnt lgkmcnt(0)\n"
"v_mfma_f32_16x16x32_bf16 v[248:251], a[152:155], a[172:175], v[248:251]\n"
"v_mfma_f32_16x16x32_bf16 v[252:255], a[156:159], a[172:175], v[252:255]\n"
"v_mfma_f32_16x16x32_bf16 a[0:3], a[128:131], a[176:179], a[0:3]\n"
"v_mfma_f32_16x16x32_bf16 a[4:7], a[132:135], a[176:179], a[4:7]\n"
"v_mfma_f32_16x16x32_bf16 a[8:11], a[136:139], a[176:179], a[8:11]\n"
"v_mfma_f32_16x16x32_bf16 a[12:15], a[140:143], a[176:179], a[12:15]\n"
"v_mfma_f32_16x16x32_bf16 a[16:19], a[144:147], a[176:179], a[16:19]\n"
"v_mfma_f32_16x16x32_bf16 a[20:23], a[148:151], a[176:179], a[20:23]\n"
"v_mfma_f32_16x16x32_bf16 a[24:27], a[152:155], a[176:179], a[24:27]\n"
"v_mfma_f32_16x16x32_bf16 a[28:31], a[156:159], a[176:179], a[28:31]\n"
"v_mfma_f32_16x16x32_bf16 a[32:35], a[128:131], a[180:183], a[32:35]\n"
"v_mfma_f32_16x16x32_bf16 a[36:39], a[132:135], a[180:183], a[36:39]\n"
"v_mfma_f32_16x16x32_bf16 a[40:43], a[136:139], a[180:183], a[40:43]\n"
"v_mfma_f32_16x16x32_bf16 a[44:47], a[140:143], a[180:183], a[44:47]\n"
"v_mfma_f32_16x16x32_bf16 a[48:51], a[144:147], a[180:183], a[48:51]\n"
"v_mfma_f32_16x16x32_bf16 a[52:55], a[148:151], a[180:183], a[52:55]\n"
"v_mfma_f32_16x16x32_bf16 a[56:59], a[152:155], a[180:183], a[56:59]\n"
"v_mfma_f32_16x16x32_bf16 a[60:63], a[156:159], a[180:183], a[60:63]\n"
"v_mfma_f32_16x16x32_bf16 a[64:67], a[128:131], a[184:187], a[64:67]\n"
"v_mfma_f32_16x16x32_bf16 a[68:71], a[132:135], a[184:187], a[68:71]\n"
"v_mfma_f32_16x16x32_bf16 a[72:75], a[136:139], a[184:187], a[72:75]\n"
"v_mfma_f32_16x16x32_bf16 a[76:79], a[140:143], a[184:187], a[76:79]\n"
"v_mfma_f32_16x16x32_bf16 a[80:83], a[144:147], a[184:187], a[80:83]\n"
"v_mfma_f32_16x16x32_bf16 a[84:87], a[148:151], a[184:187], a[84:87]\n"
"v_mfma_f32_16x16x32_bf16 a[88:91], a[152:155], a[184:187], a[88:91]\n"
"v_mfma_f32_16x16x32_bf16 a[92:95], a[156:159], a[184:187], a[92:95]\n"
"v_mfma_f32_16x16x32_bf16 a[96:99], a[128:131], a[188:191], a[96:99]\n"
"v_mfma_f32_16x16x32_bf16 a[100:103], a[132:135], a[188:191], a[100:103]\n"
"v_mfma_f32_16x16x32_bf16 a[104:107], a[136:139], a[188:191], a[104:107]\n"
"v_mfma_f32_16x16x32_bf16 a[108:111], a[140:143], a[188:191], a[108:111]\n"
"v_mfma_f32_16x16x32_bf16 a[112:115], a[144:147], a[188:191], a[112:115]\n"
"v_mfma_f32_16x16x32_bf16 a[116:119], a[148:151], a[188:191], a[116:119]\n"
"v_mfma_f32_16x16x32_bf16 a[120:123], a[152:155], a[188:191], a[120:123]\n"
"v_mfma_f32_16x16x32_bf16 a[124:127], a[156:159], a[188:191], a[124:127]\n"
"v_mfma_f32_16x16x32_bf16 v[128:131], a[192:195], a[224:227], v[128:131]\n"
"v_mfma_f32_16x16x32_bf16 v[132:135], a[196:199], a[224:227], v[132:135]\n"
"v_mfma_f32_16x16x32_bf16 v[136:139], a[200:203], a[224:227], v[136:139]\n"
"v_mfma_f32_16x16x32_bf16 v[140:143], a[204:207], a[224:227], v[140:143]\n"
"v_mfma_f32_16x16x32_bf16 v[144:147], a[208:211], a[224:227], v[144:147]\n"
"v_mfma_f32_16x16x32_bf16 v[148:151], a[212:215], a[224:227], v[148:151]\n"
"v_mfma_f32_16x16x32_bf16 v[152:155], a[216:219], a[224:227], v[152:155]\n"
"v_mfma_f32_16x16x32_bf16 v[156:159], a[220:223], a[224:227], v[156:159]\n"
"v_mfma_f32_16x16x32_bf16 v[160:163], a[192:195], a[228:231], v[160:163]\n"
"v_mfma_f32_16x16x32_bf16 v[164:167], a[196:199], a[228:231], v[164:167]\n"
"v_mfma_f32_16x16x32_bf16 v[168:171], a[200:203], a[228:231], v[168:171]\n"
"v_add_u32 v40, s51, v46\n"
"v_mfma_f32_16x16x32_bf16 v[172:175], a[204:207], a[228:231], v[172:175]\n"
"v_mfma_f32_16x16x32_bf16 v[176:179], a[208:211], a[228:231], v[176:179]\n"
"v_mfma_f32_16x16x32_bf16 v[180:183], a[212:215], a[228:231], v[180:183]\n"
"v_mfma_f32_16x16x32_bf16 v[184:187], a[216:219], a[228:231], v[184:187]\n"
"v_add_u32 v41, s51, v47\n"
"v_mfma_f32_16x16x32_bf16 v[188:191], a[220:223], a[228:231], v[188:191]\n"
"v_mfma_f32_16x16x32_bf16 v[192:195], a[192:195], a[232:235], v[192:195]\n"
"v_mfma_f32_16x16x32_bf16 v[196:199], a[196:199], a[232:235], v[196:199]\n"
"v_mfma_f32_16x16x32_bf16 v[200:203], a[200:203], a[232:235], v[200:203]\n"
"v_add_u32 v42, s51, v48\n"
"v_mfma_f32_16x16x32_bf16 v[204:207], a[204:207], a[232:235], v[204:207]\n"
"v_mfma_f32_16x16x32_bf16 v[208:211], a[208:211], a[232:235], v[208:211]\n"
"v_mfma_f32_16x16x32_bf16 v[212:215], a[212:215], a[232:235], v[212:215]\n"
"v_mfma_f32_16x16x32_bf16 v[216:219], a[216:219], a[232:235], v[216:219]\n"
"v_add_u32 v43, s51, v49\n"
"v_mfma_f32_16x16x32_bf16 v[220:223], a[220:223], a[232:235], v[220:223]\n"
"v_mfma_f32_16x16x32_bf16 v[224:227], a[192:195], a[236:239], v[224:227]\n"
"v_mfma_f32_16x16x32_bf16 v[228:231], a[196:199], a[236:239], v[228:231]\n"
"v_mfma_f32_16x16x32_bf16 v[232:235], a[200:203], a[236:239], v[232:235]\n"
"v_add_u32 v44, s51, v50\n"
"v_mfma_f32_16x16x32_bf16 v[236:239], a[204:207], a[236:239], v[236:239]\n"
"v_mfma_f32_16x16x32_bf16 v[240:243], a[208:211], a[236:239], v[240:243]\n"
"v_mfma_f32_16x16x32_bf16 v[244:247], a[212:215], a[236:239], v[244:247]\n"
"v_mfma_f32_16x16x32_bf16 v[248:251], a[216:219], a[236:239], v[248:251]\n"
"v_add_u32 v45, s51, v51\n"
"v_mfma_f32_16x16x32_bf16 v[252:255], a[220:223], a[236:239], v[252:255]\n"
"v_mfma_f32_16x16x32_bf16 a[0:3], a[192:195], a[240:243], a[0:3]\n"
"v_mfma_f32_16x16x32_bf16 a[4:7], a[196:199], a[240:243], a[4:7]\n"
"s_waitcnt vmcnt(0)\n"
"s_barrier\n"
"v_mfma_f32_16x16x32_bf16 a[8:11], a[200:203], a[240:243], a[8:11]\n"
"ds_read_b64_tr_b16 a[160:161], v42 offset:32768\n"
"v_mfma_f32_16x16x32_bf16 a[12:15], a[204:207], a[240:243], a[12:15]\n"
"ds_read_b64_tr_b16 a[162:163], v42 offset:33792\n"
"v_mfma_f32_16x16x32_bf16 a[16:19], a[208:211], a[240:243], a[16:19]\n"
"ds_read_b128 a[128:131], v40 offset:0\n"
"v_mfma_f32_16x16x32_bf16 a[20:23], a[212:215], a[240:243], a[20:23]\n"
"ds_read_b128 a[132:135], v40 offset:2048\n"
"v_mfma_f32_16x16x32_bf16 a[24:27], a[216:219], a[240:243], a[24:27]\n"
"ds_read_b128 a[136:139], v40 offset:4096\n"
"v_mfma_f32_16x16x32_bf16 a[28:31], a[220:223], a[240:243], a[28:31]\n"
"ds_read_b128 a[140:143], v40 offset:6144\n"
"v_mfma_f32_16x16x32_bf16 a[32:35], a[192:195], a[244:247], a[32:35]\n"
"ds_read_b128 a[144:147], v40 offset:8192\n"
"v_mfma_f32_16x16x32_bf16 a[36:39], a[196:199], a[244:247], a[36:39]\n"
"ds_read_b128 a[148:151], v40 offset:10240\n"
"v_mfma_f32_16x16x32_bf16 a[40:43], a[200:203], a[244:247], a[40:43]\n"
"ds_read_b128 a[152:155], v40 offset:12288\n"
"v_mfma_f32_16x16x32_bf16 a[44:47], a[204:207], a[244:247], a[44:47]\n"
"ds_read_b128 a[156:159], v40 offset:14336\n"
"v_mfma_f32_16x16x32_bf16 a[48:51], a[208:211], a[244:247], a[48:51]\n"
"ds_read_b64_tr_b16 a[164:165], v43 offset:32768\n"
"v_mfma_f32_16x16x32_bf16 a[52:55], a[212:215], a[244:247], a[52:55]\n"
"ds_read_b64_tr_b16 a[166:167], v43 offset:33792\n"
"v_mfma_f32_16x16x32_bf16 a[56:59], a[216:219], a[244:247], a[56:59]\n"
"ds_read_b64_tr_b16 a[168:169], v44 offset:32768\n"
"v_mfma_f32_16x16x32_bf16 a[60:63], a[220:223], a[244:247], a[60:63]\n"
"ds_read_b64_tr_b16 a[170:171], v44 offset:33792\n"
"v_mfma_f32_16x16x32_bf16 a[64:67], a[192:195], a[248:251], a[64:67]\n"
"ds_read_b64_tr_b16 a[172:173], v45 offset:32768\n"
"v_mfma_f32_16x16x32_bf16 a[68:71], a[196:199], a[248:251], a[68:71]\n"
"ds_read_b64_tr_b16 a[174:175], v45 offset:33792\n"
"v_mfma_f32_16x16x32_bf16 a[72:75], a[200:203], a[248:251], a[72:75]\n"
"ds_read_b64_tr_b16 a[176:177], v42 offset:49152\n"
"v_mfma_f32_16x16x32_bf16 a[76:79], a[204:207], a[248:251], a[76:79]\n"
"ds_read_b64_tr_b16 a[178:179], v42 offset:50176\n"
"v_mfma_f32_16x16x32_bf16 a[80:83], a[208:211], a[248:251], a[80:83]\n"
"ds_read_b64_tr_b16 a[180:181], v43 offset:49152\n"
"v_mfma_f32_16x16x32_bf16 a[84:87], a[212:215], a[248:251], a[84:87]\n"
"ds_read_b64_tr_b16 a[182:183], v43 offset:50176\n"
"v_mfma_f32_16x16x32_bf16 a[88:91], a[216:219], a[248:251], a[88:91]\n"
"ds_read_b64_tr_b16 a[184:185], v44 offset:49152\n"
"v_mfma_f32_16x16x32_bf16 a[92:95], a[220:223], a[248:251], a[92:95]\n"
"ds_read_b64_tr_b16 a[186:187], v44 offset:50176\n"
"v_mfma_f32_16x16x32_bf16 a[96:99], a[192:195], a[252:255], a[96:99]\n"
"ds_read_b64_tr_b16 a[188:189], v45 offset:49152\n"
"v_mfma_f32_16x16x32_bf16 a[100:103], a[196:199], a[252:255], a[100:103]\n"
"ds_read_b64_tr_b16 a[190:191], v45 offset:50176\n"
"v_mfma_f32_16x16x32_bf16 a[104:107], a[200:203], a[252:255], a[104:107]\n"
"v_mfma_f32_16x16x32_bf16 a[108:111], a[204:207], a[252:255], a[108:111]\n"
"v_mfma_f32_16x16x32_bf16 a[112:115], a[208:211], a[252:255], a[112:115]\n"
"v_mfma_f32_16x16x32_bf16 a[116:119], a[212:215], a[252:255], a[116:119]\n"
"v_mfma_f32_16x16x32_bf16 a[120:123], a[216:219], a[252:255], a[120:123]\n"
"v_mfma_f32_16x16x32_bf16 a[124:127], a[220:223], a[252:255], a[124:127]\n"
"s_waitcnt lgkmcnt(0)\n"
"v_mfma_f32_16x16x32_bf16 v[128:131], a[128:131], a[160:163], v[128:131]\n"
"ds_read_b64_tr_b16 a[224:225], v42 offset:40960\n"
"v_mfma_f32_16x16x32_bf16 v[132:135], a[132:135], a[160:163], v[132:135]\n"
"ds_read_b64_tr_b16 a[226:227], v42 offset:41984\n"
"v_mfma_f32_16x16x32_bf16 v[136:139], a[136:139], a[160:163], v[136:139]\n"
"ds_read_b128 a[192:195], v41 offset:0\n"
"v_mfma_f32_16x16x32_bf16 v[140:143], a[140:143], a[160:163], v[140:143]\n"
"ds_read_b128 a[196:199], v41 offset:2048\n"
"v_mfma_f32_16x16x32_bf16 v[144:147], a[144:147], a[160:163], v[144:147]\n"
"ds_read_b128 a[200:203], v41 offset:4096\n"
"v_mfma_f32_16x16x32_bf16 v[148:151], a[148:151], a[160:163], v[148:151]\n"
"ds_read_b128 a[204:207], v41 offset:6144\n"
"v_mfma_f32_16x16x32_bf16 v[152:155], a[152:155], a[160:163], v[152:155]\n"
"ds_read_b128 a[208:211], v41 offset:8192\n"
"v_mfma_f32_16x16x32_bf16 v[156:159], a[156:159], a[160:163], v[156:159]\n"
"ds_read_b128 a[212:215], v41 offset:10240\n"
"v_mfma_f32_16x16x32_bf16 v[160:163], a[128:131], a[164:167], v[160:163]\n"
"ds_read_b128 a[216:219], v41 offset:12288\n"
"v_mfma_f32_16x16x32_bf16 v[164:167], a[132:135], a[164:167], v[164:167]\n"
"ds_read_b128 a[220:223], v41 offset:14336\n"
"v_mfma_f32_16x16x32_bf16 v[168:171], a[136:139], a[164:167], v[168:171]\n"
"ds_read_b64_tr_b16 a[228:229], v43 offset:40960\n"
"v_mfma_f32_16x16x32_bf16 v[172:175], a[140:143], a[164:167], v[172:175]\n"
"ds_read_b64_tr_b16 a[230:231], v43 offset:41984\n"
"v_mfma_f32_16x16x32_bf16 v[176:179], a[144:147], a[164:167], v[176:179]\n"
"ds_read_b64_tr_b16 a[232:233], v44 offset:40960\n"
"v_mfma_f32_16x16x32_bf16 v[180:183], a[148:151], a[164:167], v[180:183]\n"
"ds_read_b64_tr_b16 a[234:235], v44 offset:41984\n"
"v_mfma_f32_16x16x32_bf16 v[184:187], a[152:155], a[164:167], v[184:187]\n"
"ds_read_b64_tr_b16 a[236:237], v45 offset:40960\n"
"v_mfma_f32_16x16x32_bf16 v[188:191], a[156:159], a[164:167], v[188:191]\n"
"ds_read_b64_tr_b16 a[238:239], v45 offset:41984\n"
"v_mfma_f32_16x16x32_bf16 v[192:195], a[128:131], a[168:171], v[192:195]\n"
"ds_read_b64_tr_b16 a[240:241], v42 offset:57344\n"
"v_mfma_f32_16x16x32_bf16 v[196:199], a[132:135], a[168:171], v[196:199]\n"
"ds_read_b64_tr_b16 a[242:243], v42 offset:58368\n"
"v_mfma_f32_16x16x32_bf16 v[200:203], a[136:139], a[168:171], v[200:203]\n"
"ds_read_b64_tr_b16 a[244:245], v43 offset:57344\n"
"v_mfma_f32_16x16x32_bf16 v[204:207], a[140:143], a[168:171], v[204:207]\n"
"ds_read_b64_tr_b16 a[246:247], v43 offset:58368\n"
"v_mfma_f32_16x16x32_bf16 v[208:211], a[144:147], a[168:171], v[208:211]\n"
"ds_read_b64_tr_b16 a[248:249], v44 offset:57344\n"
"v_mfma_f32_16x16x32_bf16 v[212:215], a[148:151], a[168:171], v[212:215]\n"
"ds_read_b64_tr_b16 a[250:251], v44 offset:58368\n"
"v_mfma_f32_16x16x32_bf16 v[216:219], a[152:155], a[168:171], v[216:219]\n"
"ds_read_b64_tr_b16 a[252:253], v45 offset:57344\n"
"v_mfma_f32_16x16x32_bf16 v[220:223], a[156:159], a[168:171], v[220:223]\n"
"ds_read_b64_tr_b16 a[254:255], v45 offset:58368\n"
"v_mfma_f32_16x16x32_bf16 v[224:227], a[128:131], a[172:175], v[224:227]\n"
"v_mfma_f32_16x16x32_bf16 v[228:231], a[132:135], a[172:175], v[228:231]\n"
"v_mfma_f32_16x16x32_bf16 v[232:235], a[136:139], a[172:175], v[232:235]\n"
"v_mfma_f32_16x16x32_bf16 v[236:239], a[140:143], a[172:175], v[236:239]\n"
"s_add_u32 s51, s51, 65536\n"
"s_and_b32 s51, s51, 131071\n"
"v_mfma_f32_16x16x32_bf16 v[240:243], a[144:147], a[172:175], v[240:243]\n"
"v_mfma_f32_16x16x32_bf16 v[244:247], a[148:151], a[172:175], v[244:247]\n"
"s_waitcnt lgkmcnt(0)\n"
"v_mfma_f32_16x16x32_bf16 v[248:251], a[152:155], a[172:175], v[248:251]\n"
"v_mfma_f32_16x16x32_bf16 v[252:255], a[156:159], a[172:175], v[252:255]\n"
"v_mfma_f32_16x16x32_bf16 a[0:3], a[128:131], a[176:179], a[0:3]\n"
"v_mfma_f32_16x16x32_bf16 a[4:7], a[132:135], a[176:179], a[4:7]\n"
"v_mfma_f32_16x16x32_bf16 a[8:11], a[136:139], a[176:179], a[8:11]\n"
"v_mfma_f32_16x16x32_bf16 a[12:15], a[140:143], a[176:179], a[12:15]\n"
"v_mfma_f32_16x16x32_bf16 a[16:19], a[144:147], a[176:179], a[16:19]\n"
"v_mfma_f32_16x16x32_bf16 a[20:23], a[148:151], a[176:179], a[20:23]\n"
"v_mfma_f32_16x16x32_bf16 a[24:27], a[152:155], a[176:179], a[24:27]\n"
"v_mfma_f32_16x16x32_bf16 a[28:31], a[156:159], a[176:179], a[28:31]\n"
"v_mfma_f32_16x16x32_bf16 a[32:35], a[128:131], a[180:183], a[32:35]\n"
"v_mfma_f32_16x16x32_bf16 a[36:39], a[132:135], a[180:183], a[36:39]\n"
"v_mfma_f32_16x16x32_bf16 a[40:43], a[136:139], a[180:183], a[40:43]\n"
"v_mfma_f32_16x16x32_bf16 a[44:47], a[140:143], a[180:183], a[44:47]\n"
"v_mfma_f32_16x16x32_bf16 a[48:51], a[144:147], a[180:183], a[48:51]\n"
"v_mfma_f32_16x16x32_bf16 a[52:55], a[148:151], a[180:183], a[52:55]\n"
"v_mfma_f32_16x16x32_bf16 a[56:59], a[152:155], a[180:183], a[56:59]\n"
"v_mfma_f32_16x16x32_bf16 a[60:63], a[156:159], a[180:183], a[60:63]\n"
"v_mfma_f32_16x16x32_bf16 a[64:67], a[128:131], a[184:187], a[64:67]\n"
"v_mfma_f32_16x16x32_bf16 a[68:71], a[132:135], a[184:187], a[68:71]\n"
"v_mfma_f32_16x16x32_bf16 a[72:75], a[136:139], a[184:187], a[72:75]\n"
"v_mfma_f32_16x16x32_bf16 a[76:79], a[140:143], a[184:187], a[76:79]\n"
"v_mfma_f32_16x16x32_bf16 a[80:83], a[144:147], a[184:187], a[80:83]\n"
"v_mfma_f32_16x16x32_bf16 a[84:87], a[148:151], a[184:187], a[84:87]\n"
"v_mfma_f32_16x16x32_bf16 a[88:91], a[152:155], a[184:187], a[88:91]\n"
"v_mfma_f32_16x16x32_bf16 a[92:95], a[156:159], a[184:187], a[92:95]\n"
"v_mfma_f32_16x16x32_bf16 a[96:99], a[128:131], a[188:191], a[96:99]\n"
"v_mfma_f32_16x16x32_bf16 a[100:103], a[132:135], a[188:191], a[100:103]\n"
"v_mfma_f32_16x16x32_bf16 a[104:107], a[136:139], a[188:191], a[104:107]\n"
"v_mfma_f32_16x16x32_bf16 a[108:111], a[140:143], a[188:191], a[108:111]\n"
"v_mfma_f32_16x16x32_bf16 a[112:115], a[144:147], a[188:191], a[112:115]\n"
"v_mfma_f32_16x16x32_bf16 a[116:119], a[148:151], a[188:191], a[116:119]\n"
"v_mfma_f32_16x16x32_bf16 a[120:123], a[152:155], a[188:191], a[120:123]\n"
"v_mfma_f32_16x16x32_bf16 a[124:127], a[156:159], a[188:191], a[124:127]\n"
"v_mfma_f32_16x16x32_bf16 v[128:131], a[192:195], a[224:227], v[128:131]\n"
"v_mfma_f32_16x16x32_bf16 v[132:135], a[196:199], a[224:227], v[132:135]\n"
"v_mfma_f32_16x16x32_bf16 v[136:139], a[200:203], a[224:227], v[136:139]\n"
"v_mfma_f32_16x16x32_bf16 v[140:143], a[204:207], a[224:227], v[140:143]\n"
"v_mfma_f32_16x16x32_bf16 v[144:147], a[208:211], a[224:227], v[144:147]\n"
"v_mfma_f32_16x16x32_bf16 v[148:151], a[212:215], a[224:227], v[148:151]\n"
"v_mfma_f32_16x16x32_bf16 v[152:155], a[216:219], a[224:227], v[152:155]\n"
"v_mfma_f32_16x16x32_bf16 v[156:159], a[220:223], a[224:227], v[156:159]\n"
"v_mfma_f32_16x16x32_bf16 v[160:163], a[192:195], a[228:231], v[160:163]\n"
"v_mfma_f32_16x16x32_bf16 v[164:167], a[196:199], a[228:231], v[164:167]\n"
"v_mfma_f32_16x16x32_bf16 v[168:171], a[200:203], a[228:231], v[168:171]\n"
"v_mfma_f32_16x16x32_bf16 v[172:175], a[204:207], a[228:231], v[172:175]\n"
"v_mfma_f32_16x16x32_bf16 v[176:179], a[208:211], a[228:231], v[176:179]\n"
"v_mfma_f32_16x16x32_bf16 v[180:183], a[212:215], a[228:231], v[180:183]\n"
"v_mfma_f32_16x16x32_bf16 v[184:187], a[216:219], a[228:231], v[184:187]\n"
"v_mfma_f32_16x16x32_bf16 v[188:191], a[220:223], a[228:231], v[188:191]\n"
"v_mfma_f32_16x16x32_bf16 v[192:195], a[192:195], a[232:235], v[192:195]\n"
"v_mfma_f32_16x16x32_bf16 v[196:199], a[196:199], a[232:235], v[196:199]\n"
"v_mfma_f32_16x16x32_bf16 v[200:203], a[200:203], a[232:235], v[200:203]\n"
"v_mfma_f32_16x16x32_bf16 v[204:207], a[204:207], a[232:235], v[204:207]\n"
"v_mfma_f32_16x16x32_bf16 v[208:211], a[208:211], a[232:235], v[208:211]\n"
"v_mfma_f32_16x16x32_bf16 v[212:215], a[212:215], a[232:235], v[212:215]\n"
"v_mfma_f32_16x16x32_bf16 v[216:219], a[216:219], a[232:235], v[216:219]\n"
"v_mfma_f32_16x16x32_bf16 v[220:223], a[220:223], a[232:235], v[220:223]\n"
"v_mfma_f32_16x16x32_bf16 v[224:227], a[192:195], a[236:239], v[224:227]\n"
"v_mfma_f32_16x16x32_bf16 v[228:231], a[196:199], a[236:239], v[228:231]\n"
"v_mfma_f32_16x16x32_bf16 v[232:235], a[200:203], a[236:239], v[232:235]\n"
"v_mfma_f32_16x16x32_bf16 v[236:239], a[204:207], a[236:239], v[236:239]\n"
"v_mfma_f32_16x16x32_bf16 v[240:243], a[208:211], a[236:239], v[240:243]\n"
"v_mfma_f32_16x16x32_bf16 v[244:247], a[212:215], a[236:239], v[244:247]\n"
"v_mfma_f32_16x16x32_bf16 v[248:251], a[216:219], a[236:239], v[248:251]\n"
"v_mfma_f32_16x16x32_bf16 v[252:255], a[220:223], a[236:239], v[252:255]\n"
"v_mfma_f32_16x16x32_bf16 a[0:3], a[192:195], a[240:243], a[0:3]\n"
"v_mfma_f32_16x16x32_bf16 a[4:7], a[196:199], a[240:243], a[4:7]\n"
"v_mfma_f32_16x16x32_bf16 a[8:11], a[200:203], a[240:243], a[8:11]\n"
"v_mfma_f32_16x16x32_bf16 a[12:15], a[204:207], a[240:243], a[12:15]\n"
"v_mfma_f32_16x16x32_bf16 a[16:19], a[208:211], a[240:243], a[16:19]\n"
"v_mfma_f32_16x16x32_bf16 a[20:23], a[212:215], a[240:243], a[20:23]\n"
"v_mfma_f32_16x16x32_bf16 a[24:27], a[216:219], a[240:243], a[24:27]\n"
"v_mfma_f32_16x16x32_bf16 a[28:31], a[220:223], a[240:243], a[28:31]\n"
"v_mfma_f32_16x16x32_bf16 a[32:35], a[192:195], a[244:247], a[32:35]\n"
"v_mfma_f32_16x16x32_bf16 a[36:39], a[196:199], a[244:247], a[36:39]\n"
"v_mfma_f32_16x16x32_bf16 a[40:43], a[200:203], a[244:247], a[40:43]\n"
"v_mfma_f32_16x16x32_bf16 a[44:47], a[204:207], a[244:247], a[44:47]\n"
"v_mfma_f32_16x16x32_bf16 a[48:51], a[208:211], a[244:247], a[48:51]\n"
"v_mfma_f32_16x16x32_bf16 a[52:55], a[212:215], a[244:247], a[52:55]\n"
"v_mfma_f32_16x16x32_bf16 a[56:59], a[216:219], a[244:247], a[56:59]\n"
"v_mfma_f32_16x16x32_bf16 a[60:63], a[220:223], a[244:247], a[60:63]\n"
"v_mfma_f32_16x16x32_bf16 a[64:67], a[192:195], a[248:251], a[64:67]\n"
"v_mfma_f32_16x16x32_bf16 a[68:71], a[196:199], a[248:251], a[68:71]\n"
"v_mfma_f32_16x16x32_bf16 a[72:75], a[200:203], a[248:251], a[72:75]\n"
"v_mfma_f32_16x16x32_bf16 a[76:79], a[204:207], a[248:251], a[76:79]\n"
"v_mfma_f32_16x16x32_bf16 a[80:83], a[208:211], a[248:251], a[80:83]\n"
"v_mfma_f32_16x16x32_bf16 a[84:87], a[212:215], a[248:251], a[84:87]\n"
"v_mfma_f32_16x16x32_bf16 a[88:91], a[216:219], a[248:251], a[88:91]\n"
"v_mfma_f32_16x16x32_bf16 a[92:95], a[220:223], a[248:251], a[92:95]\n"
"v_mfma_f32_16x16x32_bf16 a[96:99], a[192:195], a[252:255], a[96:99]\n"
"v_mfma_f32_16x16x32_bf16 a[100:103], a[196:199], a[252:255], a[100:103]\n"
"v_mfma_f32_16x16x32_bf16 a[104:107], a[200:203], a[252:255], a[104:107]\n"
"v_mfma_f32_16x16x32_bf16 a[108:111], a[204:207], a[252:255], a[108:111]\n"
"v_mfma_f32_16x16x32_bf16 a[112:115], a[208:211], a[252:255], a[112:115]\n"
"v_mfma_f32_16x16x32_bf16 a[116:119], a[212:215], a[252:255], a[116:119]\n"
"v_mfma_f32_16x16x32_bf16 a[120:123], a[216:219], a[252:255], a[120:123]\n"
"v_mfma_f32_16x16x32_bf16 a[124:127], a[220:223], a[252:255], a[124:127]\n"
"s_barrier\n"
"s_add_u32 s54, %[o0], 0\n"
"v_cvt_pk_bf16_f32 v60, v128, v129\n"
"v_cvt_pk_bf16_f32 v61, v130, v131\n"
"v_cvt_pk_bf16_f32 v62, v132, v133\n"
"v_cvt_pk_bf16_f32 v63, v134, v135\n"
"buffer_store_dwordx4 v[60:63], %[vst], s[56:59], s54 offen offset:0 sc1\n"
"v_cvt_pk_bf16_f32 v64, v160, v161\n"
"v_cvt_pk_bf16_f32 v65, v162, v163\n"
"v_cvt_pk_bf16_f32 v66, v164, v165\n"
"v_cvt_pk_bf16_f32 v67, v166, v167\n"
"buffer_store_dwordx4 v[64:67], %[vst], s[56:59], s54 offen offset:1024 sc1\n"
"v_cvt_pk_bf16_f32 v68, v192, v193\n"
"v_cvt_pk_bf16_f32 v69, v194, v195\n"
"v_cvt_pk_bf16_f32 v70, v196, v197\n"
"v_cvt_pk_bf16_f32 v71, v198, v199\n"
"buffer_store_dwordx4 v[68:71], %[vst], s[56:59], s54 offen offset:2048 sc1\n"
"v_cvt_pk_bf16_f32 v72, v224, v225\n"
"v_cvt_pk_bf16_f32 v73, v226, v227\n"
"v_cvt_pk_bf16_f32 v74, v228, v229\n"
"v_cvt_pk_bf16_f32 v75, v230, v231\n"
"buffer_store_dwordx4 v[72:75], %[vst], s[56:59], s54 offen offset:3072 sc1\n"
"s_add_u32 s54, %[o0], 0\n"
"s_add_u32 s54, s54, %[o1]\n"
"v_accvgpr_read_b32 v52, a0\n"
"v_accvgpr_read_b32 v53, a1\n"
"v_accvgpr_read_b32 v54, a2\n"
"v_accvgpr_read_b32 v55, a3\n"
"v_cvt_pk_bf16_f32 v60, v52, v53\n"
"v_cvt_pk_bf16_f32 v61, v54, v55\n"
"v_accvgpr_read_b32 v56, a4\n"
"v_accvgpr_read_b32 v57, a5\n"
"v_accvgpr_read_b32 v58, a6\n"
"v_accvgpr_read_b32 v59, a7\n"
"v_cvt_pk_bf16_f32 v62, v56, v57\n"
"v_cvt_pk_bf16_f32 v63, v58, v59\n"
"buffer_store_dwordx4 v[60:63], %[vst], s[56:59], s54 offen offset:0 sc1\n"
"v_accvgpr_read_b32 v52, a32\n"
"v_accvgpr_read_b32 v53, a33\n"
"v_accvgpr_read_b32 v54, a34\n"
"v_accvgpr_read_b32 v55, a35\n"
"v_cvt_pk_bf16_f32 v64, v52, v53\n"
"v_cvt_pk_bf16_f32 v65, v54, v55\n"
"v_accvgpr_read_b32 v56, a36\n"
"v_accvgpr_read_b32 v57, a37\n"
"v_accvgpr_read_b32 v58, a38\n"
"v_accvgpr_read_b32 v59, a39\n"
"v_cvt_pk_bf16_f32 v66, v56, v57\n"
"v_cvt_pk_bf16_f32 v67, v58, v59\n"
"buffer_store_dwordx4 v[64:67], %[vst], s[56:59], s54 offen offset:1024 sc1\n"
"v_accvgpr_read_b32 v52, a64\n"
"v_accvgpr_read_b32 v53, a65\n"
"v_accvgpr_read_b32 v54, a66\n"
"v_accvgpr_read_b32 v55, a67\n"
"v_cvt_pk_bf16_f32 v68, v52, v53\n"
"v_cvt_pk_bf16_f32 v69, v54, v55\n"
"v_accvgpr_read_b32 v56, a68\n"
"v_accvgpr_read_b32 v57, a69\n"
"v_accvgpr_read_b32 v58, a70\n"
"v_accvgpr_read_b32 v59, a71\n"
"v_cvt_pk_bf16_f32 v70, v56, v57\n"
"v_cvt_pk_bf16_f32 v71, v58, v59\n"
"buffer_store_dwordx4 v[68:71], %[vst], s[56:59], s54 offen offset:2048 sc1\n"
"v_accvgpr_read_b32 v52, a96\n"
"v_accvgpr_read_b32 v53, a97\n"
"v_accvgpr_read_b32 v54, a98\n"
"v_accvgpr_read_b32 v55, a99\n"
"v_cvt_pk_bf16_f32 v72, v52, v53\n"
"v_cvt_pk_bf16_f32 v73, v54, v55\n"
"v_accvgpr_read_b32 v56, a100\n"
"v_accvgpr_read_b32 v57, a101\n"
"v_accvgpr_read_b32 v58, a102\n"
"v_accvgpr_read_b32 v59, a103\n"
"v_cvt_pk_bf16_f32 v74, v56, v57\n"
"v_cvt_pk_bf16_f32 v75, v58, v59\n"
"buffer_store_dwordx4 v[72:75], %[vst], s[56:59], s54 offen offset:3072 sc1\n"
"s_add_u32 s54, %[o0], 262144\n"
"v_cvt_pk_bf16_f32 v60, v136, v137\n"
"v_cvt_pk_bf16_f32 v61, v138, v139\n"
"v_cvt_pk_bf16_f32 v62, v140, v141\n"
"v_cvt_pk_bf16_f32 v63, v142, v143\n"
"buffer_store_dwordx4 v[60:63], %[vst], s[56:59], s54 offen offset:0 sc1\n"
"v_cvt_pk_bf16_f32 v64, v168, v169\n"
"v_cvt_pk_bf16_f32 v65, v170, v171\n"
"v_cvt_pk_bf16_f32 v66, v172, v173\n"
"v_cvt_pk_bf16_f32 v67, v174, v175\n"
"buffer_store_dwordx4 v[64:67], %[vst], s[56:59], s54 offen offset:1024 sc1\n"
"v_cvt_pk_bf16_f32 v68, v200, v201\n"
"v_cvt_pk_bf16_f32 v69, v202, v203\n"
"v_cvt_pk_bf16_f32 v70, v204, v205\n"
"v_cvt_pk_bf16_f32 v71, v206, v207\n"
"buffer_store_dwordx4 v[68:71], %[vst], s[56:59], s54 offen offset:2048 sc1\n"
"v_cvt_pk_bf16_f32 v72, v232, v233\n"
"v_cvt_pk_bf16_f32 v73, v234, v235\n"
"v_cvt_pk_bf16_f32 v74, v236, v237\n"
"v_cvt_pk_bf16_f32 v75, v238, v239\n"
"buffer_store_dwordx4 v[72:75], %[vst], s[56:59], s54 offen offset:3072 sc1\n"
"s_add_u32 s54, %[o0], 262144\n"
"s_add_u32 s54, s54, %[o1]\n"
"v_accvgpr_read_b32 v52, a8\n"
"v_accvgpr_read_b32 v53, a9\n"
"v_accvgpr_read_b32 v54, a10\n"
"v_accvgpr_read_b32 v55, a11\n"
"v_cvt_pk_bf16_f32 v60, v52, v53\n"
"v_cvt_pk_bf16_f32 v61, v54, v55\n"
"v_accvgpr_read_b32 v56, a12\n"
"v_accvgpr_read_b32 v57, a13\n"
"v_accvgpr_read_b32 v58, a14\n"
"v_accvgpr_read_b32 v59, a15\n"
"v_cvt_pk_bf16_f32 v62, v56, v57\n"
"v_cvt_pk_bf16_f32 v63, v58, v59\n"
"buffer_store_dwordx4 v[60:63], %[vst], s[56:59], s54 offen offset:0 sc1\n"
"v_accvgpr_read_b32 v52, a40\n"
"v_accvgpr_read_b32 v53, a41\n"
"v_accvgpr_read_b32 v54, a42\n"
"v_accvgpr_read_b32 v55, a43\n"
"v_cvt_pk_bf16_f32 v64, v52, v53\n"
"v_cvt_pk_bf16_f32 v65, v54, v55\n"
"v_accvgpr_read_b32 v56, a44\n"
"v_accvgpr_read_b32 v57, a45\n"
"v_accvgpr_read_b32 v58, a46\n"
"v_accvgpr_read_b32 v59, a47\n"
"v_cvt_pk_bf16_f32 v66, v56, v57\n"
"v_cvt_pk_bf16_f32 v67, v58, v59\n"
"buffer_store_dwordx4 v[64:67], %[vst], s[56:59], s54 offen offset:1024 sc1\n"
"v_accvgpr_read_b32 v52, a72\n"
"v_accvgpr_read_b32 v53, a73\n"
"v_accvgpr_read_b32 v54, a74\n"
"v_accvgpr_read_b32 v55, a75\n"
"v_cvt_pk_bf16_f32 v68, v52, v53\n"
"v_cvt_pk_bf16_f32 v69, v54, v55\n"
"v_accvgpr_read_b32 v56, a76\n"
"v_accvgpr_read_b32 v57, a77\n"
"v_accvgpr_read_b32 v58, a78\n"
"v_accvgpr_read_b32 v59, a79\n"
"v_cvt_pk_bf16_f32 v70, v56, v57\n"
"v_cvt_pk_bf16_f32 v71, v58, v59\n"
"buffer_store_dwordx4 v[68:71], %[vst], s[56:59], s54 offen offset:2048 sc1\n"
"v_accvgpr_read_b32 v52, a104\n"
"v_accvgpr_read_b32 v53, a105\n"
"v_accvgpr_read_b32 v54, a106\n"
"v_accvgpr_read_b32 v55, a107\n"
"v_cvt_pk_bf16_f32 v72, v52, v53\n"
"v_cvt_pk_bf16_f32 v73, v54, v55\n"
"v_accvgpr_read_b32 v56, a108\n"
"v_accvgpr_read_b32 v57, a109\n"
"v_accvgpr_read_b32 v58, a110\n"
"v_accvgpr_read_b32 v59, a111\n"
"v_cvt_pk_bf16_f32 v74, v56, v57\n"
"v_cvt_pk_bf16_f32 v75, v58, v59\n"
"buffer_store_dwordx4 v[72:75], %[vst], s[56:59], s54 offen offset:3072 sc1\n"
"s_add_u32 s54, %[o0], 524288\n"
"v_cvt_pk_bf16_f32 v60, v144, v145\n"
"v_cvt_pk_bf16_f32 v61, v146, v147\n"
"v_cvt_pk_bf16_f32 v62, v148, v149\n"
"v_cvt_pk_bf16_f32 v63, v150, v151\n"
"buffer_store_dwordx4 v[60:63], %[vst], s[56:59], s54 offen offset:0 sc1\n"
"v_cvt_pk_bf16_f32 v64, v176, v177\n"
"v_cvt_pk_bf16_f32 v65, v178, v179\n"
"v_cvt_pk_bf16_f32 v66, v180, v181\n"
"v_cvt_pk_bf16_f32 v67, v182, v183\n"
"buffer_store_dwordx4 v[64:67], %[vst], s[56:59], s54 offen offset:1024 sc1\n"
"v_cvt_pk_bf16_f32 v68, v208, v209\n"
"v_cvt_pk_bf16_f32 v69, v210, v211\n"
"v_cvt_pk_bf16_f32 v70, v212, v213\n"
"v_cvt_pk_bf16_f32 v71, v214, v215\n"
"buffer_store_dwordx4 v[68:71], %[vst], s[56:59], s54 offen offset:2048 sc1\n"
"v_cvt_pk_bf16_f32 v72, v240, v241\n"
"v_cvt_pk_bf16_f32 v73, v242, v243\n"
"v_cvt_pk_bf16_f32 v74, v244, v245\n"
"v_cvt_pk_bf16_f32 v75, v246, v247\n"
"buffer_store_dwordx4 v[72:75], %[vst], s[56:59], s54 offen offset:3072 sc1\n"
"s_add_u32 s54, %[o0], 524288\n"
"s_add_u32 s54, s54, %[o1]\n"
"v_accvgpr_read_b32 v52, a16\n"
"v_accvgpr_read_b32 v53, a17\n"
"v_accvgpr_read_b32 v54, a18\n"
"v_accvgpr_read_b32 v55, a19\n"
"v_cvt_pk_bf16_f32 v60, v52, v53\n"
"v_cvt_pk_bf16_f32 v61, v54, v55\n"
"v_accvgpr_read_b32 v56, a20\n"
"v_accvgpr_read_b32 v57, a21\n"
"v_accvgpr_read_b32 v58, a22\n"
"v_accvgpr_read_b32 v59, a23\n"
"v_cvt_pk_bf16_f32 v62, v56, v57\n"
"v_cvt_pk_bf16_f32 v63, v58, v59\n"
"buffer_store_dwordx4 v[60:63], %[vst], s[56:59], s54 offen offset:0 sc1\n"
"v_accvgpr_read_b32 v52, a48\n"
"v_accvgpr_read_b32 v53, a49\n"
"v_accvgpr_read_b32 v54, a50\n"
"v_accvgpr_read_b32 v55, a51\n"
"v_cvt_pk_bf16_f32 v64, v52, v53\n"
"v_cvt_pk_bf16_f32 v65, v54, v55\n"
"v_accvgpr_read_b32 v56, a52\n"
"v_accvgpr_read_b32 v57, a53\n"
"v_accvgpr_read_b32 v58, a54\n"
"v_accvgpr_read_b32 v59, a55\n"
"v_cvt_pk_bf16_f32 v66, v56, v57\n"
"v_cvt_pk_bf16_f32 v67, v58, v59\n"
"buffer_store_dwordx4 v[64:67], %[vst], s[56:59], s54 offen offset:1024 sc1\n"
"v_accvgpr_read_b32 v52, a80\n"
"v_accvgpr_read_b32 v53, a81\n"
"v_accvgpr_read_b32 v54, a82\n"
"v_accvgpr_read_b32 v55, a83\n"
"v_cvt_pk_bf16_f32 v68, v52, v53\n"
"v_cvt_pk_bf16_f32 v69, v54, v55\n"
"v_accvgpr_read_b32 v56, a84\n"
"v_accvgpr_read_b32 v57, a85\n"
"v_accvgpr_read_b32 v58, a86\n"
"v_accvgpr_read_b32 v59, a87\n"
"v_cvt_pk_bf16_f32 v70, v56, v57\n"
"v_cvt_pk_bf16_f32 v71, v58, v59\n"
"buffer_store_dwordx4 v[68:71], %[vst], s[56:59], s54 offen offset:2048 sc1\n"
"v_accvgpr_read_b32 v52, a112\n"
"v_accvgpr_read_b32 v53, a113\n"
"v_accvgpr_read_b32 v54, a114\n"
"v_accvgpr_read_b32 v55, a115\n"
"v_cvt_pk_bf16_f32 v72, v52, v53\n"
"v_cvt_pk_bf16_f32 v73, v54, v55\n"
"v_accvgpr_read_b32 v56, a116\n"
"v_accvgpr_read_b32 v57, a117\n"
"v_accvgpr_read_b32 v58, a118\n"
"v_accvgpr_read_b32 v59, a119\n"
"v_cvt_pk_bf16_f32 v74, v56, v57\n"
"v_cvt_pk_bf16_f32 v75, v58, v59\n"
"buffer_store_dwordx4 v[72:75], %[vst], s[56:59], s54 offen offset:3072 sc1\n"
"s_add_u32 s54, %[o0], 786432\n"
"v_cvt_pk_bf16_f32 v60, v152, v153\n"
"v_cvt_pk_bf16_f32 v61, v154, v155\n"
"v_cvt_pk_bf16_f32 v62, v156, v157\n"
"v_cvt_pk_bf16_f32 v63, v158, v159\n"
"buffer_store_dwordx4 v[60:63], %[vst], s[56:59], s54 offen offset:0 sc1\n"
"v_cvt_pk_bf16_f32 v64, v184, v185\n"
"v_cvt_pk_bf16_f32 v65, v186, v187\n"
"v_cvt_pk_bf16_f32 v66, v188, v189\n"
"v_cvt_pk_bf16_f32 v67, v190, v191\n"
"buffer_store_dwordx4 v[64:67], %[vst], s[56:59], s54 offen offset:1024 sc1\n"
"v_cvt_pk_bf16_f32 v68, v216, v217\n"
"v_cvt_pk_bf16_f32 v69, v218, v219\n"
"v_cvt_pk_bf16_f32 v70, v220, v221\n"
"v_cvt_pk_bf16_f32 v71, v222, v223\n"
"buffer_store_dwordx4 v[68:71], %[vst], s[56:59], s54 offen offset:2048 sc1\n"
"v_cvt_pk_bf16_f32 v72, v248, v249\n"
"v_cvt_pk_bf16_f32 v73, v250, v251\n"
"v_cvt_pk_bf16_f32 v74, v252, v253\n"
"v_cvt_pk_bf16_f32 v75, v254, v255\n"
"buffer_store_dwordx4 v[72:75], %[vst], s[56:59], s54 offen offset:3072 sc1\n"
"s_add_u32 s54, %[o0], 786432\n"
"s_add_u32 s54, s54, %[o1]\n"
"v_accvgpr_read_b32 v52, a24\n"
"v_accvgpr_read_b32 v53, a25\n"
"v_accvgpr_read_b32 v54, a26\n"
"v_accvgpr_read_b32 v55, a27\n"
"v_cvt_pk_bf16_f32 v60, v52, v53\n"
"v_cvt_pk_bf16_f32 v61, v54, v55\n"
"v_accvgpr_read_b32 v56, a28\n"
"v_accvgpr_read_b32 v57, a29\n"
"v_accvgpr_read_b32 v58, a30\n"
"v_accvgpr_read_b32 v59, a31\n"
"v_cvt_pk_bf16_f32 v62, v56, v57\n"
"v_cvt_pk_bf16_f32 v63, v58, v59\n"
"buffer_store_dwordx4 v[60:63], %[vst], s[56:59], s54 offen offset:0 sc1\n"
"v_accvgpr_read_b32 v52, a56\n"
"v_accvgpr_read_b32 v53, a57\n"
"v_accvgpr_read_b32 v54, a58\n"
"v_accvgpr_read_b32 v55, a59\n"
"v_cvt_pk_bf16_f32 v64, v52, v53\n"
"v_cvt_pk_bf16_f32 v65, v54, v55\n"
"v_accvgpr_read_b32 v56, a60\n"
"v_accvgpr_read_b32 v57, a61\n"
"v_accvgpr_read_b32 v58, a62\n"
"v_accvgpr_read_b32 v59, a63\n"
"v_cvt_pk_bf16_f32 v66, v56, v57\n"
"v_cvt_pk_bf16_f32 v67, v58, v59\n"
"buffer_store_dwordx4 v[64:67], %[vst], s[56:59], s54 offen offset:1024 sc1\n"
"v_accvgpr_read_b32 v52, a88\n"
"v_accvgpr_read_b32 v53, a89\n"
"v_accvgpr_read_b32 v54, a90\n"
"v_accvgpr_read_b32 v55, a91\n"
"v_cvt_pk_bf16_f32 v68, v52, v53\n"
"v_cvt_pk_bf16_f32 v69, v54, v55\n"
"v_accvgpr_read_b32 v56, a92\n"
"v_accvgpr_read_b32 v57, a93\n"
"v_accvgpr_read_b32 v58, a94\n"
"v_accvgpr_read_b32 v59, a95\n"
"v_cvt_pk_bf16_f32 v70, v56, v57\n"
"v_cvt_pk_bf16_f32 v71, v58, v59\n"
"buffer_store_dwordx4 v[68:71], %[vst], s[56:59], s54 offen offset:2048 sc1\n"
"v_accvgpr_read_b32 v52, a120\n"
"v_accvgpr_read_b32 v53, a121\n"
"v_accvgpr_read_b32 v54, a122\n"
"v_accvgpr_read_b32 v55, a123\n"
"v_cvt_pk_bf16_f32 v72, v52, v53\n"
"v_cvt_pk_bf16_f32 v73, v54, v55\n"
"v_accvgpr_read_b32 v56, a124\n"
"v_accvgpr_read_b32 v57, a125\n"
"v_accvgpr_read_b32 v58, a126\n"
"v_accvgpr_read_b32 v59, a127\n"
"v_cvt_pk_bf16_f32 v74, v56, v57\n"
"v_cvt_pk_bf16_f32 v75, v58, v59\n"
"buffer_store_dwordx4 v[72:75], %[vst], s[56:59], s54 offen offset:3072 sc1\n"
"s_branch END_%=\n"
"SW_%=:\n"
"s_mov_b32 s50, 0\n"
"s_mov_b32 s51, 0\n"
"s_add_u32 m0, s50, s60\n"
"s_nop 0\n"
"buffer_load_dwordx4 v24, s[36:39], s44 offen offset:0 sc1 lds\n"
"buffer_load_dwordx4 v25, s[36:39], s44 offen offset:1024 sc1 lds\n"
"buffer_load_dwordx4 v26, s[36:39], s44 offen offset:2048 sc1 lds\n"
"buffer_load_dwordx4 v27, s[36:39], s44 offen offset:3072 sc1 lds\n"
"s_add_u32 m0, s50, s61\n"
"s_nop 0\n"
"buffer_load_dwordx4 v28, s[36:39], s44 offen offset:0 sc1 lds\n"
"buffer_load_dwordx4 v29, s[36:39], s44 offen offset:1024 sc1 lds\n"
"buffer_load_dwordx4 v30, s[36:39], s44 offen offset:2048 sc1 lds\n"
"buffer_load_dwordx4 v31, s[36:39], s44 offen offset:3072 sc1 lds\n"
"s_add_u32 m0, s50, s62\n"
"s_nop 0\n"
"buffer_load_dwordx4 v32, s[40:43], s45 offen offset:0 lds\n"
"buffer_load_dwordx4 v33, s[40:43], s45 offen offset:1024 lds\n"
"buffer_load_dwordx4 v34, s[40:43], s45 offen offset:2048 lds\n"
"buffer_load_dwordx4 v35, s[40:43], s45 offen offset:3072 lds\n"
"s_add_u32 m0, s50, s63\n"
"s_nop 0\n"
"buffer_load_dwordx4 v36, s[40:43], s45 offen offset:0 lds\n"
"buffer_load_dwordx4 v37, s[40:43], s45 offen offset:1024 lds\n"
"buffer_load_dwordx4 v38, s[40:43], s45 offen offset:2048 lds\n"
"buffer_load_dwordx4 v39, s[40:43], s45 offen offset:3072 lds\n"
"s_add_u32 s44, s44, 2048\n"
"s_add_u32 s45, s45, s48\n"
"s_add_u32 s50, s50, 65536\n"
"s_and_b32 s50, s50, 131071\n"
"v_mov_b64 v[128:129], 0\n"
"v_mov_b64 v[130:131], 0\n"
"v_mov_b64 v[136:137], 0\n"
"v_mov_b64 v[138:139], 0\n"
"v_mov_b64 v[144:145], 0\n"
"v_mov_b64 v[146:147], 0\n"
"v_mov_b64 v[152:153], 0\n"
"v_mov_b64 v[154:155], 0\n"
"v_mov_b64 v[160:161], 0\n"
"v_mov_b64 v[162:163], 0\n"
"v_mov_b64 v[168:169], 0\n"
"v_mov_b64 v[170:171], 0\n"
"v_mov_b64 v[176:177], 0\n"
"v_mov_b64 v[178:179], 0\n"
"v_mov_b64 v[184:185], 0\n"
"v_mov_b64 v[186:187], 0\n"
"v_mov_b64 v[192:193], 0\n"
"v_mov_b64 v[194:195], 0\n"
"v_mov_b64 v[200:201], 0\n"
"v_mov_b64 v[202:203], 0\n"
"v_mov_b64 v[208:209], 0\n"
"v_mov_b64 v[210:211], 0\n"
"v_mov_b64 v[216:217], 0\n"
"v_mov_b64 v[218:219], 0\n"
"v_mov_b64 v[224:225], 0\n"
"v_mov_b64 v[226:227], 0\n"
"v_mov_b64 v[232:233], 0\n"
"v_mov_b64 v[234:235], 0\n"
"v_mov_b64 v[240:241], 0\n"
"v_mov_b64 v[242:243], 0\n"
"v_mov_b64 v[248:249], 0\n"
"v_mov_b64 v[250:251], 0\n"
"v_accvgpr_write_b32 a0, 0\n"
"v_accvgpr_write_b32 a1, 0\n"
"v_accvgpr_write_b32 a2, 0\n"
"v_accvgpr_write_b32 a3, 0\n"
"v_accvgpr_write_b32 a8, 0\n"
"v_accvgpr_write_b32 a9, 0\n"
"v_accvgpr_write_b32 a10, 0\n"
"v_accvgpr_write_b32 a11, 0\n"
"v_accvgpr_write_b32 a16, 0\n"
"v_accvgpr_write_b32 a17, 0\n"
"v_accvgpr_write_b32 a18, 0\n"
"v_accvgpr_write_b32 a19, 0\n"
"v_accvgpr_write_b32 a24, 0\n"
"v_accvgpr_write_b32 a25, 0\n"
"v_accvgpr_write_b32 a26, 0\n"
"v_accvgpr_write_b32 a27, 0\n"
"v_accvgpr_write_b32 a32, 0\n"
"v_accvgpr_write_b32 a33, 0\n"
"v_accvgpr_write_b32 a34, 0\n"
"v_accvgpr_write_b32 a35, 0\n"
"v_accvgpr_write_b32 a40, 0\n"
"v_accvgpr_write_b32 a41, 0\n"
"v_accvgpr_write_b32 a42, 0\n"
"v_accvgpr_write_b32 a43, 0\n"
"v_accvgpr_write_b32 a48, 0\n"
"v_accvgpr_write_b32 a49, 0\n"
"v_accvgpr_write_b32 a50, 0\n"
"v_accvgpr_write_b32 a51, 0\n"
"v_accvgpr_write_b32 a56, 0\n"
"v_accvgpr_write_b32 a57, 0\n"
"v_accvgpr_write_b32 a58, 0\n"
"v_accvgpr_write_b32 a59, 0\n"
"v_accvgpr_write_b32 a64, 0\n"
"v_accvgpr_write_b32 a65, 0\n"
"v_accvgpr_write_b32 a66, 0\n"
"v_accvgpr_write_b32 a67, 0\n"
"v_accvgpr_write_b32 a72, 0\n"
"v_accvgpr_write_b32 a73, 0\n"
"v_accvgpr_write_b32 a74, 0\n"
"v_accvgpr_write_b32 a75, 0\n"
"v_accvgpr_write_b32 a80, 0\n"
"v_accvgpr_write_b32 a81, 0\n"
"v_accvgpr_write_b32 a82, 0\n"
"v_accvgpr_write_b32 a83, 0\n"
"v_accvgpr_write_b32 a88, 0\n"
"v_accvgpr_write_b32 a89, 0\n"
"v_accvgpr_write_b32 a90, 0\n"
"v_accvgpr_write_b32 a91, 0\n"
"v_accvgpr_write_b32 a96, 0\n"
"v_accvgpr_write_b32 a97, 0\n"
"v_accvgpr_write_b32 a98, 0\n"
"v_accvgpr_write_b32 a99, 0\n"
"v_accvgpr_write_b32 a104, 0\n"
"v_accvgpr_write_b32 a105, 0\n"
"v_accvgpr_write_b32 a106, 0\n"
"v_accvgpr_write_b32 a107, 0\n"
"v_accvgpr_write_b32 a112, 0\n"
"v_accvgpr_write_b32 a113, 0\n"
"v_accvgpr_write_b32 a114, 0\n"
"v_accvgpr_write_b32 a115, 0\n"
"v_accvgpr_write_b32 a120, 0\n"
"v_accvgpr_write_b32 a121, 0\n"
"v_accvgpr_write_b32 a122, 0\n"
"v_accvgpr_write_b32 a123, 0\n"
"v_mov_b64 v[132:133], 0\n"
"v_mov_b64 v[134:135], 0\n"
"v_mov_b64 v[140:141], 0\n"
"v_mov_b64 v[142:143], 0\n"
"v_mov_b64 v[148:149], 0\n"
"v_mov_b64 v[150:151], 0\n"
"v_mov_b64 v[156:157], 0\n"
"v_mov_b64 v[158:159], 0\n"
"v_mov_b64 v[164:165], 0\n"
"v_mov_b64 v[166:167], 0\n"
"v_mov_b64 v[172:173], 0\n"
"v_mov_b64 v[174:175], 0\n"
"v_mov_b64 v[180:181], 0\n"
"v_mov_b64 v[182:183], 0\n"
"v_mov_b64 v[188:189], 0\n"
"v_mov_b64 v[190:191], 0\n"
"v_mov_b64 v[196:197], 0\n"
"v_mov_b64 v[198:199], 0\n"
"v_mov_b64 v[204:205], 0\n"
"v_mov_b64 v[206:207], 0\n"
"v_mov_b64 v[212:213], 0\n"
"v_mov_b64 v[214:215], 0\n"
"v_mov_b64 v[220:221], 0\n"
"v_mov_b64 v[222:223], 0\n"
"v_mov_b64 v[228:229], 0\n"
"v_mov_b64 v[230:231], 0\n"
"v_mov_b64 v[236:237], 0\n"
"v_mov_b64 v[238:239], 0\n"
"v_mov_b64 v[244:245], 0\n"
"v_mov_b64 v[246:247], 0\n"
"v_mov_b64 v[252:253], 0\n"
"v_mov_b64 v[254:255], 0\n"
"v_accvgpr_write_b32 a4, 0\n"
"v_accvgpr_write_b32 a5, 0\n"
"v_accvgpr_write_b32 a6, 0\n"
"v_accvgpr_write_b32 a7, 0\n"
"v_accvgpr_write_b32 a12, 0\n"
"v_accvgpr_write_b32 a13, 0\n"
"v_accvgpr_write_b32 a14, 0\n"
"v_accvgpr_write_b32 a15, 0\n"
"v_accvgpr_write_b32 a20, 0\n"
"v_accvgpr_write_b32 a21, 0\n"
"v_accvgpr_write_b32 a22, 0\n"
"v_accvgpr_write_b32 a23, 0\n"
"v_accvgpr_write_b32 a28, 0\n"
"v_accvgpr_write_b32 a29, 0\n"
"v_accvgpr_write_b32 a30, 0\n"
"v_accvgpr_write_b32 a31, 0\n"
"v_accvgpr_write_b32 a36, 0\n"
"v_accvgpr_write_b32 a37, 0\n"
"v_accvgpr_write_b32 a38, 0\n"
"v_accvgpr_write_b32 a39, 0\n"
"v_accvgpr_write_b32 a44, 0\n"
"v_accvgpr_write_b32 a45, 0\n"
"v_accvgpr_write_b32 a46, 0\n"
"v_accvgpr_write_b32 a47, 0\n"
"v_accvgpr_write_b32 a52, 0\n"
"v_accvgpr_write_b32 a53, 0\n"
"v_accvgpr_write_b32 a54, 0\n"
"v_accvgpr_write_b32 a55, 0\n"
"v_accvgpr_write_b32 a60, 0\n"
"v_accvgpr_write_b32 a61, 0\n"
"v_accvgpr_write_b32 a62, 0\n"
"v_accvgpr_write_b32 a63, 0\n"
"v_accvgpr_write_b32 a68, 0\n"
"v_accvgpr_write_b32 a69, 0\n"
"v_accvgpr_write_b32 a70, 0\n"
"v_accvgpr_write_b32 a71, 0\n"
"v_accvgpr_write_b32 a76, 0\n"
"v_accvgpr_write_b32 a77, 0\n"
"v_accvgpr_write_b32 a78, 0\n"
"v_accvgpr_write_b32 a79, 0\n"
"v_accvgpr_write_b32 a84, 0\n"
"v_accvgpr_write_b32 a85, 0\n"
"v_accvgpr_write_b32 a86, 0\n"
"v_accvgpr_write_b32 a87, 0\n"
"v_accvgpr_write_b32 a92, 0\n"
"v_accvgpr_write_b32 a93, 0\n"
"v_accvgpr_write_b32 a94, 0\n"
"v_accvgpr_write_b32 a95, 0\n"
"v_accvgpr_write_b32 a100, 0\n"
"v_accvgpr_write_b32 a101, 0\n"
"v_accvgpr_write_b32 a102, 0\n"
"v_accvgpr_write_b32 a103, 0\n"
"v_accvgpr_write_b32 a108, 0\n"
"v_accvgpr_write_b32 a109, 0\n"
"v_accvgpr_write_b32 a110, 0\n"
"v_accvgpr_write_b32 a111, 0\n"
"v_accvgpr_write_b32 a116, 0\n"
"v_accvgpr_write_b32 a117, 0\n"
"v_accvgpr_write_b32 a118, 0\n"
"v_accvgpr_write_b32 a119, 0\n"
"v_accvgpr_write_b32 a124, 0\n"
"v_accvgpr_write_b32 a125, 0\n"
"v_accvgpr_write_b32 a126, 0\n"
"v_accvgpr_write_b32 a127, 0\n"
"v_add_u32 v40, s51, v46\n"
"v_add_u32 v41, s51, v47\n"
"v_add_u32 v42, s51, v48\n"
"v_add_u32 v43, s51, v49\n"
"v_add_u32 v44, s51, v50\n"
"v_add_u32 v45, s51, v51\n"
"s_add_u32 m0, s50, s60\n"
"s_nop 0\n"
"buffer_load_dwordx4 v24, s[36:39], s44 offen offset:0 sc1 lds\n"
"buffer_load_dwordx4 v25, s[36:39], s44 offen offset:1024 sc1 lds\n"
"buffer_load_dwordx4 v26, s[36:39], s44 offen offset:2048 sc1 lds\n"
"buffer_load_dwordx4 v27, s[36:39], s44 offen offset:3072 sc1 lds\n"
"s_add_u32 m0, s50, s61\n"
"s_nop 0\n"
"buffer_load_dwordx4 v28, s[36:39], s44 offen offset:0 sc1 lds\n"
"buffer_load_dwordx4 v29, s[36:39], s44 offen offset:1024 sc1 lds\n"
"buffer_load_dwordx4 v30, s[36:39], s44 offen offset:2048 sc1 lds\n"
"buffer_load_dwordx4 v31, s[36:39], s44 offen offset:3072 sc1 lds\n"
"s_add_u32 m0, s50, s62\n"
"s_nop 0\n"
"buffer_load_dwordx4 v32, s[40:43], s45 offen offset:0 lds\n"
"buffer_load_dwordx4 v33, s[40:43], s45 offen offset:1024 lds\n"
"buffer_load_dwordx4 v34, s[40:43], s45 offen offset:2048 lds\n"
"buffer_load_dwordx4 v35, s[40:43], s45 offen offset:3072 lds\n"
"s_add_u32 m0, s50, s63\n"
"s_nop 0\n"
"buffer_load_dwordx4 v36, s[40:43], s45 offen offset:0 lds\n"
"buffer_load_dwordx4 v37, s[40:43], s45 offen offset:1024 lds\n"
"buffer_load_dwordx4 v38, s[40:43], s45 offen offset:2048 lds\n"
"buffer_load_dwordx4 v39, s[40:43], s45 offen offset:3072 lds\n"
"s_add_u32 s44, s44, 2048\n"
"s_add_u32 s45, s45, s48\n"
"s_add_u32 s50, s50, 65536\n"
"s_and_b32 s50, s50, 131071\n"
"s_waitcnt vmcnt(16)\n"
"s_barrier\n"
"ds_read_b64_tr_b16 a[160:161], v42 offset:32768\n"
"ds_read_b64_tr_b16 a[162:163], v42 offset:33792\n"
"ds_read_b128 a[128:131], v40 offset:0\n"
"ds_read_b128 a[132:135], v40 offset:2048\n"
"ds_read_b128 a[136:139], v40 offset:4096\n"
"ds_read_b128 a[140:143], v40 offset:6144\n"
"ds_read_b128 a[144:147], v40 offset:8192\n"
"ds_read_b128 a[148:151], v40 offset:10240\n"
"ds_read_b128 a[152:155], v40 offset:12288\n"
"ds_read_b128 a[156:159], v40 offset:14336\n"
"ds_read_b64_tr_b16 a[164:165], v43 offset:32768\n"
"ds_read_b64_tr_b16 a[166:167], v43 offset:33792\n"
"ds_read_b64_tr_b16 a[168:169], v44 offset:32768\n"
"ds_read_b64_tr_b16 a[170:171], v44 offset:33792\n"
"ds_read_b64_tr_b16 a[172:173], v45 offset:32768\n"
"ds_read_b64_tr_b16 a[174:175], v45 offset:33792\n"
"ds_read_b64_tr_b16 a[176:177], v42 offset:49152\n"
"ds_read_b64_tr_b16 a[178:179], v42 offset:50176\n"
"ds_read_b64_tr_b16 a[180:181], v43 offset:49152\n"
"ds_read_b64_tr_b16 a[182:183], v43 offset:50176\n"
"ds_read_b64_tr_b16 a[184:185], v44 offset:49152\n"
"ds_read_b64_tr_b16 a[186:187], v44 offset:50176\n"
"ds_read_b64_tr_b16 a[188:189], v45 offset:49152\n"
"ds_read_b64_tr_b16 a[190:191], v45 offset:50176\n"
"s_sub_u32 s46, s47, 2\n"
"s_waitcnt lgkmcnt(0)\n"
"KL_s_%=:\n"
"v_mfma_f32_16x16x32_bf16 v[128:131], a[160:163], a[128:131], v[128:131]\n"
"ds_read_b64_tr_b16 a[224:225], v42 offset:40960\n"
"v_mfma_f32_16x16x32_bf16 v[132:135], a[160:163], a[132:135], v[132:135]\n"
"ds_read_b64_tr_b16 a[226:227], v42 offset:41984\n"
"v_mfma_f32_16x16x32_bf16 v[136:139], a[160:163], a[136:139], v[136:139]\n"
"ds_read_b128 a[192:195], v41 offset:0\n"
"v_mfma_f32_16x16x32_bf16 v[140:143], a[160:163], a[140:143], v[140:143]\n"
"ds_read_b128 a[196:199], v41 offset:2048\n"
"v_mfma_f32_16x16x32_bf16 v[144:147], a[160:163], a[144:147], v[144:147]\n"
"ds_read_b128 a[200:203], v41 offset:4096\n"
"v_mfma_f32_16x16x32_bf16 v[148:151], a[160:163], a[148:151], v[148:151]\n"
"ds_read_b128 a[204:207], v41 offset:6144\n"
"v_mfma_f32_16x16x32_bf16 v[152:155], a[160:163], a[152:155], v[152:155]\n"
"ds_read_b128 a[208:211], v41 offset:8192\n"
"v_mfma_f32_16x16x32_bf16 v[156:159], a[160:163], a[156:159], v[156:159]\n"
"ds_read_b128 a[212:215], v41 offset:10240\n"
"v_mfma_f32_16x16x32_bf16 v[160:163], a[164:167], a[128:131], v[160:163]\n"
"ds_read_b128 a[216:219], v41 offset:12288\n"
"v_mfma_f32_16x16x32_bf16 v[164:167], a[164:167], a[132:135], v[164:167]\n"
"ds_read_b128 a[220:223], v41 offset:14336\n"
"v_mfma_f32_16x16x32_bf16 v[168:171], a[164:167], a[136:139], v[168:171]\n"
"ds_read_b64_tr_b16 a[228:229], v43 offset:40960\n"
"v_mfma_f32_16x16x32_bf16 v[172:175], a[164:167], a[140:143], v[172:175]\n"
"ds_read_b64_tr_b16 a[230:231], v43 offset:41984\n"
"v_mfma_f32_16x16x32_bf16 v[176:179], a[164:167], a[144:147], v[176:179]\n"
"ds_read_b64_tr_b16 a[232:233], v44 offset:40960\n"
"v_mfma_f32_16x16x32_bf16 v[180:183], a[164:167], a[148:151], v[180:183]\n"
"ds_read_b64_tr_b16 a[234:235], v44 offset:41984\n"
"v_mfma_f32_16x16x32_bf16 v[184:187], a[164:167], a[152:155], v[184:187]\n"
"ds_read_b64_tr_b16 a[236:237], v45 offset:40960\n"
"v_mfma_f32_16x16x32_bf16 v[188:191], a[164:167], a[156:159], v[188:191]\n"
"ds_read_b64_tr_b16 a[238:239], v45 offset:41984\n"
"v_mfma_f32_16x16x32_bf16 v[192:195], a[168:171], a[128:131], v[192:195]\n"
"ds_read_b64_tr_b16 a[240:241], v42 offset:57344\n"
"v_mfma_f32_16x16x32_bf16 v[196:199], a[168:171], a[132:135], v[196:199]\n"
"ds_read_b64_tr_b16 a[242:243], v42 offset:58368\n"
"v_mfma_f32_16x16x32_bf16 v[200:203], a[168:171], a[136:139], v[200:203]\n"
"ds_read_b64_tr_b16 a[244:245], v43 offset:57344\n"
"v_mfma_f32_16x16x32_bf16 v[204:207], a[168:171], a[140:143], v[204:207]\n"
"ds_read_b64_tr_b16 a[246:247], v43 offset:58368\n"
"v_mfma_f32_16x16x32_bf16 v[208:211], a[168:171], a[144:147], v[208:211]\n"
"ds_read_b64_tr_b16 a[248:249], v44 offset:57344\n"
"v_mfma_f32_16x16x32_bf16 v[212:215], a[168:171], a[148:151], v[212:215]\n"
"ds_read_b64_tr_b16 a[250:251], v44 offset:58368\n"
"v_mfma_f32_16x16x32_bf16 v[216:219], a[168:171], a[152:155], v[216:219]\n"
"ds_read_b64_tr_b16 a[252:253], v45 offset:57344\n"
"v_mfma_f32_16x16x32_bf16 v[220:223], a[168:171], a[156:159], v[220:223]\n"
"ds_read_b64_tr_b16 a[254:255], v45 offset:58368\n"
"v_mfma_f32_16x16x32_bf16 v[224:227], a[172:175], a[128:131], v[224:227]\n"
"v_mfma_f32_16x16x32_bf16 v[228:231], a[172:175], a[132:135], v[228:231]\n"
"v_mfma_f32_16x16x32_bf16 v[232:235], a[172:175], a[136:139], v[232:235]\n"
"v_mfma_f32_16x16x32_bf16 v[236:239], a[172:175], a[140:143], v[236:239]\n"
"s_add_u32 s51, s51, 65536\n"
"s_and_b32 s51, s51, 131071\n"
"v_mfma_f32_16x16x32_bf16 v[240:243], a[172:175], a[144:147], v[240:243]\n"
"v_mfma_f32_16x16x32_bf16 v[244:247], a[172:175], a[148:151], v[244:247]\n"
"s_waitcnt lgkmcnt(0)\n"
"s_barrier\n"
"v_mfma_f32_16x16x32_bf16 v[248:251], a[172:175], a[152:155], v[248:251]\n"
"s_add_u32 m0, s50, s60\n"
"v_mfma_f32_16x16x32_bf16 v[252:255], a[172:175], a[156:159], v[252:255]\n"
"buffer_load_dwordx4 v24, s[36:39], s44 offen offset:0 sc1 lds\n"
"v_mfma_f32_16x16x32_bf16 a[0:3], a[176:179], a[128:131], a[0:3]\n"
"v_mfma_f32_16x16x32_bf16 a[4:7], a[176:179], a[132:135], a[4:7]\n"
"v_mfma_f32_16x16x32_bf16 a[8:11], a[176:179], a[136:139], a[8:11]\n"
"v_mfma_f32_16x16x32_bf16 a[12:15], a[176:179], a[140:143], a[12:15]\n"
"buffer_load_dwordx4 v25, s[36:39], s44 offen offset:1024 sc1 lds\n"
"v_mfma_f32_16x16x32_bf16 a[16:19], a[176:179], a[144:147], a[16:19]\n"
"v_mfma_f32_16x16x32_bf16 a[20:23], a[176:179], a[148:151], a[20:23]\n"
"v_mfma_f32_16x16x32_bf16 a[24:27], a[176:179], a[152:155], a[24:27]\n"
"v_mfma_f32_16x16x32_bf16 a[28:31], a[176:179], a[156:159], a[28:31]\n"
"buffer_load_dwordx4 v26, s[36:39], s44 offen offset:2048 sc1 lds\n"
"v_mfma_f32_16x16x32_bf16 a[32:35], a[180:183], a[128:131], a[32:35]\n"
"v_mfma_f32_16x16x32_bf16 a[36:39], a[180:183], a[132:135], a[36:39]\n"
"v_mfma_f32_16x16x32_bf16 a[40:43], a[180:183], a[136:139], a[40:43]\n"
"v_mfma_f32_16x16x32_bf16 a[44:47], a[180:183], a[140:143], a[44:47]\n"
"buffer_load_dwordx4 v27, s[36:39], s44 offen offset:3072 sc1 lds\n"
"v_mfma_f32_16x16x32_bf16 a[48:51], a[180:183], a[144:147], a[48:51]\n"
"v_mfma_f32_16x16x32_bf16 a[52:55], a[180:183], a[148:151], a[52:55]\n"
"v_mfma_f32_16x16x32_bf16 a[56:59], a[180:183], a[152:155], a[56:59]\n"
"s_add_u32 m0, s50, s61\n"
"v_mfma_f32_16x16x32_bf16 a[60:63], a[180:183], a[156:159], a[60:63]\n"
"buffer_load_dwordx4 v28, s[36:39], s44 offen offset:0 sc1 lds\n"
"v_mfma_f32_16x16x32_bf16 a[64:67], a[184:187], a[128:131], a[64:67]\n"
"v_mfma_f32_16x16x32_bf16 a[68:71], a[184:187], a[132:135], a[68:71]\n"
"v_mfma_f32_16x16x32_bf16 a[72:75], a[184:187], a[136:139], a[72:75]\n"
"v_mfma_f32_16x16x32_bf16 a[76:79], a[184:187], a[140:143], a[76:79]\n"
"buffer_load_dwordx4 v29, s[36:39], s44 offen offset:1024 sc1 lds\n"
"v_mfma_f32_16x16x32_bf16 a[80:83], a[184:187], a[144:147], a[80:83]\n"
"v_mfma_f32_16x16x32_bf16 a[84:87], a[184:187], a[148:151], a[84:87]\n"
"v_mfma_f32_16x16x32_bf16 a[88:91], a[184:187], a[152:155], a[88:91]\n"
"v_mfma_f32_16x16x32_bf16 a[92:95], a[184:187], a[156:159], a[92:95]\n"
"buffer_load_dwordx4 v30, s[36:39], s44 offen offset:2048 sc1 lds\n"
"v_mfma_f32_16x16x32_bf16 a[96:99], a[188:191], a[128:131], a[96:99]\n"
"v_mfma_f32_16x16x32_bf16 a[100:103], a[188:191], a[132:135], a[100:103]\n"
"v_mfma_f32_16x16x32_bf16 a[104:107], a[188:191], a[136:139], a[104:107]\n"
"v_mfma_f32_16x16x32_bf16 a[108:111], a[188:191], a[140:143], a[108:111]\n"
"buffer_load_dwordx4 v31, s[36:39], s44 offen offset:3072 sc1 lds\n"
"v_mfma_f32_16x16x32_bf16 a[112:115], a[188:191], a[144:147], a[112:115]\n"
"v_mfma_f32_16x16x32_bf16 a[116:119], a[188:191], a[148:151], a[116:119]\n"
"v_mfma_f32_16x16x32_bf16 a[120:123], a[188:191], a[152:155], a[120:123]\n"
"s_add_u32 m0, s50, s62\n"
"v_mfma_f32_16x16x32_bf16 a[124:127], a[188:191], a[156:159], a[124:127]\n"
"buffer_load_dwordx4 v32, s[40:43], s45 offen offset:0 lds\n"
"v_mfma_f32_16x16x32_bf16 v[128:131], a[224:227], a[192:195], v[128:131]\n"
"v_mfma_f32_16x16x32_bf16 v[132:135], a[224:227], a[196:199], v[132:135]\n"
"v_mfma_f32_16x16x32_bf16 v[136:139], a[224:227], a[200:203], v[136:139]\n"
"v_mfma_f32_16x16x32_bf16 v[140:143], a[224:227], a[204:207], v[140:143]\n"
"buffer_load_dwordx4 v33, s[40:43], s45 offen offset:1024 lds\n"
"v_mfma_f32_16x16x32_bf16 v[144:147], a[224:227], a[208:211], v[144:147]\n"
"v_mfma_f32_16x16x32_bf16 v[148:151], a[224:227], a[212:215], v[148:151]\n"
"v_mfma_f32_16x16x32_bf16 v[152:155], a[224:227], a[216:219], v[152:155]\n"
"v_mfma_f32_16x16x32_bf16 v[156:159], a[224:227], a[220:223], v[156:159]\n"
"buffer_load_dwordx4 v34, s[40:43], s45 offen offset:2048 lds\n"
"v_mfma_f32_16x16x32_bf16 v[160:163], a[228:231], a[192:195], v[160:163]\n"
"v_mfma_f32_16x16x32_bf16 v[164:167], a[228:231], a[196:199], v[164:167]\n"
"v_mfma_f32_16x16x32_bf16 v[168:171], a[228:231], a[200:203], v[168:171]\n"
"v_add_u32 v40, s51, v46\n"
"v_mfma_f32_16x16x32_bf16 v[172:175], a[228:231], a[204:207], v[172:175]\n"
"buffer_load_dwordx4 v35, s[40:43], s45 offen offset:3072 lds\n"
"v_mfma_f32_16x16x32_bf16 v[176:179], a[228:231], a[208:211], v[176:179]\n"
"v_mfma_f32_16x16x32_bf16 v[180:183], a[228:231], a[212:215], v[180:183]\n"
"v_mfma_f32_16x16x32_bf16 v[184:187], a[228:231], a[216:219], v[184:187]\n"
"s_add_u32 m0, s50, s63\n"
"v_add_u32 v41, s51, v47\n"
"v_mfma_f32_16x16x32_bf16 v[188:191], a[228:231], a[220:223], v[188:191]\n"
"buffer_load_dwordx4 v36, s[40:43], s45 offen offset:0 lds\n"
"v_mfma_f32_16x16x32_bf16 v[192:195], a[232:235], a[192:195], v[192:195]\n"
"v_mfma_f32_16x16x32_bf16 v[196:199], a[232:235], a[196:199], v[196:199]\n"
"v_mfma_f32_16x16x32_bf16 v[200:203], a[232:235], a[200:203], v[200:203]\n"
"v_add_u32 v42, s51, v48\n"
"v_mfma_f32_16x16x32_bf16 v[204:207], a[232:235], a[204:207], v[204:207]\n"
"buffer_load_dwordx4 v37, s[40:43], s45 offen offset:1024 lds\n"
"v_mfma_f32_16x16x32_bf16 v[208:211], a[232:235], a[208:211], v[208:211]\n"
"v_mfma_f32_16x16x32_bf16 v[212:215], a[232:235], a[212:215], v[212:215]\n"
"v_mfma_f32_16x16x32_bf16 v[216:219], a[232:235], a[216:219], v[216:219]\n"
"v_add_u32 v43, s51, v49\n"
"v_mfma_f32_16x16x32_bf16 v[220:223], a[232:235], a[220:223], v[220:223]\n"
"buffer_load_dwordx4 v38, s[40:43], s45 offen offset:2048 lds\n"
"v_mfma_f32_16x16x32_bf16 v[224:227], a[236:239], a[192:195], v[224:227]\n"
"v_mfma_f32_16x16x32_bf16 v[228:231], a[236:239], a[196:199], v[228:231]\n"
"v_mfma_f32_16x16x32_bf16 v[232:235], a[236:239], a[200:203], v[232:235]\n"
"v_add_u32 v44, s51, v50\n"
"v_mfma_f32_16x16x32_bf16 v[236:239], a[236:239], a[204:207], v[236:239]\n"
"buffer_load_dwordx4 v39, s[40:43], s45 offen offset:3072 lds\n"
"v_mfma_f32_16x16x32_bf16 v[240:243], a[236:239], a[208:211], v[240:243]\n"
"v_mfma_f32_16x16x32_bf16 v[244:247], a[236:239], a[212:215], v[244:247]\n"
"v_mfma_f32_16x16x32_bf16 v[248:251], a[236:239], a[216:219], v[248:251]\n"
"v_add_u32 v45, s51, v51\n"
"v_mfma_f32_16x16x32_bf16 v[252:255], a[236:239], a[220:223], v[252:255]\n"
"v_mfma_f32_16x16x32_bf16 a[0:3], a[240:243], a[192:195], a[0:3]\n"
"v_mfma_f32_16x16x32_bf16 a[4:7], a[240:243], a[196:199], a[4:7]\n"
"s_waitcnt vmcnt(16)\n"
"s_barrier\n"
"v_mfma_f32_16x16x32_bf16 a[8:11], a[240:243], a[200:203], a[8:11]\n"
"ds_read_b64_tr_b16 a[160:161], v42 offset:32768\n"
"v_mfma_f32_16x16x32_bf16 a[12:15], a[240:243], a[204:207], a[12:15]\n"
"ds_read_b64_tr_b16 a[162:163], v42 offset:33792\n"
"v_mfma_f32_16x16x32_bf16 a[16:19], a[240:243], a[208:211], a[16:19]\n"
"ds_read_b128 a[128:131], v40 offset:0\n"
"v_mfma_f32_16x16x32_bf16 a[20:23], a[240:243], a[212:215], a[20:23]\n"
"ds_read_b128 a[132:135], v40 offset:2048\n"
"v_mfma_f32_16x16x32_bf16 a[24:27], a[240:243], a[216:219], a[24:27]\n"
"ds_read_b128 a[136:139], v40 offset:4096\n"
"v_mfma_f32_16x16x32_bf16 a[28:31], a[240:243], a[220:223], a[28:31]\n"
"ds_read_b128 a[140:143], v40 offset:6144\n"
"v_mfma_f32_16x16x32_bf16 a[32:35], a[244:247], a[192:195], a[32:35]\n"
"ds_read_b128 a[144:147], v40 offset:8192\n"
"v_mfma_f32_16x16x32_bf16 a[36:39], a[244:247], a[196:199], a[36:39]\n"
"ds_read_b128 a[148:151], v40 offset:10240\n"
"v_mfma_f32_16x16x32_bf16 a[40:43], a[244:247], a[200:203], a[40:43]\n"
"ds_read_b128 a[152:155], v40 offset:12288\n"
"v_mfma_f32_16x16x32_bf16 a[44:47], a[244:247], a[204:207], a[44:47]\n"
"ds_read_b128 a[156:159], v40 offset:14336\n"
"v_mfma_f32_16x16x32_bf16 a[48:51], a[244:247], a[208:211], a[48:51]\n"
"ds_read_b64_tr_b16 a[164:165], v43 offset:32768\n"
"v_mfma_f32_16x16x32_bf16 a[52:55], a[244:247], a[212:215], a[52:55]\n"
"ds_read_b64_tr_b16 a[166:167], v43 offset:33792\n"
"v_mfma_f32_16x16x32_bf16 a[56:59], a[244:247], a[216:219], a[56:59]\n"
"ds_read_b64_tr_b16 a[168:169], v44 offset:32768\n"
"v_mfma_f32_16x16x32_bf16 a[60:63], a[244:247], a[220:223], a[60:63]\n"
"ds_read_b64_tr_b16 a[170:171], v44 offset:33792\n"
"v_mfma_f32_16x16x32_bf16 a[64:67], a[248:251], a[192:195], a[64:67]\n"
"ds_read_b64_tr_b16 a[172:173], v45 offset:32768\n"
"v_mfma_f32_16x16x32_bf16 a[68:71], a[248:251], a[196:199], a[68:71]\n"
"ds_read_b64_tr_b16 a[174:175], v45 offset:33792\n"
"v_mfma_f32_16x16x32_bf16 a[72:75], a[248:251], a[200:203], a[72:75]\n"
"ds_read_b64_tr_b16 a[176:177], v42 offset:49152\n"
"v_mfma_f32_16x16x32_bf16 a[76:79], a[248:251], a[204:207], a[76:79]\n"
"ds_read_b64_tr_b16 a[178:179], v42 offset:50176\n"
"v_mfma_f32_16x16x32_bf16 a[80:83], a[248:251], a[208:211], a[80:83]\n"
"ds_read_b64_tr_b16 a[180:181], v43 offset:49152\n"
"v_mfma_f32_16x16x32_bf16 a[84:87], a[248:251], a[212:215], a[84:87]\n"
"ds_read_b64_tr_b16 a[182:183], v43 offset:50176\n"
"v_mfma_f32_16x16x32_bf16 a[88:91], a[248:251], a[216:219], a[88:91]\n"
"ds_read_b64_tr_b16 a[184:185], v44 offset:49152\n"
"v_mfma_f32_16x16x32_bf16 a[92:95], a[248:251], a[220:223], a[92:95]\n"
"ds_read_b64_tr_b16 a[186:187], v44 offset:50176\n"
"v_mfma_f32_16x16x32_bf16 a[96:99], a[252:255], a[192:195], a[96:99]\n"
"ds_read_b64_tr_b16 a[188:189], v45 offset:49152\n"
"v_mfma_f32_16x16x32_bf16 a[100:103], a[252:255], a[196:199], a[100:103]\n"
"ds_read_b64_tr_b16 a[190:191], v45 offset:50176\n"
"v_mfma_f32_16x16x32_bf16 a[104:107], a[252:255], a[200:203], a[104:107]\n"
"v_mfma_f32_16x16x32_bf16 a[108:111], a[252:255], a[204:207], a[108:111]\n"
"v_mfma_f32_16x16x32_bf16 a[112:115], a[252:255], a[208:211], a[112:115]\n"
"v_mfma_f32_16x16x32_bf16 a[116:119], a[252:255], a[212:215], a[116:119]\n"
"s_add_u32 s44, s44, 2048\n"
"s_add_u32 s45, s45, s48\n"
"s_add_u32 s50, s50, 65536\n"
"s_and_b32 s50, s50, 131071\n"
"v_mfma_f32_16x16x32_bf16 a[120:123], a[252:255], a[216:219], a[120:123]\n"
"v_mfma_f32_16x16x32_bf16 a[124:127], a[252:255], a[220:223], a[124:127]\n"
"s_sub_u32 s46, s46, 1\n"
"s_cmp_lg_u32 s46, 0\n"
"s_waitcnt lgkmcnt(0)\n"
"s_cbranch_scc1 KL_s_%=\n"
"v_mfma_f32_16x16x32_bf16 v[128:131], a[160:163], a[128:131], v[128:131]\n"
"ds_read_b64_tr_b16 a[224:225], v42 offset:40960\n"
"v_mfma_f32_16x16x32_bf16 v[132:135], a[160:163], a[132:135], v[132:135]\n"
"ds_read_b64_tr_b16 a[226:227], v42 offset:41984\n"
"v_mfma_f32_16x16x32_bf16 v[136:139], a[160:163], a[136:139], v[136:139]\n"
"ds_read_b128 a[192:195], v41 offset:0\n"
"v_mfma_f32_16x16x32_bf16 v[140:143], a[160:163], a[140:143], v[140:143]\n"
"ds_read_b128 a[196:199], v41 offset:2048\n"
"v_mfma_f32_16x16x32_bf16 v[144:147], a[160:163], a[144:147], v[144:147]\n"
"ds_read_b128 a[200:203], v41 offset:4096\n"
"v_mfma_f32_16x16x32_bf16 v[148:151], a[160:163], a[148:151], v[148:151]\n"
"ds_read_b128 a[204:207], v41 offset:6144\n"
"v_mfma_f32_16x16x32_bf16 v[152:155], a[160:163], a[152:155], v[152:155]\n"
"ds_read_b128 a[208:211], v41 offset:8192\n"
"v_mfma_f32_16x16x32_bf16 v[156:159], a[160:163], a[156:159], v[156:159]\n"
"ds_read_b128 a[212:215], v41 offset:10240\n"
"v_mfma_f32_16x16x32_bf16 v[160:163], a[164:167], a[128:131], v[160:163]\n"
"ds_read_b128 a[216:219], v41 offset:12288\n"
"v_mfma_f32_16x16x32_bf16 v[164:167], a[164:167], a[132:135], v[164:167]\n"
"ds_read_b128 a[220:223], v41 offset:14336\n"
"v_mfma_f32_16x16x32_bf16 v[168:171], a[164:167], a[136:139], v[168:171]\n"
"ds_read_b64_tr_b16 a[228:229], v43 offset:40960\n"
"v_mfma_f32_16x16x32_bf16 v[172:175], a[164:167], a[140:143], v[172:175]\n"
"ds_read_b64_tr_b16 a[230:231], v43 offset:41984\n"
"v_mfma_f32_16x16x32_bf16 v[176:179], a[164:167], a[144:147], v[176:179]\n"
"ds_read_b64_tr_b16 a[232:233], v44 offset:40960\n"
"v_mfma_f32_16x16x32_bf16 v[180:183], a[164:167], a[148:151], v[180:183]\n"
"ds_read_b64_tr_b16 a[234:235], v44 offset:41984\n"
"v_mfma_f32_16x16x32_bf16 v[184:187], a[164:167], a[152:155], v[184:187]\n"
"ds_read_b64_tr_b16 a[236:237], v45 offset:40960\n"
"v_mfma_f32_16x16x32_bf16 v[188:191], a[164:167], a[156:159], v[188:191]\n"
"ds_read_b64_tr_b16 a[238:239], v45 offset:41984\n"
"v_mfma_f32_16x16x32_bf16 v[192:195], a[168:171], a[128:131], v[192:195]\n"
"ds_read_b64_tr_b16 a[240:241], v42 offset:57344\n"
"v_mfma_f32_16x16x32_bf16 v[196:199], a[168:171], a[132:135], v[196:199]\n"
"ds_read_b64_tr_b16 a[242:243], v42 offset:58368\n"
"v_mfma_f32_16x16x32_bf16 v[200:203], a[168:171], a[136:139], v[200:203]\n"
"ds_read_b64_tr_b16 a[244:245], v43 offset:57344\n"
"v_mfma_f32_16x16x32_bf16 v[204:207], a[168:171], a[140:143], v[204:207]\n"
"ds_read_b64_tr_b16 a[246:247], v43 offset:58368\n"
"v_mfma_f32_16x16x32_bf16 v[208:211], a[168:171], a[144:147], v[208:211]\n"
"ds_read_b64_tr_b16 a[248:249], v44 offset:57344\n"
"v_mfma_f32_16x16x32_bf16 v[212:215], a[168:171], a[148:151], v[212:215]\n"
"ds_read_b64_tr_b16 a[250:251], v44 offset:58368\n"
"v_mfma_f32_16x16x32_bf16 v[216:219], a[168:171], a[152:155], v[216:219]\n"
"ds_read_b64_tr_b16 a[252:253], v45 offset:57344\n"
"v_mfma_f32_16x16x32_bf16 v[220:223], a[168:171], a[156:159], v[220:223]\n"
"ds_read_b64_tr_b16 a[254:255], v45 offset:58368\n"
"v_mfma_f32_16x16x32_bf16 v[224:227], a[172:175], a[128:131], v[224:227]\n"
"v_mfma_f32_16x16x32_bf16 v[228:231], a[172:175], a[132:135], v[228:231]\n"
"v_mfma_f32_16x16x32_bf16 v[232:235], a[172:175], a[136:139], v[232:235]\n"
"v_mfma_f32_16x16x32_bf16 v[236:239], a[172:175], a[140:143], v[236:239]\n"
"s_add_u32 s51, s51, 65536\n"
"s_and_b32 s51, s51, 131071\n"
"v_mfma_f32_16x16x32_bf16 v[240:243], a[172:175], a[144:147], v[240:243]\n"
"v_mfma_f32_16x16x32_bf16 v[244:247], a[172:175], a[148:151], v[244:247]\n"
"s_waitcnt lgkmcnt(0)\n"
"v_mfma_f32_16x16x32_bf16 v[248:251], a[172:175], a[152:155], v[248:251]\n"
"v_mfma_f32_16x16x32_bf16 v[252:255], a[172:175], a[156:159], v[252:255]\n"
"v_mfma_f32_16x16x32_bf16 a[0:3], a[176:179], a[128:131], a[0:3]\n"
"v_mfma_f32_16x16x32_bf16 a[4:7], a[176:179], a[132:135], a[4:7]\n"
"v_mfma_f32_16x16x32_bf16 a[8:11], a[176:179], a[136:139], a[8:11]\n"
"v_mfma_f32_16x16x32_bf16 a[12:15], a[176:179], a[140:143], a[12:15]\n"
"v_mfma_f32_16x16x32_bf16 a[16:19], a[176:179], a[144:147], a[16:19]\n"
"v_mfma_f32_16x16x32_bf16 a[20:23], a[176:179], a[148:151], a[20:23]\n"
"v_mfma_f32_16x16x32_bf16 a[24:27], a[176:179], a[152:155], a[24:27]\n"
"v_mfma_f32_16x16x32_bf16 a[28:31], a[176:179], a[156:159], a[28:31]\n"
"v_mfma_f32_16x16x32_bf16 a[32:35], a[180:183], a[128:131], a[32:35]\n"
"v_mfma_f32_16x16x32_bf16 a[36:39], a[180:183], a[132:135], a[36:39]\n"
"v_mfma_f32_16x16x32_bf16 a[40:43], a[180:183], a[136:139], a[40:43]\n"
"v_mfma_f32_16x16x32_bf16 a[44:47], a[180:183], a[140:143], a[44:47]\n"
"v_mfma_f32_16x16x32_bf16 a[48:51], a[180:183], a[144:147], a[48:51]\n"
"v_mfma_f32_16x16x32_bf16 a[52:55], a[180:183], a[148:151], a[52:55]\n"
"v_mfma_f32_16x16x32_bf16 a[56:59], a[180:183], a[152:155], a[56:59]\n"
"v_mfma_f32_16x16x32_bf16 a[60:63], a[180:183], a[156:159], a[60:63]\n"
"v_mfma_f32_16x16x32_bf16 a[64:67], a[184:187], a[128:131], a[64:67]\n"
"v_mfma_f32_16x16x32_bf16 a[68:71], a[184:187], a[132:135], a[68:71]\n"
"v_mfma_f32_16x16x32_bf16 a[72:75], a[184:187], a[136:139], a[72:75]\n"
"v_mfma_f32_16x16x32_bf16 a[76:79], a[184:187], a[140:143], a[76:79]\n"
"v_mfma_f32_16x16x32_bf16 a[80:83], a[184:187], a[144:147], a[80:83]\n"
"v_mfma_f32_16x16x32_bf16 a[84:87], a[184:187], a[148:151], a[84:87]\n"
"v_mfma_f32_16x16x32_bf16 a[88:91], a[184:187], a[152:155], a[88:91]\n"
"v_mfma_f32_16x16x32_bf16 a[92:95], a[184:187], a[156:159], a[92:95]\n"
"v_mfma_f32_16x16x32_bf16 a[96:99], a[188:191], a[128:131], a[96:99]\n"
"v_mfma_f32_16x16x32_bf16 a[100:103], a[188:191], a[132:135], a[100:103]\n"
"v_mfma_f32_16x16x32_bf16 a[104:107], a[188:191], a[136:139], a[104:107]\n"
"v_mfma_f32_16x16x32_bf16 a[108:111], a[188:191], a[140:143], a[108:111]\n"
"v_mfma_f32_16x16x32_bf16 a[112:115], a[188:191], a[144:147], a[112:115]\n"
"v_mfma_f32_16x16x32_bf16 a[116:119], a[188:191], a[148:151], a[116:119]\n"
"v_mfma_f32_16x16x32_bf16 a[120:123], a[188:191], a[152:155], a[120:123]\n"
"v_mfma_f32_16x16x32_bf16 a[124:127], a[188:191], a[156:159], a[124:127]\n"
"v_mfma_f32_16x16x32_bf16 v[128:131], a[224:227], a[192:195], v[128:131]\n"
"v_mfma_f32_16x16x32_bf16 v[132:135], a[224:227], a[196:199], v[132:135]\n"
"v_mfma_f32_16x16x32_bf16 v[136:139], a[224:227], a[200:203], v[136:139]\n"
"v_mfma_f32_16x16x32_bf16 v[140:143], a[224:227], a[204:207], v[140:143]\n"
"v_mfma_f32_16x16x32_bf16 v[144:147], a[224:227], a[208:211], v[144:147]\n"
"v_mfma_f32_16x16x32_bf16 v[148:151], a[224:227], a[212:215], v[148:151]\n"
"v_mfma_f32_16x16x32_bf16 v[152:155], a[224:227], a[216:219], v[152:155]\n"
"v_mfma_f32_16x16x32_bf16 v[156:159], a[224:227], a[220:223], v[156:159]\n"
"v_mfma_f32_16x16x32_bf16 v[160:163], a[228:231], a[192:195], v[160:163]\n"
"v_mfma_f32_16x16x32_bf16 v[164:167], a[228:231], a[196:199], v[164:167]\n"
"v_mfma_f32_16x16x32_bf16 v[168:171], a[228:231], a[200:203], v[168:171]\n"
"v_add_u32 v40, s51, v46\n"
"v_mfma_f32_16x16x32_bf16 v[172:175], a[228:231], a[204:207], v[172:175]\n"
"v_mfma_f32_16x16x32_bf16 v[176:179], a[228:231], a[208:211], v[176:179]\n"
"v_mfma_f32_16x16x32_bf16 v[180:183], a[228:231], a[212:215], v[180:183]\n"
"v_mfma_f32_16x16x32_bf16 v[184:187], a[228:231], a[216:219], v[184:187]\n"
"v_add_u32 v41, s51, v47\n"
"v_mfma_f32_16x16x32_bf16 v[188:191], a[228:231], a[220:223], v[188:191]\n"
"v_mfma_f32_16x16x32_bf16 v[192:195], a[232:235], a[192:195], v[192:195]\n"
"v_mfma_f32_16x16x32_bf16 v[196:199], a[232:235], a[196:199], v[196:199]\n"
"v_mfma_f32_16x16x32_bf16 v[200:203], a[232:235], a[200:203], v[200:203]\n"
"v_add_u32 v42, s51, v48\n"
"v_mfma_f32_16x16x32_bf16 v[204:207], a[232:235], a[204:207], v[204:207]\n"
"v_mfma_f32_16x16x32_bf16 v[208:211], a[232:235], a[208:211], v[208:211]\n"
"v_mfma_f32_16x16x32_bf16 v[212:215], a[232:235], a[212:215], v[212:215]\n"
"v_mfma_f32_16x16x32_bf16 v[216:219], a[232:235], a[216:219], v[216:219]\n"
"v_add_u32 v43, s51, v49\n"
"v_mfma_f32_16x16x32_bf16 v[220:223], a[232:235], a[220:223], v[220:223]\n"
"v_mfma_f32_16x16x32_bf16 v[224:227], a[236:239], a[192:195], v[224:227]\n"
"v_mfma_f32_16x16x32_bf16 v[228:231], a[236:239], a[196:199], v[228:231]\n"
"v_mfma_f32_16x16x32_bf16 v[232:235], a[236:239], a[200:203], v[232:235]\n"
"v_add_u32 v44, s51, v50\n"
"v_mfma_f32_16x16x32_bf16 v[236:239], a[236:239], a[204:207], v[236:239]\n"
"v_mfma_f32_16x16x32_bf16 v[240:243], a[236:239], a[208:211], v[240:243]\n"
"v_mfma_f32_16x16x32_bf16 v[244:247], a[236:239], a[212:215], v[244:247]\n"
"v_mfma_f32_16x16x32_bf16 v[248:251], a[236:239], a[216:219], v[248:251]\n"
"v_add_u32 v45, s51, v51\n"
"v_mfma_f32_16x16x32_bf16 v[252:255], a[236:239], a[220:223], v[252:255]\n"
"v_mfma_f32_16x16x32_bf16 a[0:3], a[240:243], a[192:195], a[0:3]\n"
"v_mfma_f32_16x16x32_bf16 a[4:7], a[240:243], a[196:199], a[4:7]\n"
"s_waitcnt vmcnt(0)\n"
"s_barrier\n"
"v_mfma_f32_16x16x32_bf16 a[8:11], a[240:243], a[200:203], a[8:11]\n"
"ds_read_b64_tr_b16 a[160:161], v42 offset:32768\n"
"v_mfma_f32_16x16x32_bf16 a[12:15], a[240:243], a[204:207], a[12:15]\n"
"ds_read_b64_tr_b16 a[162:163], v42 offset:33792\n"
"v_mfma_f32_16x16x32_bf16 a[16:19], a[240:243], a[208:211], a[16:19]\n"
"ds_read_b128 a[128:131], v40 offset:0\n"
"v_mfma_f32_16x16x32_bf16 a[20:23], a[240:243], a[212:215], a[20:23]\n"
"ds_read_b128 a[132:135], v40 offset:2048\n"
"v_mfma_f32_16x16x32_bf16 a[24:27], a[240:243], a[216:219], a[24:27]\n"
"ds_read_b128 a[136:139], v40 offset:4096\n"
"v_mfma_f32_16x16x32_bf16 a[28:31], a[240:243], a[220:223], a[28:31]\n"
"ds_read_b128 a[140:143], v40 offset:6144\n"
"v_mfma_f32_16x16x32_bf16 a[32:35], a[244:247], a[192:195], a[32:35]\n"
"ds_read_b128 a[144:147], v40 offset:8192\n"
"v_mfma_f32_16x16x32_bf16 a[36:39], a[244:247], a[196:199], a[36:39]\n"
"ds_read_b128 a[148:151], v40 offset:10240\n"
"v_mfma_f32_16x16x32_bf16 a[40:43], a[244:247], a[200:203], a[40:43]\n"
"ds_read_b128 a[152:155], v40 offset:12288\n"
"v_mfma_f32_16x16x32_bf16 a[44:47], a[244:247], a[204:207], a[44:47]\n"
"ds_read_b128 a[156:159], v40 offset:14336\n"
"v_mfma_f32_16x16x32_bf16 a[48:51], a[244:247], a[208:211], a[48:51]\n"
"ds_read_b64_tr_b16 a[164:165], v43 offset:32768\n"
"v_mfma_f32_16x16x32_bf16 a[52:55], a[244:247], a[212:215], a[52:55]\n"
"ds_read_b64_tr_b16 a[166:167], v43 offset:33792\n"
"v_mfma_f32_16x16x32_bf16 a[56:59], a[244:247], a[216:219], a[56:59]\n"
"ds_read_b64_tr_b16 a[168:169], v44 offset:32768\n"
"v_mfma_f32_16x16x32_bf16 a[60:63], a[244:247], a[220:223], a[60:63]\n"
"ds_read_b64_tr_b16 a[170:171], v44 offset:33792\n"
"v_mfma_f32_16x16x32_bf16 a[64:67], a[248:251], a[192:195], a[64:67]\n"
"ds_read_b64_tr_b16 a[172:173], v45 offset:32768\n"
"v_mfma_f32_16x16x32_bf16 a[68:71], a[248:251], a[196:199], a[68:71]\n"
"ds_read_b64_tr_b16 a[174:175], v45 offset:33792\n"
"v_mfma_f32_16x16x32_bf16 a[72:75], a[248:251], a[200:203], a[72:75]\n"
"ds_read_b64_tr_b16 a[176:177], v42 offset:49152\n"
"v_mfma_f32_16x16x32_bf16 a[76:79], a[248:251], a[204:207], a[76:79]\n"
"ds_read_b64_tr_b16 a[178:179], v42 offset:50176\n"
"v_mfma_f32_16x16x32_bf16 a[80:83], a[248:251], a[208:211], a[80:83]\n"
"ds_read_b64_tr_b16 a[180:181], v43 offset:49152\n"
"v_mfma_f32_16x16x32_bf16 a[84:87], a[248:251], a[212:215], a[84:87]\n"
"ds_read_b64_tr_b16 a[182:183], v43 offset:50176\n"
"v_mfma_f32_16x16x32_bf16 a[88:91], a[248:251], a[216:219], a[88:91]\n"
"ds_read_b64_tr_b16 a[184:185], v44 offset:49152\n"
"v_mfma_f32_16x16x32_bf16 a[92:95], a[248:251], a[220:223], a[92:95]\n"
"ds_read_b64_tr_b16 a[186:187], v44 offset:50176\n"
"v_mfma_f32_16x16x32_bf16 a[96:99], a[252:255], a[192:195], a[96:99]\n"
"ds_read_b64_tr_b16 a[188:189], v45 offset:49152\n"
"v_mfma_f32_16x16x32_bf16 a[100:103], a[252:255], a[196:199], a[100:103]\n"
"ds_read_b64_tr_b16 a[190:191], v45 offset:50176\n"
"v_mfma_f32_16x16x32_bf16 a[104:107], a[252:255], a[200:203], a[104:107]\n"
"v_mfma_f32_16x16x32_bf16 a[108:111], a[252:255], a[204:207], a[108:111]\n"
"v_mfma_f32_16x16x32_bf16 a[112:115], a[252:255], a[208:211], a[112:115]\n"
"v_mfma_f32_16x16x32_bf16 a[116:119], a[252:255], a[212:215], a[116:119]\n"
"v_mfma_f32_16x16x32_bf16 a[120:123], a[252:255], a[216:219], a[120:123]\n"
"v_mfma_f32_16x16x32_bf16 a[124:127], a[252:255], a[220:223], a[124:127]\n"
"s_waitcnt lgkmcnt(0)\n"
"v_mfma_f32_16x16x32_bf16 v[128:131], a[160:163], a[128:131], v[128:131]\n"
"ds_read_b64_tr_b16 a[224:225], v42 offset:40960\n"
"v_mfma_f32_16x16x32_bf16 v[132:135], a[160:163], a[132:135], v[132:135]\n"
"ds_read_b64_tr_b16 a[226:227], v42 offset:41984\n"
"v_mfma_f32_16x16x32_bf16 v[136:139], a[160:163], a[136:139], v[136:139]\n"
"ds_read_b128 a[192:195], v41 offset:0\n"
"v_mfma_f32_16x16x32_bf16 v[140:143], a[160:163], a[140:143], v[140:143]\n"
"ds_read_b128 a[196:199], v41 offset:2048\n"
"v_mfma_f32_16x16x32_bf16 v[144:147], a[160:163], a[144:147], v[144:147]\n"
"ds_read_b128 a[200:203], v41 offset:4096\n"
"v_mfma_f32_16x16x32_bf16 v[148:151], a[160:163], a[148:151], v[148:151]\n"
"ds_read_b128 a[204:207], v41 offset:6144\n"
"v_mfma_f32_16x16x32_bf16 v[152:155], a[160:163], a[152:155], v[152:155]\n"
"ds_read_b128 a[208:211], v41 offset:8192\n"
"v_mfma_f32_16x16x32_bf16 v[156:159], a[160:163], a[156:159], v[156:159]\n"
"ds_read_b128 a[212:215], v41 offset:10240\n"
"v_mfma_f32_16x16x32_bf16 v[160:163], a[164:167], a[128:131], v[160:163]\n"
"ds_read_b128 a[216:219], v41 offset:12288\n"
"v_mfma_f32_16x16x32_bf16 v[164:167], a[164:167], a[132:135], v[164:167]\n"
"ds_read_b128 a[220:223], v41 offset:14336\n"
"v_mfma_f32_16x16x32_bf16 v[168:171], a[164:167], a[136:139], v[168:171]\n"
"ds_read_b64_tr_b16 a[228:229], v43 offset:40960\n"
"v_mfma_f32_16x16x32_bf16 v[172:175], a[164:167], a[140:143], v[172:175]\n"
"ds_read_b64_tr_b16 a[230:231], v43 offset:41984\n"
"v_mfma_f32_16x16x32_bf16 v[176:179], a[164:167], a[144:147], v[176:179]\n"
"ds_read_b64_tr_b16 a[232:233], v44 offset:40960\n"
"v_mfma_f32_16x16x32_bf16 v[180:183], a[164:167], a[148:151], v[180:183]\n"
"ds_read_b64_tr_b16 a[234:235], v44 offset:41984\n"
"v_mfma_f32_16x16x32_bf16 v[184:187], a[164:167], a[152:155], v[184:187]\n"
"ds_read_b64_tr_b16 a[236:237], v45 offset:40960\n"
"v_mfma_f32_16x16x32_bf16 v[188:191], a[164:167], a[156:159], v[188:191]\n"
"ds_read_b64_tr_b16 a[238:239], v45 offset:41984\n"
"v_mfma_f32_16x16x32_bf16 v[192:195], a[168:171], a[128:131], v[192:195]\n"
"ds_read_b64_tr_b16 a[240:241], v42 offset:57344\n"
"v_mfma_f32_16x16x32_bf16 v[196:199], a[168:171], a[132:135], v[196:199]\n"
"ds_read_b64_tr_b16 a[242:243], v42 offset:58368\n"
"v_mfma_f32_16x16x32_bf16 v[200:203], a[168:171], a[136:139], v[200:203]\n"
"ds_read_b64_tr_b16 a[244:245], v43 offset:57344\n"
"v_mfma_f32_16x16x32_bf16 v[204:207], a[168:171], a[140:143], v[204:207]\n"
"ds_read_b64_tr_b16 a[246:247], v43 offset:58368\n"
"v_mfma_f32_16x16x32_bf16 v[208:211], a[168:171], a[144:147], v[208:211]\n"
"ds_read_b64_tr_b16 a[248:249], v44 offset:57344\n"
"v_mfma_f32_16x16x32_bf16 v[212:215], a[168:171], a[148:151], v[212:215]\n"
"ds_read_b64_tr_b16 a[250:251], v44 offset:58368\n"
"v_mfma_f32_16x16x32_bf16 v[216:219], a[168:171], a[152:155], v[216:219]\n"
"ds_read_b64_tr_b16 a[252:253], v45 offset:57344\n"
"v_mfma_f32_16x16x32_bf16 v[220:223], a[168:171], a[156:159], v[220:223]\n"
"ds_read_b64_tr_b16 a[254:255], v45 offset:58368\n"
"v_mfma_f32_16x16x32_bf16 v[224:227], a[172:175], a[128:131], v[224:227]\n"
"v_mfma_f32_16x16x32_bf16 v[228:231], a[172:175], a[132:135], v[228:231]\n"
"v_mfma_f32_16x16x32_bf16 v[232:235], a[172:175], a[136:139], v[232:235]\n"
"v_mfma_f32_16x16x32_bf16 v[236:239], a[172:175], a[140:143], v[236:239]\n"
"s_add_u32 s51, s51, 65536\n"
"s_and_b32 s51, s51, 131071\n"
"v_mfma_f32_16x16x32_bf16 v[240:243], a[172:175], a[144:147], v[240:243]\n"
"v_mfma_f32_16x16x32_bf16 v[244:247], a[172:175], a[148:151], v[244:247]\n"
"s_waitcnt lgkmcnt(0)\n"
"v_mfma_f32_16x16x32_bf16 v[248:251], a[172:175], a[152:155], v[248:251]\n"
"v_mfma_f32_16x16x32_bf16 v[252:255], a[172:175], a[156:159], v[252:255]\n"
"v_mfma_f32_16x16x32_bf16 a[0:3], a[176:179], a[128:131], a[0:3]\n"
"v_mfma_f32_16x16x32_bf16 a[4:7], a[176:179], a[132:135], a[4:7]\n"
"v_mfma_f32_16x16x32_bf16 a[8:11], a[176:179], a[136:139], a[8:11]\n"
"v_mfma_f32_16x16x32_bf16 a[12:15], a[176:179], a[140:143], a[12:15]\n"
"v_mfma_f32_16x16x32_bf16 a[16:19], a[176:179], a[144:147], a[16:19]\n"
"v_mfma_f32_16x16x32_bf16 a[20:23], a[176:179], a[148:151], a[20:23]\n"
"v_mfma_f32_16x16x32_bf16 a[24:27], a[176:179], a[152:155], a[24:27]\n"
"v_mfma_f32_16x16x32_bf16 a[28:31], a[176:179], a[156:159], a[28:31]\n"
"v_mfma_f32_16x16x32_bf16 a[32:35], a[180:183], a[128:131], a[32:35]\n"
"v_mfma_f32_16x16x32_bf16 a[36:39], a[180:183], a[132:135], a[36:39]\n"
"v_mfma_f32_16x16x32_bf16 a[40:43], a[180:183], a[136:139], a[40:43]\n"
"v_mfma_f32_16x16x32_bf16 a[44:47], a[180:183], a[140:143], a[44:47]\n"
"v_mfma_f32_16x16x32_bf16 a[48:51], a[180:183], a[144:147], a[48:51]\n"
"v_mfma_f32_16x16x32_bf16 a[52:55], a[180:183], a[148:151], a[52:55]\n"
"v_mfma_f32_16x16x32_bf16 a[56:59], a[180:183], a[152:155], a[56:59]\n"
"v_mfma_f32_16x16x32_bf16 a[60:63], a[180:183], a[156:159], a[60:63]\n"
"v_mfma_f32_16x16x32_bf16 a[64:67], a[184:187], a[128:131], a[64:67]\n"
"v_mfma_f32_16x16x32_bf16 a[68:71], a[184:187], a[132:135], a[68:71]\n"
"v_mfma_f32_16x16x32_bf16 a[72:75], a[184:187], a[136:139], a[72:75]\n"
"v_mfma_f32_16x16x32_bf16 a[76:79], a[184:187], a[140:143], a[76:79]\n"
"v_mfma_f32_16x16x32_bf16 a[80:83], a[184:187], a[144:147], a[80:83]\n"
"v_mfma_f32_16x16x32_bf16 a[84:87], a[184:187], a[148:151], a[84:87]\n"
"v_mfma_f32_16x16x32_bf16 a[88:91], a[184:187], a[152:155], a[88:91]\n"
"v_mfma_f32_16x16x32_bf16 a[92:95], a[184:187], a[156:159], a[92:95]\n"
"v_mfma_f32_16x16x32_bf16 a[96:99], a[188:191], a[128:131], a[96:99]\n"
"v_mfma_f32_16x16x32_bf16 a[100:103], a[188:191], a[132:135], a[100:103]\n"
"v_mfma_f32_16x16x32_bf16 a[104:107], a[188:191], a[136:139], a[104:107]\n"
"v_mfma_f32_16x16x32_bf16 a[108:111], a[188:191], a[140:143], a[108:111]\n"
"v_mfma_f32_16x16x32_bf16 a[112:115], a[188:191], a[144:147], a[112:115]\n"
"v_mfma_f32_16x16x32_bf16 a[116:119], a[188:191], a[148:151], a[116:119]\n"
"v_mfma_f32_16x16x32_bf16 a[120:123], a[188:191], a[152:155], a[120:123]\n"
"v_mfma_f32_16x16x32_bf16 a[124:127], a[188:191], a[156:159], a[124:127]\n"
"v_mfma_f32_16x16x32_bf16 v[128:131], a[224:227], a[192:195], v[128:131]\n"
"v_mfma_f32_16x16x32_bf16 v[132:135], a[224:227], a[196:199], v[132:135]\n"
"v_mfma_f32_16x16x32_bf16 v[136:139], a[224:227], a[200:203], v[136:139]\n"
"v_mfma_f32_16x16x32_bf16 v[140:143], a[224:227], a[204:207], v[140:143]\n"
"v_mfma_f32_16x16x32_bf16 v[144:147], a[224:227], a[208:211], v[144:147]\n"
"v_mfma_f32_16x16x32_bf16 v[148:151], a[224:227], a[212:215], v[148:151]\n"
"v_mfma_f32_16x16x32_bf16 v[152:155], a[224:227], a[216:219], v[152:155]\n"
"v_mfma_f32_16x16x32_bf16 v[156:159], a[224:227], a[220:223], v[156:159]\n"
"v_mfma_f32_16x16x32_bf16 v[160:163], a[228:231], a[192:195], v[160:163]\n"
"v_mfma_f32_16x16x32_bf16 v[164:167], a[228:231], a[196:199], v[164:167]\n"
"v_mfma_f32_16x16x32_bf16 v[168:171], a[228:231], a[200:203], v[168:171]\n"
"v_mfma_f32_16x16x32_bf16 v[172:175], a[228:231], a[204:207], v[172:175]\n"
"v_mfma_f32_16x16x32_bf16 v[176:179], a[228:231], a[208:211], v[176:179]\n"
"v_mfma_f32_16x16x32_bf16 v[180:183], a[228:231], a[212:215], v[180:183]\n"
"v_mfma_f32_16x16x32_bf16 v[184:187], a[228:231], a[216:219], v[184:187]\n"
"v_mfma_f32_16x16x32_bf16 v[188:191], a[228:231], a[220:223], v[188:191]\n"
"v_mfma_f32_16x16x32_bf16 v[192:195], a[232:235], a[192:195], v[192:195]\n"
"v_mfma_f32_16x16x32_bf16 v[196:199], a[232:235], a[196:199], v[196:199]\n"
"v_mfma_f32_16x16x32_bf16 v[200:203], a[232:235], a[200:203], v[200:203]\n"
"v_mfma_f32_16x16x32_bf16 v[204:207], a[232:235], a[204:207], v[204:207]\n"
"v_mfma_f32_16x16x32_bf16 v[208:211], a[232:235], a[208:211], v[208:211]\n"
"v_mfma_f32_16x16x32_bf16 v[212:215], a[232:235], a[212:215], v[212:215]\n"
"v_mfma_f32_16x16x32_bf16 v[216:219], a[232:235], a[216:219], v[216:219]\n"
"v_mfma_f32_16x16x32_bf16 v[220:223], a[232:235], a[220:223], v[220:223]\n"
"v_mfma_f32_16x16x32_bf16 v[224:227], a[236:239], a[192:195], v[224:227]\n"
"v_mfma_f32_16x16x32_bf16 v[228:231], a[236:239], a[196:199], v[228:231]\n"
"v_mfma_f32_16x16x32_bf16 v[232:235], a[236:239], a[200:203], v[232:235]\n"
"v_mfma_f32_16x16x32_bf16 v[236:239], a[236:239], a[204:207], v[236:239]\n"
"v_mfma_f32_16x16x32_bf16 v[240:243], a[236:239], a[208:211], v[240:243]\n"
"v_mfma_f32_16x16x32_bf16 v[244:247], a[236:239], a[212:215], v[244:247]\n"
"v_mfma_f32_16x16x32_bf16 v[248:251], a[236:239], a[216:219], v[248:251]\n"
"v_mfma_f32_16x16x32_bf16 v[252:255], a[236:239], a[220:223], v[252:255]\n"
"v_mfma_f32_16x16x32_bf16 a[0:3], a[240:243], a[192:195], a[0:3]\n"
"v_mfma_f32_16x16x32_bf16 a[4:7], a[240:243], a[196:199], a[4:7]\n"
"v_mfma_f32_16x16x32_bf16 a[8:11], a[240:243], a[200:203], a[8:11]\n"
"v_mfma_f32_16x16x32_bf16 a[12:15], a[240:243], a[204:207], a[12:15]\n"
"v_mfma_f32_16x16x32_bf16 a[16:19], a[240:243], a[208:211], a[16:19]\n"
"v_mfma_f32_16x16x32_bf16 a[20:23], a[240:243], a[212:215], a[20:23]\n"
"v_mfma_f32_16x16x32_bf16 a[24:27], a[240:243], a[216:219], a[24:27]\n"
"v_mfma_f32_16x16x32_bf16 a[28:31], a[240:243], a[220:223], a[28:31]\n"
"v_mfma_f32_16x16x32_bf16 a[32:35], a[244:247], a[192:195], a[32:35]\n"
"v_mfma_f32_16x16x32_bf16 a[36:39], a[244:247], a[196:199], a[36:39]\n"
"v_mfma_f32_16x16x32_bf16 a[40:43], a[244:247], a[200:203], a[40:43]\n"
"v_mfma_f32_16x16x32_bf16 a[44:47], a[244:247], a[204:207], a[44:47]\n"
"v_mfma_f32_16x16x32_bf16 a[48:51], a[244:247], a[208:211], a[48:51]\n"
"v_mfma_f32_16x16x32_bf16 a[52:55], a[244:247], a[212:215], a[52:55]\n"
"v_mfma_f32_16x16x32_bf16 a[56:59], a[244:247], a[216:219], a[56:59]\n"
"v_mfma_f32_16x16x32_bf16 a[60:63], a[244:247], a[220:223], a[60:63]\n"
"v_mfma_f32_16x16x32_bf16 a[64:67], a[248:251], a[192:195], a[64:67]\n"
"v_mfma_f32_16x16x32_bf16 a[68:71], a[248:251], a[196:199], a[68:71]\n"
"v_mfma_f32_16x16x32_bf16 a[72:75], a[248:251], a[200:203], a[72:75]\n"
"v_mfma_f32_16x16x32_bf16 a[76:79], a[248:251], a[204:207], a[76:79]\n"
"v_mfma_f32_16x16x32_bf16 a[80:83], a[248:251], a[208:211], a[80:83]\n"
"v_mfma_f32_16x16x32_bf16 a[84:87], a[248:251], a[212:215], a[84:87]\n"
"v_mfma_f32_16x16x32_bf16 a[88:91], a[248:251], a[216:219], a[88:91]\n"
"v_mfma_f32_16x16x32_bf16 a[92:95], a[248:251], a[220:223], a[92:95]\n"
"v_mfma_f32_16x16x32_bf16 a[96:99], a[252:255], a[192:195], a[96:99]\n"
"v_mfma_f32_16x16x32_bf16 a[100:103], a[252:255], a[196:199], a[100:103]\n"
"v_mfma_f32_16x16x32_bf16 a[104:107], a[252:255], a[200:203], a[104:107]\n"
"v_mfma_f32_16x16x32_bf16 a[108:111], a[252:255], a[204:207], a[108:111]\n"
"v_mfma_f32_16x16x32_bf16 a[112:115], a[252:255], a[208:211], a[112:115]\n"
"v_mfma_f32_16x16x32_bf16 a[116:119], a[252:255], a[212:215], a[116:119]\n"
"v_mfma_f32_16x16x32_bf16 a[120:123], a[252:255], a[216:219], a[120:123]\n"
"v_mfma_f32_16x16x32_bf16 a[124:127], a[252:255], a[220:223], a[124:127]\n"
"s_barrier\n"
"s_add_u32 s54, %[o0], 0\n"
"v_cvt_pk_bf16_f32 v60, v128, v129\n"
"v_cvt_pk_bf16_f32 v61, v130, v131\n"
"v_cvt_pk_bf16_f32 v62, v160, v161\n"
"v_cvt_pk_bf16_f32 v63, v162, v163\n"
"s_nop 0\n"
"v_permlane16_swap_b32 v60, v62\n"
"v_permlane16_swap_b32 v61, v63\n"
"buffer_store_dwordx4 v[60:63], %[vst], s[56:59], s54 offen offset:0 sc1\n"
"v_cvt_pk_bf16_f32 v64, v192, v193\n"
"v_cvt_pk_bf16_f32 v65, v194, v195\n"
"v_cvt_pk_bf16_f32 v66, v224, v225\n"
"v_cvt_pk_bf16_f32 v67, v226, v227\n"
"s_nop 0\n"
"v_permlane16_swap_b32 v64, v66\n"
"v_permlane16_swap_b32 v65, v67\n"
"buffer_store_dwordx4 v[64:67], %[vst], s[56:59], s54 offen offset:1024 sc1\n"
"s_add_u32 s54, %[o0], 0\n"
"s_add_u32 s54, s54, %[o1]\n"
"v_accvgpr_read_b32 v52, a0\n"
"v_accvgpr_read_b32 v53, a1\n"
"v_accvgpr_read_b32 v54, a2\n"
"v_accvgpr_read_b32 v55, a3\n"
"v_cvt_pk_bf16_f32 v68, v52, v53\n"
"v_cvt_pk_bf16_f32 v69, v54, v55\n"
"v_accvgpr_read_b32 v56, a32\n"
"v_accvgpr_read_b32 v57, a33\n"
"v_accvgpr_read_b32 v58, a34\n"
"v_accvgpr_read_b32 v59, a35\n"
"v_cvt_pk_bf16_f32 v70, v56, v57\n"
"v_cvt_pk_bf16_f32 v71, v58, v59\n"
"s_nop 0\n"
"v_permlane16_swap_b32 v68, v70\n"
"v_permlane16_swap_b32 v69, v71\n"
"buffer_store_dwordx4 v[68:71], %[vst], s[56:59], s54 offen offset:0 sc1\n"
"v_accvgpr_read_b32 v52, a64\n"
"v_accvgpr_read_b32 v53, a65\n"
"v_accvgpr_read_b32 v54, a66\n"
"v_accvgpr_read_b32 v55, a67\n"
"v_cvt_pk_bf16_f32 v72, v52, v53\n"
"v_cvt_pk_bf16_f32 v73, v54, v55\n"
"v_accvgpr_read_b32 v56, a96\n"
"v_accvgpr_read_b32 v57, a97\n"
"v_accvgpr_read_b32 v58, a98\n"
"v_accvgpr_read_b32 v59, a99\n"
"v_cvt_pk_bf16_f32 v74, v56, v57\n"
"v_cvt_pk_bf16_f32 v75, v58, v59\n"
"s_nop 0\n"
"v_permlane16_swap_b32 v72, v74\n"
"v_permlane16_swap_b32 v73, v75\n"
"buffer_store_dwordx4 v[72:75], %[vst], s[56:59], s54 offen offset:1024 sc1\n"
"s_add_u32 s54, %[o0], 4096\n"
"v_cvt_pk_bf16_f32 v60, v132, v133\n"
"v_cvt_pk_bf16_f32 v61, v134, v135\n"
"v_cvt_pk_bf16_f32 v62, v164, v165\n"
"v_cvt_pk_bf16_f32 v63, v166, v167\n"
"s_nop 0\n"
"v_permlane16_swap_b32 v60, v62\n"
"v_permlane16_swap_b32 v61, v63\n"
"buffer_store_dwordx4 v[60:63], %[vst], s[56:59], s54 offen offset:0 sc1\n"
"v_cvt_pk_bf16_f32 v64, v196, v197\n"
"v_cvt_pk_bf16_f32 v65, v198, v199\n"
"v_cvt_pk_bf16_f32 v66, v228, v229\n"
"v_cvt_pk_bf16_f32 v67, v230, v231\n"
"s_nop 0\n"
"v_permlane16_swap_b32 v64, v66\n"
"v_permlane16_swap_b32 v65, v67\n"
"buffer_store_dwordx4 v[64:67], %[vst], s[56:59], s54 offen offset:1024 sc1\n"
"s_add_u32 s54, %[o0], 4096\n"
"s_add_u32 s54, s54, %[o1]\n"
"v_accvgpr_read_b32 v52, a4\n"
"v_accvgpr_read_b32 v53, a5\n"
"v_accvgpr_read_b32 v54, a6\n"
"v_accvgpr_read_b32 v55, a7\n"
"v_cvt_pk_bf16_f32 v68, v52, v53\n"
"v_cvt_pk_bf16_f32 v69, v54, v55\n"
"v_accvgpr_read_b32 v56, a36\n"
"v_accvgpr_read_b32 v57, a37\n"
"v_accvgpr_read_b32 v58, a38\n"
"v_accvgpr_read_b32 v59, a39\n"
"v_cvt_pk_bf16_f32 v70, v56, v57\n"
"v_cvt_pk_bf16_f32 v71, v58, v59\n"
"s_nop 0\n"
"v_permlane16_swap_b32 v68, v70\n"
"v_permlane16_swap_b32 v69, v71\n"
"buffer_store_dwordx4 v[68:71], %[vst], s[56:59], s54 offen offset:0 sc1\n"
"v_accvgpr_read_b32 v52, a68\n"
"v_accvgpr_read_b32 v53, a69\n"
"v_accvgpr_read_b32 v54, a70\n"
"v_accvgpr_read_b32 v55, a71\n"
"v_cvt_pk_bf16_f32 v72, v52, v53\n"
"v_cvt_pk_bf16_f32 v73, v54, v55\n"
"v_accvgpr_read_b32 v56, a100\n"
"v_accvgpr_read_b32 v57, a101\n"
"v_accvgpr_read_b32 v58, a102\n"
"v_accvgpr_read_b32 v59, a103\n"
"v_cvt_pk_bf16_f32 v74, v56, v57\n"
"v_cvt_pk_bf16_f32 v75, v58, v59\n"
"s_nop 0\n"
"v_permlane16_swap_b32 v72, v74\n"
"v_permlane16_swap_b32 v73, v75\n"
"buffer_store_dwordx4 v[72:75], %[vst], s[56:59], s54 offen offset:1024 sc1\n"
"s_add_u32 s54, %[o0], 262144\n"
"v_cvt_pk_bf16_f32 v60, v136, v137\n"
"v_cvt_pk_bf16_f32 v61, v138, v139\n"
"v_cvt_pk_bf16_f32 v62, v168, v169\n"
"v_cvt_pk_bf16_f32 v63, v170, v171\n"
"s_nop 0\n"
"v_permlane16_swap_b32 v60, v62\n"
"v_permlane16_swap_b32 v61, v63\n"
"buffer_store_dwordx4 v[60:63], %[vst], s[56:59], s54 offen offset:0 sc1\n"
"v_cvt_pk_bf16_f32 v64, v200, v201\n"
"v_cvt_pk_bf16_f32 v65, v202, v203\n"
"v_cvt_pk_bf16_f32 v66, v232, v233\n"
"v_cvt_pk_bf16_f32 v67, v234, v235\n"
"s_nop 0\n"
"v_permlane16_swap_b32 v64, v66\n"
"v_permlane16_swap_b32 v65, v67\n"
"buffer_store_dwordx4 v[64:67], %[vst], s[56:59], s54 offen offset:1024 sc1\n"
"s_add_u32 s54, %[o0], 262144\n"
"s_add_u32 s54, s54, %[o1]\n"
"v_accvgpr_read_b32 v52, a8\n"
"v_accvgpr_read_b32 v53, a9\n"
"v_accvgpr_read_b32 v54, a10\n"
"v_accvgpr_read_b32 v55, a11\n"
"v_cvt_pk_bf16_f32 v68, v52, v53\n"
"v_cvt_pk_bf16_f32 v69, v54, v55\n"
"v_accvgpr_read_b32 v56, a40\n"
"v_accvgpr_read_b32 v57, a41\n"
"v_accvgpr_read_b32 v58, a42\n"
"v_accvgpr_read_b32 v59, a43\n"
"v_cvt_pk_bf16_f32 v70, v56, v57\n"
"v_cvt_pk_bf16_f32 v71, v58, v59\n"
"s_nop 0\n"
"v_permlane16_swap_b32 v68, v70\n"
"v_permlane16_swap_b32 v69, v71\n"
"buffer_store_dwordx4 v[68:71], %[vst], s[56:59], s54 offen offset:0 sc1\n"
"v_accvgpr_read_b32 v52, a72\n"
"v_accvgpr_read_b32 v53, a73\n"
"v_accvgpr_read_b32 v54, a74\n"
"v_accvgpr_read_b32 v55, a75\n"
"v_cvt_pk_bf16_f32 v72, v52, v53\n"
"v_cvt_pk_bf16_f32 v73, v54, v55\n"
"v_accvgpr_read_b32 v56, a104\n"
"v_accvgpr_read_b32 v57, a105\n"
"v_accvgpr_read_b32 v58, a106\n"
"v_accvgpr_read_b32 v59, a107\n"
"v_cvt_pk_bf16_f32 v74, v56, v57\n"
"v_cvt_pk_bf16_f32 v75, v58, v59\n"
"s_nop 0\n"
"v_permlane16_swap_b32 v72, v74\n"
"v_permlane16_swap_b32 v73, v75\n"
"buffer_store_dwordx4 v[72:75], %[vst], s[56:59], s54 offen offset:1024 sc1\n"
"s_add_u32 s54, %[o0], 266240\n"
"v_cvt_pk_bf16_f32 v60, v140, v141\n"
"v_cvt_pk_bf16_f32 v61, v142, v143\n"
"v_cvt_pk_bf16_f32 v62, v172, v173\n"
"v_cvt_pk_bf16_f32 v63, v174, v175\n"
"s_nop 0\n"
"v_permlane16_swap_b32 v60, v62\n"
"v_permlane16_swap_b32 v61, v63\n"
"buffer_store_dwordx4 v[60:63], %[vst], s[56:59], s54 offen offset:0 sc1\n"
"v_cvt_pk_bf16_f32 v64, v204, v205\n"
"v_cvt_pk_bf16_f32 v65, v206, v207\n"
"v_cvt_pk_bf16_f32 v66, v236, v237\n"
"v_cvt_pk_bf16_f32 v67, v238, v239\n"
"s_nop 0\n"
"v_permlane16_swap_b32 v64, v66\n"
"v_permlane16_swap_b32 v65, v67\n"
"buffer_store_dwordx4 v[64:67], %[vst], s[56:59], s54 offen offset:1024 sc1\n"
"s_add_u32 s54, %[o0], 266240\n"
"s_add_u32 s54, s54, %[o1]\n"
"v_accvgpr_read_b32 v52, a12\n"
"v_accvgpr_read_b32 v53, a13\n"
"v_accvgpr_read_b32 v54, a14\n"
"v_accvgpr_read_b32 v55, a15\n"
"v_cvt_pk_bf16_f32 v68, v52, v53\n"
"v_cvt_pk_bf16_f32 v69, v54, v55\n"
"v_accvgpr_read_b32 v56, a44\n"
"v_accvgpr_read_b32 v57, a45\n"
"v_accvgpr_read_b32 v58, a46\n"
"v_accvgpr_read_b32 v59, a47\n"
"v_cvt_pk_bf16_f32 v70, v56, v57\n"
"v_cvt_pk_bf16_f32 v71, v58, v59\n"
"s_nop 0\n"
"v_permlane16_swap_b32 v68, v70\n"
"v_permlane16_swap_b32 v69, v71\n"
"buffer_store_dwordx4 v[68:71], %[vst], s[56:59], s54 offen offset:0 sc1\n"
"v_accvgpr_read_b32 v52, a76\n"
"v_accvgpr_read_b32 v53, a77\n"
"v_accvgpr_read_b32 v54, a78\n"
"v_accvgpr_read_b32 v55, a79\n"
"v_cvt_pk_bf16_f32 v72, v52, v53\n"
"v_cvt_pk_bf16_f32 v73, v54, v55\n"
"v_accvgpr_read_b32 v56, a108\n"
"v_accvgpr_read_b32 v57, a109\n"
"v_accvgpr_read_b32 v58, a110\n"
"v_accvgpr_read_b32 v59, a111\n"
"v_cvt_pk_bf16_f32 v74, v56, v57\n"
"v_cvt_pk_bf16_f32 v75, v58, v59\n"
"s_nop 0\n"
"v_permlane16_swap_b32 v72, v74\n"
"v_permlane16_swap_b32 v73, v75\n"
"buffer_store_dwordx4 v[72:75], %[vst], s[56:59], s54 offen offset:1024 sc1\n"
"s_add_u32 s54, %[o0], 524288\n"
"v_cvt_pk_bf16_f32 v60, v144, v145\n"
"v_cvt_pk_bf16_f32 v61, v146, v147\n"
"v_cvt_pk_bf16_f32 v62, v176, v177\n"
"v_cvt_pk_bf16_f32 v63, v178, v179\n"
"s_nop 0\n"
"v_permlane16_swap_b32 v60, v62\n"
"v_permlane16_swap_b32 v61, v63\n"
"buffer_store_dwordx4 v[60:63], %[vst], s[56:59], s54 offen offset:0 sc1\n"
"v_cvt_pk_bf16_f32 v64, v208, v209\n"
"v_cvt_pk_bf16_f32 v65, v210, v211\n"
"v_cvt_pk_bf16_f32 v66, v240, v241\n"
"v_cvt_pk_bf16_f32 v67, v242, v243\n"
"s_nop 0\n"
"v_permlane16_swap_b32 v64, v66\n"
"v_permlane16_swap_b32 v65, v67\n"
"buffer_store_dwordx4 v[64:67], %[vst], s[56:59], s54 offen offset:1024 sc1\n"
"s_add_u32 s54, %[o0], 524288\n"
"s_add_u32 s54, s54, %[o1]\n"
"v_accvgpr_read_b32 v52, a16\n"
"v_accvgpr_read_b32 v53, a17\n"
"v_accvgpr_read_b32 v54, a18\n"
"v_accvgpr_read_b32 v55, a19\n"
"v_cvt_pk_bf16_f32 v68, v52, v53\n"
"v_cvt_pk_bf16_f32 v69, v54, v55\n"
"v_accvgpr_read_b32 v56, a48\n"
"v_accvgpr_read_b32 v57, a49\n"
"v_accvgpr_read_b32 v58, a50\n"
"v_accvgpr_read_b32 v59, a51\n"
"v_cvt_pk_bf16_f32 v70, v56, v57\n"
"v_cvt_pk_bf16_f32 v71, v58, v59\n"
"s_nop 0\n"
"v_permlane16_swap_b32 v68, v70\n"
"v_permlane16_swap_b32 v69, v71\n"
"buffer_store_dwordx4 v[68:71], %[vst], s[56:59], s54 offen offset:0 sc1\n"
"v_accvgpr_read_b32 v52, a80\n"
"v_accvgpr_read_b32 v53, a81\n"
"v_accvgpr_read_b32 v54, a82\n"
"v_accvgpr_read_b32 v55, a83\n"
"v_cvt_pk_bf16_f32 v72, v52, v53\n"
"v_cvt_pk_bf16_f32 v73, v54, v55\n"
"v_accvgpr_read_b32 v56, a112\n"
"v_accvgpr_read_b32 v57, a113\n"
"v_accvgpr_read_b32 v58, a114\n"
"v_accvgpr_read_b32 v59, a115\n"
"v_cvt_pk_bf16_f32 v74, v56, v57\n"
"v_cvt_pk_bf16_f32 v75, v58, v59\n"
"s_nop 0\n"
"v_permlane16_swap_b32 v72, v74\n"
"v_permlane16_swap_b32 v73, v75\n"
"buffer_store_dwordx4 v[72:75], %[vst], s[56:59], s54 offen offset:1024 sc1\n"
"s_add_u32 s54, %[o0], 528384\n"
"v_cvt_pk_bf16_f32 v60, v148, v149\n"
"v_cvt_pk_bf16_f32 v61, v150, v151\n"
"v_cvt_pk_bf16_f32 v62, v180, v181\n"
"v_cvt_pk_bf16_f32 v63, v182, v183\n"
"s_nop 0\n"
"v_permlane16_swap_b32 v60, v62\n"
"v_permlane16_swap_b32 v61, v63\n"
"buffer_store_dwordx4 v[60:63], %[vst], s[56:59], s54 offen offset:0 sc1\n"
"v_cvt_pk_bf16_f32 v64, v212, v213\n"
"v_cvt_pk_bf16_f32 v65, v214, v215\n"
"v_cvt_pk_bf16_f32 v66, v244, v245\n"
"v_cvt_pk_bf16_f32 v67, v246, v247\n"
"s_nop 0\n"
"v_permlane16_swap_b32 v64, v66\n"
"v_permlane16_swap_b32 v65, v67\n"
"buffer_store_dwordx4 v[64:67], %[vst], s[56:59], s54 offen offset:1024 sc1\n"
"s_add_u32 s54, %[o0], 528384\n"
"s_add_u32 s54, s54, %[o1]\n"
"v_accvgpr_read_b32 v52, a20\n"
"v_accvgpr_read_b32 v53, a21\n"
"v_accvgpr_read_b32 v54, a22\n"
"v_accvgpr_read_b32 v55, a23\n"
"v_cvt_pk_bf16_f32 v68, v52, v53\n"
"v_cvt_pk_bf16_f32 v69, v54, v55\n"
"v_accvgpr_read_b32 v56, a52\n"
"v_accvgpr_read_b32 v57, a53\n"
"v_accvgpr_read_b32 v58, a54\n"
"v_accvgpr_read_b32 v59, a55\n"
"v_cvt_pk_bf16_f32 v70, v56, v57\n"
"v_cvt_pk_bf16_f32 v71, v58, v59\n"
"s_nop 0\n"
"v_permlane16_swap_b32 v68, v70\n"
"v_permlane16_swap_b32 v69, v71\n"
"buffer_store_dwordx4 v[68:71], %[vst], s[56:59], s54 offen offset:0 sc1\n"
"v_accvgpr_read_b32 v52, a84\n"
"v_accvgpr_read_b32 v53, a85\n"
"v_accvgpr_read_b32 v54, a86\n"
"v_accvgpr_read_b32 v55, a87\n"
"v_cvt_pk_bf16_f32 v72, v52, v53\n"
"v_cvt_pk_bf16_f32 v73, v54, v55\n"
"v_accvgpr_read_b32 v56, a116\n"
"v_accvgpr_read_b32 v57, a117\n"
"v_accvgpr_read_b32 v58, a118\n"
"v_accvgpr_read_b32 v59, a119\n"
"v_cvt_pk_bf16_f32 v74, v56, v57\n"
"v_cvt_pk_bf16_f32 v75, v58, v59\n"
"s_nop 0\n"
"v_permlane16_swap_b32 v72, v74\n"
"v_permlane16_swap_b32 v73, v75\n"
"buffer_store_dwordx4 v[72:75], %[vst], s[56:59], s54 offen offset:1024 sc1\n"
"s_add_u32 s54, %[o0], 786432\n"
"v_cvt_pk_bf16_f32 v60, v152, v153\n"
"v_cvt_pk_bf16_f32 v61, v154, v155\n"
"v_cvt_pk_bf16_f32 v62, v184, v185\n"
"v_cvt_pk_bf16_f32 v63, v186, v187\n"
"s_nop 0\n"
"v_permlane16_swap_b32 v60, v62\n"
"v_permlane16_swap_b32 v61, v63\n"
"buffer_store_dwordx4 v[60:63], %[vst], s[56:59], s54 offen offset:0 sc1\n"
"v_cvt_pk_bf16_f32 v64, v216, v217\n"
"v_cvt_pk_bf16_f32 v65, v218, v219\n"
"v_cvt_pk_bf16_f32 v66, v248, v249\n"
"v_cvt_pk_bf16_f32 v67, v250, v251\n"
"s_nop 0\n"
"v_permlane16_swap_b32 v64, v66\n"
"v_permlane16_swap_b32 v65, v67\n"
"buffer_store_dwordx4 v[64:67], %[vst], s[56:59], s54 offen offset:1024 sc1\n"
"s_add_u32 s54, %[o0], 786432\n"
"s_add_u32 s54, s54, %[o1]\n"
"v_accvgpr_read_b32 v52, a24\n"
"v_accvgpr_read_b32 v53, a25\n"
"v_accvgpr_read_b32 v54, a26\n"
"v_accvgpr_read_b32 v55, a27\n"
"v_cvt_pk_bf16_f32 v68, v52, v53\n"
"v_cvt_pk_bf16_f32 v69, v54, v55\n"
"v_accvgpr_read_b32 v56, a56\n"
"v_accvgpr_read_b32 v57, a57\n"
"v_accvgpr_read_b32 v58, a58\n"
"v_accvgpr_read_b32 v59, a59\n"
"v_cvt_pk_bf16_f32 v70, v56, v57\n"
"v_cvt_pk_bf16_f32 v71, v58, v59\n"
"s_nop 0\n"
"v_permlane16_swap_b32 v68, v70\n"
"v_permlane16_swap_b32 v69, v71\n"
"buffer_store_dwordx4 v[68:71], %[vst], s[56:59], s54 offen offset:0 sc1\n"
"v_accvgpr_read_b32 v52, a88\n"
"v_accvgpr_read_b32 v53, a89\n"
"v_accvgpr_read_b32 v54, a90\n"
"v_accvgpr_read_b32 v55, a91\n"
"v_cvt_pk_bf16_f32 v72, v52, v53\n"
"v_cvt_pk_bf16_f32 v73, v54, v55\n"
"v_accvgpr_read_b32 v56, a120\n"
"v_accvgpr_read_b32 v57, a121\n"
"v_accvgpr_read_b32 v58, a122\n"
"v_accvgpr_read_b32 v59, a123\n"
"v_cvt_pk_bf16_f32 v74, v56, v57\n"
"v_cvt_pk_bf16_f32 v75, v58, v59\n"
"s_nop 0\n"
"v_permlane16_swap_b32 v72, v74\n"
"v_permlane16_swap_b32 v73, v75\n"
"buffer_store_dwordx4 v[72:75], %[vst], s[56:59], s54 offen offset:1024 sc1\n"
"s_add_u32 s54, %[o0], 790528\n"
"v_cvt_pk_bf16_f32 v60, v156, v157\n"
"v_cvt_pk_bf16_f32 v61, v158, v159\n"
"v_cvt_pk_bf16_f32 v62, v188, v189\n"
"v_cvt_pk_bf16_f32 v63, v190, v191\n"
"s_nop 0\n"
"v_permlane16_swap_b32 v60, v62\n"
"v_permlane16_swap_b32 v61, v63\n"
"buffer_store_dwordx4 v[60:63], %[vst], s[56:59], s54 offen offset:0 sc1\n"
"v_cvt_pk_bf16_f32 v64, v220, v221\n"
"v_cvt_pk_bf16_f32 v65, v222, v223\n"
"v_cvt_pk_bf16_f32 v66, v252, v253\n"
"v_cvt_pk_bf16_f32 v67, v254, v255\n"
"s_nop 0\n"
"v_permlane16_swap_b32 v64, v66\n"
"v_permlane16_swap_b32 v65, v67\n"
"buffer_store_dwordx4 v[64:67], %[vst], s[56:59], s54 offen offset:1024 sc1\n"
"s_add_u32 s54, %[o0], 790528\n"
"s_add_u32 s54, s54, %[o1]\n"
"v_accvgpr_read_b32 v52, a28\n"
"v_accvgpr_read_b32 v53, a29\n"
"v_accvgpr_read_b32 v54, a30\n"
"v_accvgpr_read_b32 v55, a31\n"
"v_cvt_pk_bf16_f32 v68, v52, v53\n"
"v_cvt_pk_bf16_f32 v69, v54, v55\n"
"v_accvgpr_read_b32 v56, a60\n"
"v_accvgpr_read_b32 v57, a61\n"
"v_accvgpr_read_b32 v58, a62\n"
"v_accvgpr_read_b32 v59, a63\n"
"v_cvt_pk_bf16_f32 v70, v56, v57\n"
"v_cvt_pk_bf16_f32 v71, v58, v59\n"
"s_nop 0\n"
"v_permlane16_swap_b32 v68, v70\n"
"v_permlane16_swap_b32 v69, v71\n"
"buffer_store_dwordx4 v[68:71], %[vst], s[56:59], s54 offen offset:0 sc1\n"
"v_accvgpr_read_b32 v52, a92\n"
"v_accvgpr_read_b32 v53, a93\n"
"v_accvgpr_read_b32 v54, a94\n"
"v_accvgpr_read_b32 v55, a95\n"
"v_cvt_pk_bf16_f32 v72, v52, v53\n"
"v_cvt_pk_bf16_f32 v73, v54, v55\n"
"v_accvgpr_read_b32 v56, a124\n"
"v_accvgpr_read_b32 v57, a125\n"
"v_accvgpr_read_b32 v58, a126\n"
"v_accvgpr_read_b32 v59, a127\n"
"v_cvt_pk_bf16_f32 v74, v56, v57\n"
"v_cvt_pk_bf16_f32 v75, v58, v59\n"
"s_nop 0\n"
"v_permlane16_swap_b32 v72, v74\n"
"v_permlane16_swap_b32 v73, v75\n"
"buffer_store_dwordx4 v[72:75], %[vst], s[56:59], s54 offen offset:1024 sc1\n"
"END_%=:\n"
"s_waitcnt vmcnt(0)\n"
  :: [ws] "s"(ws), [kind] "s"(kind), [wave] "s"(wave), [aoff] "s"(aoff), [lda] "s"(lda), [bp] "s"(bp), [bn] "s"(bn), [ldb] "s"(ldb), [nk] "s"(nk), [o0] "s"(o0), [o1] "s"(o1),
     [va0] "v"(va0), [vb0] "v"(vb0), [vx0] "v"(vx0), [vw0] "v"(vw0), [vst] "v"(vst)
  : "v24","v25","v26","v27","v28","v29","v30","v31","v32","v33","v34","v35","v36","v37","v38","v39","v40","v41","v42","v43","v44","v45","v46","v47","v48","v49","v50","v51","v52","v53","v54","v55",
"v56","v57","v58","v59","v60","v61","v62","v63","v64","v65","v66","v67","v68","v69","v70","v71","v72","v73","v74","v75","v76","v77","v78","v79","v80","v81","v82","v83","v84","v85","v86","v87",
"v88","v89","v90","v91","v92","v93","v94","v95","v96","v97","v98","v99","v100","v101","v102","v103","v104","v105","v106","v107","v108","v109","v110","v111","v112","v113","v114","v115","v116","v117","v118","v119",
"v120","v121","v122","v123","v124","v125","v126","v127","v128","v129","v130","v131","v132","v133","v134","v135","v136","v137","v138","v139","v140","v141","v142","v143","v144","v145","v146","v147","v148","v149","v150","v151",
"v152","v153","v154","v155","v156","v157","v158","v159","v160","v161","v162","v163","v164","v165","v166","v167","v168","v169","v170","v171","v172","v173","v174","v175","v176","v177","v178","v179","v180","v181","v182","v183",
"v184","v185","v186","v187","v188","v189","v190","v191","v192","v193","v194","v195","v196","v197","v198","v199","v200","v201","v202","v203","v204","v205","v206","v207","v208","v209","v210","v211","v212","v213","v214","v215",
"v216","v217","v218","v219","v220","v221","v222","v223","v224","v225","v226","v227","v228","v229","v230","v231","v232","v233","v234","v235","v236","v237","v238","v239","v240","v241","v242","v243","v244","v245","v246","v247",
"v248","v249","v250","v251","v252","v253","v254","v255","a0","a1","a2","a3","a4","a5","a6","a7","a8","a9","a10","a11","a12","a13","a14","a15","a16","a17","a18","a19","a20","a21","a22","a23",
"a24","a25","a26","a27","a28","a29","a30","a31","a32","a33","a34","a35","a36","a37","a38","a39","a40","a41","a42","a43","a44","a45","a46","a47","a48","a49","a50","a51","a52","a53","a54","a55",
"a56","a57","a58","a59","a60","a61","a62","a63","a64","a65","a66","a67","a68","a69","a70","a71","a72","a73","a74","a75","a76","a77","a78","a79","a80","a81","a82","a83","a84","a85","a86","a87",
"a88","a89","a90","a91","a92","a93","a94","a95","a96","a97","a98","a99","a100","a101","a102","a103","a104","a105","a106","a107","a108","a109","a110","a111","a112","a113","a114","a115","a116","a117","a118","a119",
"a120","a121","a122","a123","a124","a125","a126","a127","a128","a129","a130","a131","a132","a133","a134","a135","a136","a137","a138","a139","a140","a141","a142","a143","a144","a145","a146","a147","a148","a149","a150","a151",
"a152","a153","a154","a155","a156","a157","a158","a159","a160","a161","a162","a163","a164","a165","a166","a167","a168","a169","a170","a171","a172","a173","a174","a175","a176","a177","a178","a179","a180","a181","a182","a183",
"a184","a185","a186","a187","a188","a189","a190","a191","a192","a193","a194","a195","a196","a197","a198","a199","a200","a201","a202","a203","a204","a205","a206","a207","a208","a209","a210","a211","a212","a213","a214","a215",
"a216","a217","a218","a219","a220","a221","a222","a223","a224","a225","a226","a227","a228","a229","a230","a231","a232","a233","a234","a235","a236","a237","a238","a239","a240","a241","a242","a243","a244","a245","a246","a247",
"a248","a249","a250","a251","a252","a253","a254","a255","s36","s37","s38","s39","s40","s41","s42","s43","s44","s45","s46","s47","s48","s49","s50","s51","s52","s53","s54","s55","s56","s57","s58","s59",
"s60","s61","s62","s63","s64","s65","s66","s67","vcc","scc","memory");
}

__device__ __forceinline__ void gemm_S(char* ws, int wave, const GP& P) {
  const unsigned kind = RFL(P.kind), aoff = RFL(P.aoff), lda = RFL(P.lda), bn = RFL(P.bn), ldb = RFL(P.ldb), nk = RFL(P.nk), o0 = RFL(P.o0), o1 = RFL(P.o1);
  const unsigned long long bp = ((unsigned long long)RFL((unsigned)(P.bp >> 32)) << 32) | RFL((unsigned)P.bp);
  const unsigned l = lane_id(), w = wave, wm = w >> 1, wn = w & 1;
  const unsigned i16 = l & 15, g = l >> 4, q = (l >> 2) & 3, p = l & 3, r = l / 8;
  const unsigned c0 = (l & 7) ^ g, va0 = 2048 + 2 * w * lda + (l >> 3) * 64 + (c0 >> 2) * 1024 + (c0 & 3) * 16;
  const unsigned vb0 = (32 * wn + r) * ldb + (((l % 8) ^ (2 * ((r >> 1) & 1))) * 16);
  const unsigned vx0 = wm * 8192 + i16 * 128 + ((g ^ (i16 >> 1)) * 16);
  const unsigned vw0 = (8 * g + q) * 128 + (((2 * wn) ^ ((q >> 1) | ((g & 1) << 1))) * 32) + 8 * p;
  const unsigned gp = ((g & 1) << 1) | (g >> 1);
  const unsigned vst = kind == 0 ? gp * 256 + i16 * 16 : kind == 1 ? l * 16 : i16 * 64 + gp * 16;
  asm volatile(
"s_mov_b64 s[36:37], %[ws]\n"
"s_and_b32 s37, s37, 0xffff\n"
"s_mov_b32 s38, 0xf000000\n"
"s_mov_b32 s39, 0x20000\n"
"s_mov_b64 s[40:41], %[bp]\n"
"s_and_b32 s41, s41, 0xffff\n"
"s_mov_b32 s42, %[bn]\n"
"s_mov_b32 s43, 0x20000\n"
"s_mov_b64 s[56:57], %[ws]\n"
"s_and_b32 s57, s57, 0xffff\n"
"s_mov_b32 s58, 0xf000000\n"
"s_mov_b32 s59, 0x20000\n"
"s_sub_u32 s44, %[aoff], 0x800\n"
"s_mov_b32 s45, 0\n"
"s_mov_b32 s47, %[nk]\n"
"s_lshl_b32 s48, %[ldb], 6\n"
"s_mov_b32 s49, %[wave]\n"
"s_mul_i32 s60, s49, 4096\n"
"s_add_u32 s61, s60, 4096\n"
"s_mul_i32 s62, s49, 4096\n"
"s_add_u32 s62, s62, 16384\n"
"s_add_u32 s63, s62, 4096\n"
"v_mov_b32 v24, %[va0]\n"
"v_add_u32 v26, %[lda], v24\n"
"v_xor_b32 v25, 0x400, v24\n"
"v_add_u32 v25, 0x200, v25\n"
"v_xor_b32 v27, 0x400, v26\n"
"v_add_u32 v27, 0x200, v27\n"
"v_subrev_u32 v25, 1024, v25\n"
"v_subrev_u32 v26, 2048, v26\n"
"v_subrev_u32 v27, 3072, v27\n"
"s_mul_i32 s55, %[ldb], 8\n"
"v_mov_b32 v32, %[vb0]\n"
"v_add_u32 v33, s55, v32\n"
"v_xor_b32 v33, 64, v33\n"
"v_add_u32 v34, s55, v33\n"
"v_xor_b32 v34, 64, v34\n"
"v_add_u32 v35, s55, v34\n"
"v_xor_b32 v35, 64, v35\n"
"v_subrev_u32 v33, 1024, v33\n"
"v_subrev_u32 v34, 2048, v34\n"
"v_subrev_u32 v35, 3072, v35\n"
"v_mov_b32 v46, %[vx0]\n"
"v_xor_b32 v47, 64, %[vx0]\n"
"v_xor_b32 v48, 0, %[vw0]\n"
"v_xor_b32 v49, 32, %[vw0]\n"
"s_cmp_lg_u32 %[kind], 1\n"
"s_cbranch_scc1 SW_%=\n"
"s_mov_b32 s50, 0\n"
"s_mov_b32 s51, 0\n"
"s_add_u32 m0, s50, s60\n"
"s_nop 0\n"
"buffer_load_dwordx4 v24, s[36:39], s44 offen offset:0 sc1 lds\n"
"buffer_load_dwordx4 v25, s[36:39], s44 offen offset:1024 sc1 lds\n"
"buffer_load_dwordx4 v26, s[36:39], s44 offen offset:2048 sc1 lds\n"
"buffer_load_dwordx4 v27, s[36:39], s44 offen offset:3072 sc1 lds\n"
"s_add_u32 m0, s50, s62\n"
"s_nop 0\n"
"buffer_load_dwordx4 v32, s[40:43], s45 offen offset:0 lds\n"
"buffer_load_dwordx4 v33, s[40:43], s45 offen offset:1024 lds\n"
"buffer_load_dwordx4 v34, s[40:43], s45 offen offset:2048 lds\n"
"buffer_load_dwordx4 v35, s[40:43], s45 offen offset:3072 lds\n"
"s_add_u32 s44, s44, 2048\n"
"s_add_u32 s45, s45, s48\n"
"s_add_u32 s50, s50, 32768\n"
"s_and_b32 s50, s50, 131071\n"
"v_mov_b64 v[128:129], 0\n"
"v_mov_b64 v[130:131], 0\n"
"v_mov_b64 v[136:137], 0\n"
"v_mov_b64 v[138:139], 0\n"
"v_mov_b64 v[144:145], 0\n"
"v_mov_b64 v[146:147], 0\n"
"v_mov_b64 v[152:153], 0\n"
"v_mov_b64 v[154:155], 0\n"
"v_mov_b64 v[160:161], 0\n"
"v_mov_b64 v[162:163], 0\n"
"v_mov_b64 v[168:169], 0\n"
"v_mov_b64 v[170:171], 0\n"
"v_mov_b64 v[176:177], 0\n"
"v_mov_b64 v[178:179], 0\n"
"v_mov_b64 v[184:185], 0\n"
"v_mov_b64 v[186:187], 0\n"
"v_mov_b64 v[132:133], 0\n"
"v_mov_b64 v[134:135], 0\n"
"v_mov_b64 v[140:141], 0\n"
"v_mov_b64 v[142:143], 0\n"
"v_mov_b64 v[148:149], 0\n"
"v_mov_b64 v[150:151], 0\n"
"v_mov_b64 v[156:157], 0\n"
"v_mov_b64 v[158:159], 0\n"
"v_mov_b64 v[164:165], 0\n"
"v_mov_b64 v[166:167], 0\n"
"v_mov_b64 v[172:173], 0\n"
"v_mov_b64 v[174:175], 0\n"
"v_mov_b64 v[180:181], 0\n"
"v_mov_b64 v[182:183], 0\n"
"v_mov_b64 v[188:189], 0\n"
"v_mov_b64 v[190:191], 0\n"
"v_add_u32 v40, s51, v46\n"
"v_add_u32 v41, s51, v47\n"
"v_add_u32 v42, s51, v48\n"
"v_add_u32 v43, s51, v49\n"
"s_add_u32 m0, s50, s60\n"
"s_nop 0\n"
"buffer_load_dwordx4 v24, s[36:39], s44 offen offset:0 sc1 lds\n"
"buffer_load_dwordx4 v25, s[36:39], s44 offen offset:1024 sc1 lds\n"
"buffer_load_dwordx4 v26, s[36:39], s44 offen offset:2048 sc1 lds\n"
"buffer_load_dwordx4 v27, s[36:39], s44 offen offset:3072 sc1 lds\n"
"s_add_u32 m0, s50, s62\n"
"s_nop 0\n"
"buffer_load_dwordx4 v32, s[40:43], s45 offen offset:0 lds\n"
"buffer_load_dwordx4 v33, s[40:43], s45 offen offset:1024 lds\n"
"buffer_load_dwordx4 v34, s[40:43], s45 offen offset:2048 lds\n"
"buffer_load_dwordx4 v35, s[40:43], s45 offen offset:3072 lds\n"
"s_add_u32 s44, s44, 2048\n"
"s_add_u32 s45, s45, s48\n"
"s_add_u32 s50, s50, 32768\n"
"s_and_b32 s50, s50, 131071\n"
"s_add_u32 m0, s50, s60\n"
"s_nop 0\n"
"buffer_load_dwordx4 v24, s[36:39], s44 offen offset:0 sc1 lds\n"
"buffer_load_dwordx4 v25, s[36:39], s44 offen offset:1024 sc1 lds\n"
"buffer_load_dwordx4 v26, s[36:39], s44 offen offset:2048 sc1 lds\n"
"buffer_load_dwordx4 v27, s[36:39], s44 offen offset:3072 sc1 lds\n"
"s_add_u32 m0, s50, s62\n"
"s_nop 0\n"
"buffer_load_dwordx4 v32, s[40:43], s45 offen offset:0 lds\n"
"buffer_load_dwordx4 v33, s[40:43], s45 offen offset:1024 lds\n"
"buffer_load_dwordx4 v34, s[40:43], s45 offen offset:2048 lds\n"
"buffer_load_dwordx4 v35, s[40:43], s45 offen offset:3072 lds\n"
"s_add_u32 s44, s44, 2048\n"
"s_add_u32 s45, s45, s48\n"
"s_add_u32 s50, s50, 32768\n"
"s_and_b32 s50, s50, 131071\n"
"s_add_u32 m0, s50, s60\n"
"s_nop 0\n"
"buffer_load_dwordx4 v24, s[36:39], s44 offen offset:0 sc1 lds\n"
"buffer_load_dwordx4 v25, s[36:39], s44 offen offset:1024 sc1 lds\n"
"buffer_load_dwordx4 v26, s[36:39], s44 offen offset:2048 sc1 lds\n"
"buffer_load_dwordx4 v27, s[36:39], s44 offen offset:3072 sc1 lds\n"
"s_add_u32 s44, s44, 2048\n"
"s_waitcnt vmcnt(20)\n"
"s_barrier\n"
"ds_read_b64_tr_b16 a[160:161], v42 offset:16384\n"
"ds_read_b64_tr_b16 a[162:163], v42 offset:16896\n"
"ds_read_b128 a[128:131], v40 offset:0\n"
"ds_read_b128 a[132:135], v40 offset:2048\n"
"ds_read_b128 a[136:139], v40 offset:4096\n"
"ds_read_b128 a[140:143], v40 offset:6144\n"
"ds_read_b64_tr_b16 a[164:165], v43 offset:16384\n"
"ds_read_b64_tr_b16 a[166:167], v43 offset:16896\n"
"ds_read_b64_tr_b16 a[168:169], v42 offset:24576\n"
"ds_read_b64_tr_b16 a[170:171], v42 offset:25088\n"
"ds_read_b64_tr_b16 a[172:173], v43 offset:24576\n"
"ds_read_b64_tr_b16 a[174:175], v43 offset:25088\n"
"s_sub_u32 s46, s47, 4\n"
"s_waitcnt lgkmcnt(0)\n"
"KL_u_%=:\n"
"v_mfma_f32_16x16x32_bf16 v[128:131], a[128:131], a[160:163], v[128:131]\n"
"ds_read_b64_tr_b16 a[224:225], v42 offset:20480\n"
"s_add_u32 m0, s50, s62\n"
"v_mfma_f32_16x16x32_bf16 v[132:135], a[132:135], a[160:163], v[132:135]\n"
"ds_read_b64_tr_b16 a[226:227], v42 offset:20992\n"
"buffer_load_dwordx4 v32, s[40:43], s45 offen offset:0 lds\n"
"v_mfma_f32_16x16x32_bf16 v[136:139], a[136:139], a[160:163], v[136:139]\n"
"ds_read_b128 a[192:195], v41 offset:0\n"
"v_mfma_f32_16x16x32_bf16 v[140:143], a[140:143], a[160:163], v[140:143]\n"
"ds_read_b128 a[196:199], v41 offset:2048\n"
"v_mfma_f32_16x16x32_bf16 v[144:147], a[128:131], a[164:167], v[144:147]\n"
"ds_read_b128 a[200:203], v41 offset:4096\n"
"v_mfma_f32_16x16x32_bf16 v[148:151], a[132:135], a[164:167], v[148:151]\n"
"ds_read_b128 a[204:207], v41 offset:6144\n"
"buffer_load_dwordx4 v33, s[40:43], s45 offen offset:1024 lds\n"
"v_mfma_f32_16x16x32_bf16 v[152:155], a[136:139], a[164:167], v[152:155]\n"
"ds_read_b64_tr_b16 a[228:229], v43 offset:20480\n"
"v_mfma_f32_16x16x32_bf16 v[156:159], a[140:143], a[164:167], v[156:159]\n"
"ds_read_b64_tr_b16 a[230:231], v43 offset:20992\n"
"v_mfma_f32_16x16x32_bf16 v[160:163], a[128:131], a[168:171], v[160:163]\n"
"ds_read_b64_tr_b16 a[232:233], v42 offset:28672\n"
"v_mfma_f32_16x16x32_bf16 v[164:167], a[132:135], a[168:171], v[164:167]\n"
"ds_read_b64_tr_b16 a[234:235], v42 offset:29184\n"
"buffer_load_dwordx4 v34, s[40:43], s45 offen offset:2048 lds\n"
"v_mfma_f32_16x16x32_bf16 v[168:171], a[136:139], a[168:171], v[168:171]\n"
"ds_read_b64_tr_b16 a[236:237], v43 offset:28672\n"
"v_mfma_f32_16x16x32_bf16 v[172:175], a[140:143], a[168:171], v[172:175]\n"
"ds_read_b64_tr_b16 a[238:239], v43 offset:29184\n"
"v_mfma_f32_16x16x32_bf16 v[176:179], a[128:131], a[172:175], v[176:179]\n"
"v_mfma_f32_16x16x32_bf16 v[180:183], a[132:135], a[172:175], v[180:183]\n"
"buffer_load_dwordx4 v35, s[40:43], s45 offen offset:3072 lds\n"
"v_mfma_f32_16x16x32_bf16 v[184:187], a[136:139], a[172:175], v[184:187]\n"
"s_add_u32 s45, s45, s48\n"
"s_add_u32 s50, s50, 32768\n"
"s_and_b32 s50, s50, 131071\n"
"s_add_u32 s51, s51, 32768\n"
"s_and_b32 s51, s51, 131071\n"
"v_mfma_f32_16x16x32_bf16 v[188:191], a[140:143], a[172:175], v[188:191]\n"
"s_waitcnt lgkmcnt(0) vmcnt(16)\n"
"s_barrier\n"
"v_mfma_f32_16x16x32_bf16 v[128:131], a[192:195], a[224:227], v[128:131]\n"
"v_add_u32 v40, s51, v46\n"
"v_add_u32 v41, s51, v47\n"
"s_add_u32 m0, s50, s60\n"
"v_mfma_f32_16x16x32_bf16 v[132:135], a[196:199], a[224:227], v[132:135]\n"
"v_add_u32 v42, s51, v48\n"
"v_add_u32 v43, s51, v49\n"
"buffer_load_dwordx4 v24, s[36:39], s44 offen offset:0 sc1 lds\n"
"v_mfma_f32_16x16x32_bf16 v[136:139], a[200:203], a[224:227], v[136:139]\n"
"ds_read_b64_tr_b16 a[160:161], v42 offset:16384\n"
"v_mfma_f32_16x16x32_bf16 v[140:143], a[204:207], a[224:227], v[140:143]\n"
"ds_read_b64_tr_b16 a[162:163], v42 offset:16896\n"
"v_mfma_f32_16x16x32_bf16 v[144:147], a[192:195], a[228:231], v[144:147]\n"
"ds_read_b128 a[128:131], v40 offset:0\n"
"v_mfma_f32_16x16x32_bf16 v[148:151], a[196:199], a[228:231], v[148:151]\n"
"ds_read_b128 a[132:135], v40 offset:2048\n"
"buffer_load_dwordx4 v25, s[36:39], s44 offen offset:1024 sc1 lds\n"
"v_mfma_f32_16x16x32_bf16 v[152:155], a[200:203], a[228:231], v[152:155]\n"
"ds_read_b128 a[136:139], v40 offset:4096\n"
"v_mfma_f32_16x16x32_bf16 v[156:159], a[204:207], a[228:231], v[156:159]\n"
"ds_read_b128 a[140:143], v40 offset:6144\n"
"v_mfma_f32_16x16x32_bf16 v[160:163], a[192:195], a[232:235], v[160:163]\n"
"ds_read_b64_tr_b16 a[164:165], v43 offset:16384\n"
"v_mfma_f32_16x16x32_bf16 v[164:167], a[196:199], a[232:235], v[164:167]\n"
"ds_read_b64_tr_b16 a[166:167], v43 offset:16896\n"
"buffer_load_dwordx4 v26, s[36:39], s44 offen offset:2048 sc1 lds\n"
"v_mfma_f32_16x16x32_bf16 v[168:171], a[200:203], a[232:235], v[168:171]\n"
"ds_read_b64_tr_b16 a[168:169], v42 offset:24576\n"
"v_mfma_f32_16x16x32_bf16 v[172:175], a[204:207], a[232:235], v[172:175]\n"
"ds_read_b64_tr_b16 a[170:171], v42 offset:25088\n"
"v_mfma_f32_16x16x32_bf16 v[176:179], a[192:195], a[236:239], v[176:179]\n"
"ds_read_b64_tr_b16 a[172:173], v43 offset:24576\n"
"v_mfma_f32_16x16x32_bf16 v[180:183], a[196:199], a[236:239], v[180:183]\n"
"ds_read_b64_tr_b16 a[174:175], v43 offset:25088\n"
"buffer_load_dwordx4 v27, s[36:39], s44 offen offset:3072 sc1 lds\n"
"v_mfma_f32_16x16x32_bf16 v[184:187], a[200:203], a[236:239], v[184:187]\n"
"s_add_u32 s44, s44, 2048\n"
"v_mfma_f32_16x16x32_bf16 v[188:191], a[204:207], a[236:239], v[188:191]\n"
"s_sub_u32 s46, s46, 1\n"
"s_cmp_lg_u32 s46, 0\n"
"s_waitcnt lgkmcnt(0)\n"
"s_cbranch_scc1 KL_u_%=\n"
"v_mfma_f32_16x16x32_bf16 v[128:131], a[128:131], a[160:163], v[128:131]\n"
"ds_read_b64_tr_b16 a[224:225], v42 offset:20480\n"
"s_add_u32 m0, s50, s62\n"
"v_mfma_f32_16x16x32_bf16 v[132:135], a[132:135], a[160:163], v[132:135]\n"
"ds_read_b64_tr_b16 a[226:227], v42 offset:20992\n"
"buffer_load_dwordx4 v32, s[40:43], s45 offen offset:0 lds\n"
"v_mfma_f32_16x16x32_bf16 v[136:139], a[136:139], a[160:163], v[136:139]\n"
"ds_read_b128 a[192:195], v41 offset:0\n"
"v_mfma_f32_16x16x32_bf16 v[140:143], a[140:143], a[160:163], v[140:143]\n"
"ds_read_b128 a[196:199], v41 offset:2048\n"
"v_mfma_f32_16x16x32_bf16 v[144:147], a[128:131], a[164:167], v[144:147]\n"
"ds_read_b128 a[200:203], v41 offset:4096\n"
"v_mfma_f32_16x16x32_bf16 v[148:151], a[132:135], a[164:167], v[148:151]\n"
"ds_read_b128 a[204:207], v41 offset:6144\n"
"buffer_load_dwordx4 v33, s[40:43], s45 offen offset:1024 lds\n"
"v_mfma_f32_16x16x32_bf16 v[152:155], a[136:139], a[164:167], v[152:155]\n"
"ds_read_b64_tr_b16 a[228:229], v43 offset:20480\n"
"v_mfma_f32_16x16x32_bf16 v[156:159], a[140:143], a[164:167], v[156:159]\n"
"ds_read_b64_tr_b16 a[230:231], v43 offset:20992\n"
"v_mfma_f32_16x16x32_bf16 v[160:163], a[128:131], a[168:171], v[160:163]\n"
"ds_read_b64_tr_b16 a[232:233], v42 offset:28672\n"
"v_mfma_f32_16x16x32_bf16 v[164:167], a[132:135], a[168:171], v[164:167]\n"
"ds_read_b64_tr_b16 a[234:235], v42 offset:29184\n"
"buffer_load_dwordx4 v34, s[40:43], s45 offen offset:2048 lds\n"
"v_mfma_f32_16x16x32_bf16 v[168:171], a[136:139], a[168:171], v[168:171]\n"
"ds_read_b64_tr_b16 a[236:237], v43 offset:28672\n"
"v_mfma_f32_16x16x32_bf16 v[172:175], a[140:143], a[168:171], v[172:175]\n"
"ds_read_b64_tr_b16 a[238:239], v43 offset:29184\n"
"v_mfma_f32_16x16x32_bf16 v[176:179], a[128:131], a[172:175], v[176:179]\n"
"v_mfma_f32_16x16x32_bf16 v[180:183], a[132:135], a[172:175], v[180:183]\n"
"buffer_load_dwordx4 v35, s[40:43], s45 offen offset:3072 lds\n"
"v_mfma_f32_16x16x32_bf16 v[184:187], a[136:139], a[172:175], v[184:187]\n"
"s_add_u32 s45, s45, s48\n"
"s_add_u32 s50, s50, 32768\n"
"s_and_b32 s50, s50, 131071\n"
"s_add_u32 s51, s51, 32768\n"
"s_and_b32 s51, s51, 131071\n"
"v_mfma_f32_16x16x32_bf16 v[188:191], a[140:143], a[172:175], v[188:191]\n"
"s_waitcnt lgkmcnt(0) vmcnt(16)\n"
"s_barrier\n"
"v_mfma_f32_16x16x32_bf16 v[128:131], a[192:195], a[224:227], v[128:131]\n"
"v_add_u32 v40, s51, v46\n"
"v_add_u32 v41, s51, v47\n"
"v_mfma_f32_16x16x32_bf16 v[132:135], a[196:199], a[224:227], v[132:135]\n"
"v_add_u32 v42, s51, v48\n"
"v_add_u32 v43, s51, v49\n"
"v_mfma_f32_16x16x32_bf16 v[136:139], a[200:203], a[224:227], v[136:139]\n"
"ds_read_b64_tr_b16 a[160:161], v42 offset:16384\n"
"v_mfma_f32_16x16x32_bf16 v[140:143], a[204:207], a[224:227], v[140:143]\n"
"ds_read_b64_tr_b16 a[162:163], v42 offset:16896\n"
"v_mfma_f32_16x16x32_bf16 v[144:147], a[192:195], a[228:231], v[144:147]\n"
"ds_read_b128 a[128:131], v40 offset:0\n"
"v_mfma_f32_16x16x32_bf16 v[148:151], a[196:199], a[228:231], v[148:151]\n"
"ds_read_b128 a[132:135], v40 offset:2048\n"
"v_mfma_f32_16x16x32_bf16 v[152:155], a[200:203], a[228:231], v[152:155]\n"
"ds_read_b128 a[136:139], v40 offset:4096\n"
"v_mfma_f32_16x16x32_bf16 v[156:159], a[204:207], a[228:231], v[156:159]\n"
"ds_read_b128 a[140:143], v40 offset:6144\n"
"v_mfma_f32_16x16x32_bf16 v[160:163], a[192:195], a[232:235], v[160:163]\n"
"ds_read_b64_tr_b16 a[164:165], v43 offset:16384\n"
"v_mfma_f32_16x16x32_bf16 v[164:167], a[196:199], a[232:235], v[164:167]\n"
"ds_read_b64_tr_b16 a[166:167], v43 offset:16896\n"
"v_mfma_f32_16x16x32_bf16 v[168:171], a[200:203], a[232:235], v[168:171]\n"
"ds_read_b64_tr_b16 a[168:169], v42 offset:24576\n"
"v_mfma_f32_16x16x32_bf16 v[172:175], a[204:207], a[232:235], v[172:175]\n"
"ds_read_b64_tr_b16 a[170:171], v42 offset:25088\n"
"v_mfma_f32_16x16x32_bf16 v[176:179], a[192:195], a[236:239], v[176:179]\n"
"ds_read_b64_tr_b16 a[172:173], v43 offset:24576\n"
"v_mfma_f32_16x16x32_bf16 v[180:183], a[196:199], a[236:239], v[180:183]\n"
"ds_read_b64_tr_b16 a[174:175], v43 offset:25088\n"
"v_mfma_f32_16x16x32_bf16 v[184:187], a[200:203], a[236:239], v[184:187]\n"
"v_mfma_f32_16x16x32_bf16 v[188:191], a[204:207], a[236:239], v[188:191]\n"
"s_waitcnt lgkmcnt(0)\n"
"v_mfma_f32_16x16x32_bf16 v[128:131], a[128:131], a[160:163], v[128:131]\n"
"ds_read_b64_tr_b16 a[224:225], v42 offset:20480\n"
"v_mfma_f32_16x16x32_bf16 v[132:135], a[132:135], a[160:163], v[132:135]\n"
"ds_read_b64_tr_b16 a[226:227], v42 offset:20992\n"
"v_mfma_f32_16x16x32_bf16 v[136:139], a[136:139], a[160:163], v[136:139]\n"
"ds_read_b128 a[192:195], v41 offset:0\n"
"v_mfma_f32_16x16x32_bf16 v[140:143], a[140:143], a[160:163], v[140:143]\n"
"ds_read_b128 a[196:199], v41 offset:2048\n"
"v_mfma_f32_16x16x32_bf16 v[144:147], a[128:131], a[164:167], v[144:147]\n"
"ds_read_b128 a[200:203], v41 offset:4096\n"
"v_mfma_f32_16x16x32_bf16 v[148:151], a[132:135], a[164:167], v[148:151]\n"
"ds_read_b128 a[204:207], v41 offset:6144\n"
"v_mfma_f32_16x16x32_bf16 v[152:155], a[136:139], a[164:167], v[152:155]\n"
"ds_read_b64_tr_b16 a[228:229], v43 offset:20480\n"
"v_mfma_f32_16x16x32_bf16 v[156:159], a[140:143], a[164:167], v[156:159]\n"
"ds_read_b64_tr_b16 a[230:231], v43 offset:20992\n"
"v_mfma_f32_16x16x32_bf16 v[160:163], a[128:131], a[168:171], v[160:163]\n"
"ds_read_b64_tr_b16 a[232:233], v42 offset:28672\n"
"v_mfma_f32_16x16x32_bf16 v[164:167], a[132:135], a[168:171], v[164:167]\n"
"ds_read_b64_tr_b16 a[234:235], v42 offset:29184\n"
"v_mfma_f32_16x16x32_bf16 v[168:171], a[136:139], a[168:171], v[168:171]\n"
"ds_read_b64_tr_b16 a[236:237], v43 offset:28672\n"
"v_mfma_f32_16x16x32_bf16 v[172:175], a[140:143], a[168:171], v[172:175]\n"
"ds_read_b64_tr_b16 a[238:239], v43 offset:29184\n"
"v_mfma_f32_16x16x32_bf16 v[176:179], a[128:131], a[172:175], v[176:179]\n"
"v_mfma_f32_16x16x32_bf16 v[180:183], a[132:135], a[172:175], v[180:183]\n"
"v_mfma_f32_16x16x32_bf16 v[184:187], a[136:139], a[172:175], v[184:187]\n"
"s_add_u32 s51, s51, 32768\n"
"s_and_b32 s51, s51, 131071\n"
"v_mfma_f32_16x16x32_bf16 v[188:191], a[140:143], a[172:175], v[188:191]\n"
"s_waitcnt lgkmcnt(0) vmcnt(8)\n"
"s_barrier\n"
"v_mfma_f32_16x16x32_bf16 v[128:131], a[192:195], a[224:227], v[128:131]\n"
"v_add_u32 v40, s51, v46\n"
"v_add_u32 v41, s51, v47\n"
"v_mfma_f32_16x16x32_bf16 v[132:135], a[196:199], a[224:227], v[132:135]\n"
"v_add_u32 v42, s51, v48\n"
"v_add_u32 v43, s51, v49\n"
"v_mfma_f32_16x16x32_bf16 v[136:139], a[200:203], a[224:227], v[136:139]\n"
"ds_read_b64_tr_b16 a[160:161], v42 offset:16384\n"
"v_mfma_f32_16x16x32_bf16 v[140:143], a[204:207], a[224:227], v[140:143]\n"
"ds_read_b64_tr_b16 a[162:163], v42 offset:16896\n"
"v_mfma_f32_16x16x32_bf16 v[144:147], a[192:195], a[228:231], v[144:147]\n"
"ds_read_b128 a[128:131], v40 offset:0\n"
"v_mfma_f32_16x16x32_bf16 v[148:151], a[196:199], a[228:231], v[148:151]\n"
"ds_read_b128 a[132:135], v40 offset:2048\n"
"v_mfma_f32_16x16x32_bf16 v[152:155], a[200:203], a[228:231], v[152:155]\n"
"ds_read_b128 a[136:139], v40 offset:4096\n"
"v_mfma_f32_16x16x32_bf16 v[156:159], a[204:207], a[228:231], v[156:159]\n"
"ds_read_b128 a[140:143], v40 offset:6144\n"
"v_mfma_f32_16x16x32_bf16 v[160:163], a[192:195], a[232:235], v[160:163]\n"
"ds_read_b64_tr_b16 a[164:165], v43 offset:16384\n"
"v_mfma_f32_16x16x32_bf16 v[164:167], a[196:199], a[232:235], v[164:167]\n"
"ds_read_b64_tr_b16 a[166:167], v43 offset:16896\n"
"v_mfma_f32_16x16x32_bf16 v[168:171], a[200:203], a[232:235], v[168:171]\n"
"ds_read_b64_tr_b16 a[168:169], v42 offset:24576\n"
"v_mfma_f32_16x16x32_bf16 v[172:175], a[204:207], a[232:235], v[172:175]\n"
"ds_read_b64_tr_b16 a[170:171], v42 offset:25088\n"
"v_mfma_f32_16x16x32_bf16 v[176:179], a[192:195], a[236:239], v[176:179]\n"
"ds_read_b64_tr_b16 a[172:173], v43 offset:24576\n"
"v_mfma_f32_16x16x32_bf16 v[180:183], a[196:199], a[236:239], v[180:183]\n"
"ds_read_b64_tr_b16 a[174:175], v43 offset:25088\n"
"v_mfma_f32_16x16x32_bf16 v[184:187], a[200:203], a[236:239], v[184:187]\n"
"v_mfma_f32_16x16x32_bf16 v[188:191], a[204:207], a[236:239], v[188:191]\n"
"s_waitcnt lgkmcnt(0)\n"
"v_mfma_f32_16x16x32_bf16 v[128:131], a[128:131], a[160:163], v[128:131]\n"
"ds_read_b64_tr_b16 a[224:225], v42 offset:20480\n"
"v_mfma_f32_16x16x32_bf16 v[132:135], a[132:135], a[160:163], v[132:135]\n"
"ds_read_b64_tr_b16 a[226:227], v42 offset:20992\n"
"v_mfma_f32_16x16x32_bf16 v[136:139], a[136:139], a[160:163], v[136:139]\n"
"ds_read_b128 a[192:195], v41 offset:0\n"
"v_mfma_f32_16x16x32_bf16 v[140:143], a[140:143], a[160:163], v[140:143]\n"
"ds_read_b128 a[196:199], v41 offset:2048\n"
"v_mfma_f32_16x16x32_bf16 v[144:147], a[128:131], a[164:167], v[144:147]\n"
"ds_read_b128 a[200:203], v41 offset:4096\n"
"v_mfma_f32_16x16x32_bf16 v[148:151], a[132:135], a[164:167], v[148:151]\n"
"ds_read_b128 a[204:207], v41 offset:6144\n"
"v_mfma_f32_16x16x32_bf16 v[152:155], a[136:139], a[164:167], v[152:155]\n"
"ds_read_b64_tr_b16 a[228:229], v43 offset:20480\n"
"v_mfma_f32_16x16x32_bf16 v[156:159], a[140:143], a[164:167], v[156:159]\n"
"ds_read_b64_tr_b16 a[230:231], v43 offset:20992\n"
"v_mfma_f32_16x16x32_bf16 v[160:163], a[128:131], a[168:171], v[160:163]\n"
"ds_read_b64_tr_b16 a[232:233], v42 offset:28672\n"
"v_mfma_f32_16x16x32_bf16 v[164:167], a[132:135], a[168:171], v[164:167]\n"
"ds_read_b64_tr_b16 a[234:235], v42 offset:29184\n"
"v_mfma_f32_16x16x32_bf16 v[168:171], a[136:139], a[168:171], v[168:171]\n"
"ds_read_b64_tr_b16 a[236:237], v43 offset:28672\n"
"v_mfma_f32_16x16x32_bf16 v[172:175], a[140:143], a[168:171], v[172:175]\n"
"ds_read_b64_tr_b16 a[238:239], v43 offset:29184\n"
"v_mfma_f32_16x16x32_bf16 v[176:179], a[128:131], a[172:175], v[176:179]\n"
"v_mfma_f32_16x16x32_bf16 v[180:183], a[132:135], a[172:175], v[180:183]\n"
"v_mfma_f32_16x16x32_bf16 v[184:187], a[136:139], a[172:175], v[184:187]\n"
"s_add_u32 s51, s51, 32768\n"
"s_and_b32 s51, s51, 131071\n"
"v_mfma_f32_16x16x32_bf16 v[188:191], a[140:143], a[172:175], v[188:191]\n"
"s_waitcnt lgkmcnt(0) vmcnt(0)\n"
"s_barrier\n"
"v_mfma_f32_16x16x32_bf16 v[128:131], a[192:195], a[224:227], v[128:131]\n"
"v_add_u32 v40, s51, v46\n"
"v_add_u32 v41, s51, v47\n"
"v_mfma_f32_16x16x32_bf16 v[132:135], a[196:199], a[224:227], v[132:135]\n"
"v_add_u32 v42, s51, v48\n"
"v_add_u32 v43, s51, v49\n"
"v_mfma_f32_16x16x32_bf16 v[136:139], a[200:203], a[224:227], v[136:139]\n"
"ds_read_b64_tr_b16 a[160:161], v42 offset:16384\n"
"v_mfma_f32_16x16x32_bf16 v[140:143], a[204:207], a[224:227], v[140:143]\n"
"ds_read_b64_tr_b16 a[162:163], v42 offset:16896\n"
"v_mfma_f32_16x16x32_bf16 v[144:147], a[192:195], a[228:231], v[144:147]\n"
"ds_read_b128 a[128:131], v40 offset:0\n"
"v_mfma_f32_16x16x32_bf16 v[148:151], a[196:199], a[228:231], v[148:151]\n"
"ds_read_b128 a[132:135], v40 offset:2048\n"
"v_mfma_f32_16x16x32_bf16 v[152:155], a[200:203], a[228:231], v[152:155]\n"
"ds_read_b128 a[136:139], v40 offset:4096\n"
"v_mfma_f32_16x16x32_bf16 v[156:159], a[204:207], a[228:231], v[156:159]\n"
"ds_read_b128 a[140:143], v40 offset:6144\n"
"v_mfma_f32_16x16x32_bf16 v[160:163], a[192:195], a[232:235], v[160:163]\n"
"ds_read_b64_tr_b16 a[164:165], v43 offset:16384\n"
"v_mfma_f32_16x16x32_bf16 v[164:167], a[196:199], a[232:235], v[164:167]\n"
"ds_read_b64_tr_b16 a[166:167], v43 offset:16896\n"
"v_mfma_f32_16x16x32_bf16 v[168:171], a[200:203], a[232:235], v[168:171]\n"
"ds_read_b64_tr_b16 a[168:169], v42 offset:24576\n"
"v_mfma_f32_16x16x32_bf16 v[172:175], a[204:207], a[232:235], v[172:175]\n"
"ds_read_b64_tr_b16 a[170:171], v42 offset:25088\n"
"v_mfma_f32_16x16x32_bf16 v[176:179], a[192:195], a[236:239], v[176:179]\n"
"ds_read_b64_tr_b16 a[172:173], v43 offset:24576\n"
"v_mfma_f32_16x16x32_bf16 v[180:183], a[196:199], a[236:239], v[180:183]\n"
"ds_read_b64_tr_b16 a[174:175], v43 offset:25088\n"
"v_mfma_f32_16x16x32_bf16 v[184:187], a[200:203], a[236:239], v[184:187]\n"
"v_mfma_f32_16x16x32_bf16 v[188:191], a[204:207], a[236:239], v[188:191]\n"
"s_waitcnt lgkmcnt(0)\n"
"v_mfma_f32_16x16x32_bf16 v[128:131], a[128:131], a[160:163], v[128:131]\n"
"ds_read_b64_tr_b16 a[224:225], v42 offset:20480\n"
"v_mfma_f32_16x16x32_bf16 v[132:135], a[132:135], a[160:163], v[132:135]\n"
"ds_read_b64_tr_b16 a[226:227], v42 offset:20992\n"
"v_mfma_f32_16x16x32_bf16 v[136:139], a[136:139], a[160:163], v[136:139]\n"
"ds_read_b128 a[192:195], v41 offset:0\n"
"v_mfma_f32_16x16x32_bf16 v[140:143], a[140:143], a[160:163], v[140:143]\n"
"ds_read_b128 a[196:199], v41 offset:2048\n"
"v_mfma_f32_16x16x32_bf16 v[144:147], a[128:131], a[164:167], v[144:147]\n"
"ds_read_b128 a[200:203], v41 offset:4096\n"
"v_mfma_f32_16x16x32_bf16 v[148:151], a[132:135], a[164:167], v[148:151]\n"
"ds_read_b128 a[204:207], v41 offset:6144\n"
"v_mfma_f32_16x16x32_bf16 v[152:155], a[136:139], a[164:167], v[152:155]\n"
"ds_read_b64_tr_b16 a[228:229], v43 offset:20480\n"
"v_mfma_f32_16x16x32_bf16 v[156:159], a[140:143], a[164:167], v[156:159]\n"
"ds_read_b64_tr_b16 a[230:231], v43 offset:20992\n"
"v_mfma_f32_16x16x32_bf16 v[160:163], a[128:131], a[168:171], v[160:163]\n"
"ds_read_b64_tr_b16 a[232:233], v42 offset:28672\n"
"v_mfma_f32_16x16x32_bf16 v[164:167], a[132:135], a[168:171], v[164:167]\n"
"ds_read_b64_tr_b16 a[234:235], v42 offset:29184\n"
"v_mfma_f32_16x16x32_bf16 v[168:171], a[136:139], a[168:171], v[168:171]\n"
"ds_read_b64_tr_b16 a[236:237], v43 offset:28672\n"
"v_mfma_f32_16x16x32_bf16 v[172:175], a[140:143], a[168:171], v[172:175]\n"
"ds_read_b64_tr_b16 a[238:239], v43 offset:29184\n"
"v_mfma_f32_16x16x32_bf16 v[176:179], a[128:131], a[172:175], v[176:179]\n"
"v_mfma_f32_16x16x32_bf16 v[180:183], a[132:135], a[172:175], v[180:183]\n"
"v_mfma_f32_16x16x32_bf16 v[184:187], a[136:139], a[172:175], v[184:187]\n"
"s_add_u32 s51, s51, 32768\n"
"s_and_b32 s51, s51, 131071\n"
"v_mfma_f32_16x16x32_bf16 v[188:191], a[140:143], a[172:175], v[188:191]\n"
"s_waitcnt lgkmcnt(0)\n"
"v_mfma_f32_16x16x32_bf16 v[128:131], a[192:195], a[224:227], v[128:131]\n"
"v_mfma_f32_16x16x32_bf16 v[132:135], a[196:199], a[224:227], v[132:135]\n"
"v_mfma_f32_16x16x32_bf16 v[136:139], a[200:203], a[224:227], v[136:139]\n"
"v_mfma_f32_16x16x32_bf16 v[140:143], a[204:207], a[224:227], v[140:143]\n"
"v_mfma_f32_16x16x32_bf16 v[144:147], a[192:195], a[228:231], v[144:147]\n"
"v_mfma_f32_16x16x32_bf16 v[148:151], a[196:199], a[228:231], v[148:151]\n"
"v_mfma_f32_16x16x32_bf16 v[152:155], a[200:203], a[228:231], v[152:155]\n"
"v_mfma_f32_16x16x32_bf16 v[156:159], a[204:207], a[228:231], v[156:159]\n"
"v_mfma_f32_16x16x32_bf16 v[160:163], a[192:195], a[232:235], v[160:163]\n"
"v_mfma_f32_16x16x32_bf16 v[164:167], a[196:199], a[232:235], v[164:167]\n"
"v_mfma_f32_16x16x32_bf16 v[168:171], a[200:203], a[232:235], v[168:171]\n"
"v_mfma_f32_16x16x32_bf16 v[172:175], a[204:207], a[232:235], v[172:175]\n"
"v_mfma_f32_16x16x32_bf16 v[176:179], a[192:195], a[236:239], v[176:179]\n"
"v_mfma_f32_16x16x32_bf16 v[180:183], a[196:199], a[236:239], v[180:183]\n"
"v_mfma_f32_16x16x32_bf16 v[184:187], a[200:203], a[236:239], v[184:187]\n"
"v_mfma_f32_16x16x32_bf16 v[188:191], a[204:207], a[236:239], v[188:191]\n"
"s_barrier\n"
"s_add_u32 s54, %[o0], 0\n"
"v_cvt_pk_bf16_f32 v60, v128, v129\n"
"v_cvt_pk_bf16_f32 v61, v130, v131\n"
"v_cvt_pk_bf16_f32 v62, v132, v133\n"
"v_cvt_pk_bf16_f32 v63, v134, v135\n"
"buffer_store_dwordx4 v[60:63], %[vst], s[56:59], s54 offen offset:0 sc1\n"
"v_cvt_pk_bf16_f32 v64, v144, v145\n"
"v_cvt_pk_bf16_f32 v65, v146, v147\n"
"v_cvt_pk_bf16_f32 v66, v148, v149\n"
"v_cvt_pk_bf16_f32 v67, v150, v151\n"
"buffer_store_dwordx4 v[64:67], %[vst], s[56:59], s54 offen offset:1024 sc1\n"
"s_add_u32 s54, %[o0], 0\n"
"s_add_u32 s54, s54, %[o1]\n"
"v_cvt_pk_bf16_f32 v68, v160, v161\n"
"v_cvt_pk_bf16_f32 v69, v162, v163\n"
"v_cvt_pk_bf16_f32 v70, v164, v165\n"
"v_cvt_pk_bf16_f32 v71, v166, v167\n"
"buffer_store_dwordx4 v[68:71], %[vst], s[56:59], s54 offen offset:0 sc1\n"
"v_cvt_pk_bf16_f32 v72, v176, v177\n"
"v_cvt_pk_bf16_f32 v73, v178, v179\n"
"v_cvt_pk_bf16_f32 v74, v180, v181\n"
"v_cvt_pk_bf16_f32 v75, v182, v183\n"
"buffer_store_dwordx4 v[72:75], %[vst], s[56:59], s54 offen offset:1024 sc1\n"
"s_add_u32 s54, %[o0], 262144\n"
"v_cvt_pk_bf16_f32 v60, v136, v137\n"
"v_cvt_pk_bf16_f32 v61, v138, v139\n"
"v_cvt_pk_bf16_f32 v62, v140, v141\n"
"v_cvt_pk_bf16_f32 v63, v142, v143\n"
"buffer_store_dwordx4 v[60:63], %[vst], s[56:59], s54 offen offset:0 sc1\n"
"v_cvt_pk_bf16_f32 v64, v152, v153\n"
"v_cvt_pk_bf16_f32 v65, v154, v155\n"
"v_cvt_pk_bf16_f32 v66, v156, v157\n"
"v_cvt_pk_bf16_f32 v67, v158, v159\n"
"buffer_store_dwordx4 v[64:67], %[vst], s[56:59], s54 offen offset:1024 sc1\n"
"s_add_u32 s54, %[o0], 262144\n"
"s_add_u32 s54, s54, %[o1]\n"
"v_cvt_pk_bf16_f32 v68, v168, v169\n"
"v_cvt_pk_bf16_f32 v69, v170, v171\n"
"v_cvt_pk_bf16_f32 v70, v172, v173\n"
"v_cvt_pk_bf16_f32 v71, v174, v175\n"
"buffer_store_dwordx4 v[68:71], %[vst], s[56:59], s54 offen offset:0 sc1\n"
"v_cvt_pk_bf16_f32 v72, v184, v185\n"
"v_cvt_pk_bf16_f32 v73, v186, v187\n"
"v_cvt_pk_bf16_f32 v74, v188, v189\n"
"v_cvt_pk_bf16_f32 v75, v190, v191\n"
"buffer_store_dwordx4 v[72:75], %[vst], s[56:59], s54 offen offset:1024 sc1\n"
"s_branch END_%=\n"
"SW_%=:\n"
"s_mov_b32 s50, 0\n"
"s_mov_b32 s51, 0\n"
"s_add_u32 m0, s50, s60\n"
"s_nop 0\n"
"buffer_load_dwordx4 v24, s[36:39], s44 offen offset:0 sc1 lds\n"
"buffer_load_dwordx4 v25, s[36:39], s44 offen offset:1024 sc1 lds\n"
"buffer_load_dwordx4 v26, s[36:39], s44 offen offset:2048 sc1 lds\n"
"buffer_load_dwordx4 v27, s[36:39], s44 offen offset:3072 sc1 lds\n"
"s_add_u32 m0, s50, s62\n"
"s_nop 0\n"
"buffer_load_dwordx4 v32, s[40:43], s45 offen offset:0 lds\n"
"buffer_load_dwordx4 v33, s[40:43], s45 offen offset:1024 lds\n"
"buffer_load_dwordx4 v34, s[40:43], s45 offen offset:2048 lds\n"
"buffer_load_dwordx4 v35, s[40:43], s45 offen offset:3072 lds\n"
"s_add_u32 s44, s44, 2048\n"
"s_add_u32 s45, s45, s48\n"
"s_add_u32 s50, s50, 32768\n"
"s_and_b32 s50, s50, 131071\n"
"v_mov_b64 v[128:129], 0\n"
"v_mov_b64 v[130:131], 0\n"
"v_mov_b64 v[136:137], 0\n"
"v_mov_b64 v[138:139], 0\n"
"v_mov_b64 v[144:145], 0\n"
"v_mov_b64 v[146:147], 0\n"
"v_mov_b64 v[152:153], 0\n"
"v_mov_b64 v[154:155], 0\n"
"v_mov_b64 v[160:161], 0\n"
"v_mov_b64 v[162:163], 0\n"
"v_mov_b64 v[168:169], 0\n"
"v_mov_b64 v[170:171], 0\n"
"v_mov_b64 v[176:177], 0\n"
"v_mov_b64 v[178:179], 0\n"
"v_mov_b64 v[184:185], 0\n"
"v_mov_b64 v[186:187], 0\n"
"v_mov_b64 v[132:133], 0\n"
"v_mov_b64 v[134:135], 0\n"
"v_mov_b64 v[140:141], 0\n"
"v_mov_b64 v[142:143], 0\n"
"v_mov_b64 v[148:149], 0\n"
"v_mov_b64 v[150:151], 0\n"
"v_mov_b64 v[156:157], 0\n"
"v_mov_b64 v[158:159], 0\n"
"v_mov_b64 v[164:165], 0\n"
"v_mov_b64 v[166:167], 0\n"
"v_mov_b64 v[172:173], 0\n"
"v_mov_b64 v[174:175], 0\n"
"v_mov_b64 v[180:181], 0\n"
"v_mov_b64 v[182:183], 0\n"
"v_mov_b64 v[188:189], 0\n"
"v_mov_b64 v[190:191], 0\n"
"v_add_u32 v40, s51, v46\n"
"v_add_u32 v41, s51, v47\n"
"v_add_u32 v42, s51, v48\n"
"v_add_u32 v43, s51, v49\n"
"s_add_u32 m0, s50, s60\n"
"s_nop 0\n"
"buffer_load_dwordx4 v24, s[36:39], s44 offen offset:0 sc1 lds\n"
"buffer_load_dwordx4 v25, s[36:39], s44 offen offset:1024 sc1 lds\n"
"buffer_load_dwordx4 v26, s[36:39], s44 offen offset:2048 sc1 lds\n"
"buffer_load_dwordx4 v27, s[36:39], s44 offen offset:3072 sc1 lds\n"
"s_add_u32 m0, s50, s62\n"
"s_nop 0\n"
"buffer_load_dwordx4 v32, s[40:43], s45 offen offset:0 lds\n"
"buffer_load_dwordx4 v33, s[40:43], s45 offen offset:1024 lds\n"
"buffer_load_dwordx4 v34, s[40:43], s45 offen offset:2048 lds\n"
"buffer_load_dwordx4 v35, s[40:43], s45 offen offset:3072 lds\n"
"s_add_u32 s44, s44, 2048\n"
"s_add_u32 s45, s45, s48\n"
"s_add_u32 s50, s50, 32768\n"
"s_and_b32 s50, s50, 131071\n"
"s_add_u32 m0, s50, s60\n"
"s_nop 0\n"
"buffer_load_dwordx4 v24, s[36:39], s44 offen offset:0 sc1 lds\n"
"buffer_load_dwordx4 v25, s[36:39], s44 offen offset:1024 sc1 lds\n"
"buffer_load_dwordx4 v26, s[36:39], s44 offen offset:2048 sc1 lds\n"
"buffer_load_dwordx4 v27, s[36:39], s44 offen offset:3072 sc1 lds\n"
"s_add_u32 m0, s50, s62\n"
"s_nop 0\n"
"buffer_load_dwordx4 v32, s[40:43], s45 offen offset:0 lds\n"
"buffer_load_dwordx4 v33, s[40:43], s45 offen offset:1024 lds\n"
"buffer_load_dwordx4 v34, s[40:43], s45 offen offset:2048 lds\n"
"buffer_load_dwordx4 v35, s[40:43], s45 offen offset:3072 lds\n"
"s_add_u32 s44, s44, 2048\n"
"s_add_u32 s45, s45, s48\n"
"s_add_u32 s50, s50, 32768\n"
"s_and_b32 s50, s50, 131071\n"
"s_add_u32 m0, s50, s60\n"
"s_nop 0\n"
"buffer_load_dwordx4 v24, s[36:39], s44 offen offset:0 sc1 lds\n"
"buffer_load_dwordx4 v25, s[36:39], s44 offen offset:1024 sc1 lds\n"
"buffer_load_dwordx4 v26, s[36:39], s44 offen offset:2048 sc1 lds\n"
"buffer_load_dwordx4 v27, s[36:39], s44 offen offset:3072 sc1 lds\n"
"s_add_u32 s44, s44, 2048\n"
"s_waitcnt vmcnt(20)\n"
"s_barrier\n"
"ds_read_b64_tr_b16 a[160:161], v42 offset:16384\n"
"ds_read_b64_tr_b16 a[162:163], v42 offset:16896\n"
"ds_read_b128 a[128:131], v40 offset:0\n"
"ds_read_b128 a[132:135], v40 offset:2048\n"
"ds_read_b128 a[136:139], v40 offset:4096\n"
"ds_read_b128 a[140:143], v40 offset:6144\n"
"ds_read_b64_tr_b16 a[164:165], v43 offset:16384\n"
"ds_read_b64_tr_b16 a[166:167], v43 offset:16896\n"
"ds_read_b64_tr_b16 a[168:169], v42 offset:24576\n"
"ds_read_b64_tr_b16 a[170:171], v42 offset:25088\n"
"ds_read_b64_tr_b16 a[172:173], v43 offset:24576\n"
"ds_read_b64_tr_b16 a[174:175], v43 offset:25088\n"
"s_sub_u32 s46, s47, 4\n"
"s_waitcnt lgkmcnt(0)\n"
"KL_s_%=:\n"
"v_mfma_f32_16x16x32_bf16 v[128:131], a[160:163], a[128:131], v[128:131]\n"
"ds_read_b64_tr_b16 a[224:225], v42 offset:20480\n"
"s_add_u32 m0, s50, s62\n"
"v_mfma_f32_16x16x32_bf16 v[132:135], a[160:163], a[132:135], v[132:135]\n"
"ds_read_b64_tr_b16 a[226:227], v42 offset:20992\n"
"buffer_load_dwordx4 v32, s[40:43], s45 offen offset:0 lds\n"
"v_mfma_f32_16x16x32_bf16 v[136:139], a[160:163], a[136:139], v[136:139]\n"
"ds_read_b128 a[192:195], v41 offset:0\n"
"v_mfma_f32_16x16x32_bf16 v[140:143], a[160:163], a[140:143], v[140:143]\n"
"ds_read_b128 a[196:199], v41 offset:2048\n"
"v_mfma_f32_16x16x32_bf16 v[144:147], a[164:167], a[128:131], v[144:147]\n"
"ds_read_b128 a[200:203], v41 offset:4096\n"
"v_mfma_f32_16x16x32_bf16 v[148:151], a[164:167], a[132:135], v[148:151]\n"
"ds_read_b128 a[204:207], v41 offset:6144\n"
"buffer_load_dwordx4 v33, s[40:43], s45 offen offset:1024 lds\n"
"v_mfma_f32_16x16x32_bf16 v[152:155], a[164:167], a[136:139], v[152:155]\n"
"ds_read_b64_tr_b16 a[228:229], v43 offset:20480\n"
"v_mfma_f32_16x16x32_bf16 v[156:159], a[164:167], a[140:143], v[156:159]\n"
"ds_read_b64_tr_b16 a[230:231], v43 offset:20992\n"
"v_mfma_f32_16x16x32_bf16 v[160:163], a[168:171], a[128:131], v[160:163]\n"
"ds_read_b64_tr_b16 a[232:233], v42 offset:28672\n"
"v_mfma_f32_16x16x32_bf16 v[164:167], a[168:171], a[132:135], v[164:167]\n"
"ds_read_b64_tr_b16 a[234:235], v42 offset:29184\n"
"buffer_load_dwordx4 v34, s[40:43], s45 offen offset:2048 lds\n"
"v_mfma_f32_16x16x32_bf16 v[168:171], a[168:171], a[136:139], v[168:171]\n"
"ds_read_b64_tr_b16 a[236:237], v43 offset:28672\n"
"v_mfma_f32_16x16x32_bf16 v[172:175], a[168:171], a[140:143], v[172:175]\n"
"ds_read_b64_tr_b16 a[238:239], v43 offset:29184\n"
"v_mfma_f32_16x16x32_bf16 v[176:179], a[172:175], a[128:131], v[176:179]\n"
"v_mfma_f32_16x16x32_bf16 v[180:183], a[172:175], a[132:135], v[180:183]\n"
"buffer_load_dwordx4 v35, s[40:43], s45 offen offset:3072 lds\n"
"v_mfma_f32_16x16x32_bf16 v[184:187], a[172:175], a[136:139], v[184:187]\n"
"s_add_u32 s45, s45, s48\n"
"s_add_u32 s50, s50, 32768\n"
"s_and_b32 s50, s50, 131071\n"
"s_add_u32 s51, s51, 32768\n"
"s_and_b32 s51, s51, 131071\n"
"v_mfma_f32_16x16x32_bf16 v[188:191], a[172:175], a[140:143], v[188:191]\n"
"s_waitcnt lgkmcnt(0) vmcnt(16)\n"
"s_barrier\n"
"v_mfma_f32_16x16x32_bf16 v[128:131], a[224:227], a[192:195], v[128:131]\n"
"v_add_u32 v40, s51, v46\n"
"v_add_u32 v41, s51, v47\n"
"s_add_u32 m0, s50, s60\n"
"v_mfma_f32_16x16x32_bf16 v[132:135], a[224:227], a[196:199], v[132:135]\n"
"v_add_u32 v42, s51, v48\n"
"v_add_u32 v43, s51, v49\n"
"buffer_load_dwordx4 v24, s[36:39], s44 offen offset:0 sc1 lds\n"
"v_mfma_f32_16x16x32_bf16 v[136:139], a[224:227], a[200:203], v[136:139]\n"
"ds_read_b64_tr_b16 a[160:161], v42 offset:16384\n"
"v_mfma_f32_16x16x32_bf16 v[140:143], a[224:227], a[204:207], v[140:143]\n"
"ds_read_b64_tr_b16 a[162:163], v42 offset:16896\n"
"v_mfma_f32_16x16x32_bf16 v[144:147], a[228:231], a[192:195], v[144:147]\n"
"ds_read_b128 a[128:131], v40 offset:0\n"
"v_mfma_f32_16x16x32_bf16 v[148:151], a[228:231], a[196:199], v[148:151]\n"
"ds_read_b128 a[132:135], v40 offset:2048\n"
"buffer_load_dwordx4 v25, s[36:39], s44 offen offset:1024 sc1 lds\n"
"v_mfma_f32_16x16x32_bf16 v[152:155], a[228:231], a[200:203], v[152:155]\n"
"ds_read_b128 a[136:139], v40 offset:4096\n"
"v_mfma_f32_16x16x32_bf16 v[156:159], a[228:231], a[204:207], v[156:159]\n"
"ds_read_b128 a[140:143], v40 offset:6144\n"
"v_mfma_f32_16x16x32_bf16 v[160:163], a[232:235], a[192:195], v[160:163]\n"
"ds_read_b64_tr_b16 a[164:165], v43 offset:16384\n"
"v_mfma_f32_16x16x32_bf16 v[164:167], a[232:235], a[196:199], v[164:167]\n"
"ds_read_b64_tr_b16 a[166:167], v43 offset:16896\n"
"buffer_load_dwordx4 v26, s[36:39], s44 offen offset:2048 sc1 lds\n"
"v_mfma_f32_16x16x32_bf16 v[168:171], a[232:235], a[200:203], v[168:171]\n"
"ds_read_b64_tr_b16 a[168:169], v42 offset:24576\n"
"v_mfma_f32_16x16x32_bf16 v[172:175], a[232:235], a[204:207], v[172:175]\n"
"ds_read_b64_tr_b16 a[170:171], v42 offset:25088\n"
"v_mfma_f32_16x16x32_bf16 v[176:179], a[236:239], a[192:195], v[176:179]\n"
"ds_read_b64_tr_b16 a[172:173], v43 offset:24576\n"
"v_mfma_f32_16x16x32_bf16 v[180:183], a[236:239], a[196:199], v[180:183]\n"
"ds_read_b64_tr_b16 a[174:175], v43 offset:25088\n"
"buffer_load_dwordx4 v27, s[36:39], s44 offen offset:3072 sc1 lds\n"
"v_mfma_f32_16x16x32_bf16 v[184:187], a[236:239], a[200:203], v[184:187]\n"
"s_add_u32 s44, s44, 2048\n"
"v_mfma_f32_16x16x32_bf16 v[188:191], a[236:239], a[204:207], v[188:191]\n"
"s_sub_u32 s46, s46, 1\n"
"s_cmp_lg_u32 s46, 0\n"
"s_waitcnt lgkmcnt(0)\n"
"s_cbranch_scc1 KL_s_%=\n"
"v_mfma_f32_16x16x32_bf16 v[128:131], a[160:163], a[128:131], v[128:131]\n"
"ds_read_b64_tr_b16 a[224:225], v42 offset:20480\n"
"s_add_u32 m0, s50, s62\n"
"v_mfma_f32_16x16x32_bf16 v[132:135], a[160:163], a[132:135], v[132:135]\n"
"ds_read_b64_tr_b16 a[226:227], v42 offset:20992\n"
"buffer_load_dwordx4 v32, s[40:43], s45 offen offset:0 lds\n"
"v_mfma_f32_16x16x32_bf16 v[136:139], a[160:163], a[136:139], v[136:139]\n"
"ds_read_b128 a[192:195], v41 offset:0\n"
"v_mfma_f32_16x16x32_bf16 v[140:143], a[160:163], a[140:143], v[140:143]\n"
"ds_read_b128 a[196:199], v41 offset:2048\n"
"v_mfma_f32_16x16x32_bf16 v[144:147], a[164:167], a[128:131], v[144:147]\n"
"ds_read_b128 a[200:203], v41 offset:4096\n"
"v_mfma_f32_16x16x32_bf16 v[148:151], a[164:167], a[132:135], v[148:151]\n"
"ds_read_b128 a[204:207], v41 offset:6144\n"
"buffer_load_dwordx4 v33, s[40:43], s45 offen offset:1024 lds\n"
"v_mfma_f32_16x16x32_bf16 v[152:155], a[164:167], a[136:139], v[152:155]\n"
"ds_read_b64_tr_b16 a[228:229], v43 offset:20480\n"
"v_mfma_f32_16x16x32_bf16 v[156:159], a[164:167], a[140:143], v[156:159]\n"
"ds_read_b64_tr_b16 a[230:231], v43 offset:20992\n"
"v_mfma_f32_16x16x32_bf16 v[160:163], a[168:171], a[128:131], v[160:163]\n"
"ds_read_b64_tr_b16 a[232:233], v42 offset:28672\n"
"v_mfma_f32_16x16x32_bf16 v[164:167], a[168:171], a[132:135], v[164:167]\n"
"ds_read_b64_tr_b16 a[234:235], v42 offset:29184\n"
"buffer_load_dwordx4 v34, s[40:43], s45 offen offset:2048 lds\n"
"v_mfma_f32_16x16x32_bf16 v[168:171], a[168:171], a[136:139], v[168:171]\n"
"ds_read_b64_tr_b16 a[236:237], v43 offset:28672\n"
"v_mfma_f32_16x16x32_bf16 v[172:175], a[168:171], a[140:143], v[172:175]\n"
"ds_read_b64_tr_b16 a[238:239], v43 offset:29184\n"
"v_mfma_f32_16x16x32_bf16 v[176:179], a[172:175], a[128:131], v[176:179]\n"
"v_mfma_f32_16x16x32_bf16 v[180:183], a[172:175], a[132:135], v[180:183]\n"
"buffer_load_dwordx4 v35, s[40:43], s45 offen offset:3072 lds\n"
"v_mfma_f32_16x16x32_bf16 v[184:187], a[172:175], a[136:139], v[184:187]\n"
"s_add_u32 s45, s45, s48\n"
"s_add_u32 s50, s50, 32768\n"
"s_and_b32 s50, s50, 131071\n"
"s_add_u32 s51, s51, 32768\n"
"s_and_b32 s51, s51, 131071\n"
"v_mfma_f32_16x16x32_bf16 v[188:191], a[172:175], a[140:143], v[188:191]\n"
"s_waitcnt lgkmcnt(0) vmcnt(16)\n"
"s_barrier\n"
"v_mfma_f32_16x16x32_bf16 v[128:131], a[224:227], a[192:195], v[128:131]\n"
"v_add_u32 v40, s51, v46\n"
"v_add_u32 v41, s51, v47\n"
"v_mfma_f32_16x16x32_bf16 v[132:135], a[224:227], a[196:199], v[132:135]\n"
"v_add_u32 v42, s51, v48\n"
"v_add_u32 v43, s51, v49\n"
"v_mfma_f32_16x16x32_bf16 v[136:139], a[224:227], a[200:203], v[136:139]\n"
"ds_read_b64_tr_b16 a[160:161], v42 offset:16384\n"
"v_mfma_f32_16x16x32_bf16 v[140:143], a[224:227], a[204:207], v[140:143]\n"
"ds_read_b64_tr_b16 a[162:163], v42 offset:16896\n"
"v_mfma_f32_16x16x32_bf16 v[144:147], a[228:231], a[192:195], v[144:147]\n"
"ds_read_b128 a[128:131], v40 offset:0\n"
"v_mfma_f32_16x16x32_bf16 v[148:151], a[228:231], a[196:199], v[148:151]\n"
"ds_read_b128 a[132:135], v40 offset:2048\n"
"v_mfma_f32_16x16x32_bf16 v[152:155], a[228:231], a[200:203], v[152:155]\n"
"ds_read_b128 a[136:139], v40 offset:4096\n"
"v_mfma_f32_16x16x32_bf16 v[156:159], a[228:231], a[204:207], v[156:159]\n"
"ds_read_b128 a[140:143], v40 offset:6144\n"
"v_mfma_f32_16x16x32_bf16 v[160:163], a[232:235], a[192:195], v[160:163]\n"
"ds_read_b64_tr_b16 a[164:165], v43 offset:16384\n"
"v_mfma_f32_16x16x32_bf16 v[164:167], a[232:235], a[196:199], v[164:167]\n"
"ds_read_b64_tr_b16 a[166:167], v43 offset:16896\n"
"v_mfma_f32_16x16x32_bf16 v[168:171], a[232:235], a[200:203], v[168:171]\n"
"ds_read_b64_tr_b16 a[168:169], v42 offset:24576\n"
"v_mfma_f32_16x16x32_bf16 v[172:175], a[232:235], a[204:207], v[172:175]\n"
"ds_read_b64_tr_b16 a[170:171], v42 offset:25088\n"
"v_mfma_f32_16x16x32_bf16 v[176:179], a[236:239], a[192:195], v[176:179]\n"
"ds_read_b64_tr_b16 a[172:173], v43 offset:24576\n"
"v_mfma_f32_16x16x32_bf16 v[180:183], a[236:239], a[196:199], v[180:183]\n"
"ds_read_b64_tr_b16 a[174:175], v43 offset:25088\n"
"v_mfma_f32_16x16x32_bf16 v[184:187], a[236:239], a[200:203], v[184:187]\n"
"v_mfma_f32_16x16x32_bf16 v[188:191], a[236:239], a[204:207], v[188:191]\n"
"s_waitcnt lgkmcnt(0)\n"
"v_mfma_f32_16x16x32_bf16 v[128:131], a[160:163], a[128:131], v[128:131]\n"
"ds_read_b64_tr_b16 a[224:225], v42 offset:20480\n"
"v_mfma_f32_16x16x32_bf16 v[132:135], a[160:163], a[132:135], v[132:135]\n"
"ds_read_b64_tr_b16 a[226:227], v42 offset:20992\n"
"v_mfma_f32_16x16x32_bf16 v[136:139], a[160:163], a[136:139], v[136:139]\n"
"ds_read_b128 a[192:195], v41 offset:0\n"
"v_mfma_f32_16x16x32_bf16 v[140:143], a[160:163], a[140:143], v[140:143]\n"
"ds_read_b128 a[196:199], v41 offset:2048\n"
"v_mfma_f32_16x16x32_bf16 v[144:147], a[164:167], a[128:131], v[144:147]\n"
"ds_read_b128 a[200:203], v41 offset:4096\n"
"v_mfma_f32_16x16x32_bf16 v[148:151], a[164:167], a[132:135], v[148:151]\n"
"ds_read_b128 a[204:207], v41 offset:6144\n"
"v_mfma_f32_16x16x32_bf16 v[152:155], a[164:167], a[136:139], v[152:155]\n"
"ds_read_b64_tr_b16 a[228:229], v43 offset:20480\n"
"v_mfma_f32_16x16x32_bf16 v[156:159], a[164:167], a[140:143], v[156:159]\n"
"ds_read_b64_tr_b16 a[230:231], v43 offset:20992\n"
"v_mfma_f32_16x16x32_bf16 v[160:163], a[168:171], a[128:131], v[160:163]\n"
"ds_read_b64_tr_b16 a[232:233], v42 offset:28672\n"
"v_mfma_f32_16x16x32_bf16 v[164:167], a[168:171], a[132:135], v[164:167]\n"
"ds_read_b64_tr_b16 a[234:235], v42 offset:29184\n"
"v_mfma_f32_16x16x32_bf16 v[168:171], a[168:171], a[136:139], v[168:171]\n"
"ds_read_b64_tr_b16 a[236:237], v43 offset:28672\n"
"v_mfma_f32_16x16x32_bf16 v[172:175], a[168:171], a[140:143], v[172:175]\n"
"ds_read_b64_tr_b16 a[238:239], v43 offset:29184\n"
"v_mfma_f32_16x16x32_bf16 v[176:179], a[172:175], a[128:131], v[176:179]\n"
"v_mfma_f32_16x16x32_bf16 v[180:183], a[172:175], a[132:135], v[180:183]\n"
"v_mfma_f32_16x16x32_bf16 v[184:187], a[172:175], a[136:139], v[184:187]\n"
"s_add_u32 s51, s51, 32768\n"
"s_and_b32 s51, s51, 131071\n"
"v_mfma_f32_16x16x32_bf16 v[188:191], a[172:175], a[140:143], v[188:191]\n"
"s_waitcnt lgkmcnt(0) vmcnt(8)\n"
"s_barrier\n"
"v_mfma_f32_16x16x32_bf16 v[128:131], a[224:227], a[192:195], v[128:131]\n"
"v_add_u32 v40, s51, v46\n"
"v_add_u32 v41, s51, v47\n"
"v_mfma_f32_16x16x32_bf16 v[132:135], a[224:227], a[196:199], v[132:135]\n"
"v_add_u32 v42, s51, v48\n"
"v_add_u32 v43, s51, v49\n"
"v_mfma_f32_16x16x32_bf16 v[136:139], a[224:227], a[200:203], v[136:139]\n"
"ds_read_b64_tr_b16 a[160:161], v42 offset:16384\n"
"v_mfma_f32_16x16x32_bf16 v[140:143], a[224:227], a[204:207], v[140:143]\n"
"ds_read_b64_tr_b16 a[162:163], v42 offset:16896\n"
"v_mfma_f32_16x16x32_bf16 v[144:147], a[228:231], a[192:195], v[144:147]\n"
"ds_read_b128 a[128:131], v40 offset:0\n"
"v_mfma_f32_16x16x32_bf16 v[148:151], a[228:231], a[196:199], v[148:151]\n"
"ds_read_b128 a[132:135], v40 offset:2048\n"
"v_mfma_f32_16x16x32_bf16 v[152:155], a[228:231], a[200:203], v[152:155]\n"
"ds_read_b128 a[136:139], v40 offset:4096\n"
"v_mfma_f32_16x16x32_bf16 v[156:159], a[228:231], a[204:207], v[156:159]\n"
"ds_read_b128 a[140:143], v40 offset:6144\n"
"v_mfma_f32_16x16x32_bf16 v[160:163], a[232:235], a[192:195], v[160:163]\n"
"ds_read_b64_tr_b16 a[164:165], v43 offset:16384\n"
"v_mfma_f32_16x16x32_bf16 v[164:167], a[232:235], a[196:199], v[164:167]\n"
"ds_read_b64_tr_b16 a[166:167], v43 offset:16896\n"
"v_mfma_f32_16x16x32_bf16 v[168:171], a[232:235], a[200:203], v[168:171]\n"
"ds_read_b64_tr_b16 a[168:169], v42 offset:24576\n"
"v_mfma_f32_16x16x32_bf16 v[172:175], a[232:235], a[204:207], v[172:175]\n"
"ds_read_b64_tr_b16 a[170:171], v42 offset:25088\n"
"v_mfma_f32_16x16x32_bf16 v[176:179], a[236:239], a[192:195], v[176:179]\n"
"ds_read_b64_tr_b16 a[172:173], v43 offset:24576\n"
"v_mfma_f32_16x16x32_bf16 v[180:183], a[236:239], a[196:199], v[180:183]\n"
"ds_read_b64_tr_b16 a[174:175], v43 offset:25088\n"
"v_mfma_f32_16x16x32_bf16 v[184:187], a[236:239], a[200:203], v[184:187]\n"
"v_mfma_f32_16x16x32_bf16 v[188:191], a[236:239], a[204:207], v[188:191]\n"
"s_waitcnt lgkmcnt(0)\n"
"v_mfma_f32_16x16x32_bf16 v[128:131], a[160:163], a[128:131], v[128:131]\n"
"ds_read_b64_tr_b16 a[224:225], v42 offset:20480\n"
"v_mfma_f32_16x16x32_bf16 v[132:135], a[160:163], a[132:135], v[132:135]\n"
"ds_read_b64_tr_b16 a[226:227], v42 offset:20992\n"
"v_mfma_f32_16x16x32_bf16 v[136:139], a[160:163], a[136:139], v[136:139]\n"
"ds_read_b128 a[192:195], v41 offset:0\n"
"v_mfma_f32_16x16x32_bf16 v[140:143], a[160:163], a[140:143], v[140:143]\n"
"ds_read_b128 a[196:199], v41 offset:2048\n"
"v_mfma_f32_16x16x32_bf16 v[144:147], a[164:167], a[128:131], v[144:147]\n"
"ds_read_b128 a[200:203], v41 offset:4096\n"
"v_mfma_f32_16x16x32_bf16 v[148:151], a[164:167], a[132:135], v[148:151]\n"
"ds_read_b128 a[204:207], v41 offset:6144\n"
"v_mfma_f32_16x16x32_bf16 v[152:155], a[164:167], a[136:139], v[152:155]\n"
"ds_read_b64_tr_b16 a[228:229], v43 offset:20480\n"
"v_mfma_f32_16x16x32_bf16 v[156:159], a[164:167], a[140:143], v[156:159]\n"
"ds_read_b64_tr_b16 a[230:231], v43 offset:20992\n"
"v_mfma_f32_16x16x32_bf16 v[160:163], a[168:171], a[128:131], v[160:163]\n"
"ds_read_b64_tr_b16 a[232:233], v42 offset:28672\n"
"v_mfma_f32_16x16x32_bf16 v[164:167], a[168:171], a[132:135], v[164:167]\n"
"ds_read_b64_tr_b16 a[234:235], v42 offset:29184\n"
"v_mfma_f32_16x16x32_bf16 v[168:171], a[168:171], a[136:139], v[168:171]\n"
"ds_read_b64_tr_b16 a[236:237], v43 offset:28672\n"
"v_mfma_f32_16x16x32_bf16 v[172:175], a[168:171], a[140:143], v[172:175]\n"
"ds_read_b64_tr_b16 a[238:239], v43 offset:29184\n"
"v_mfma_f32_16x16x32_bf16 v[176:179], a[172:175], a[128:131], v[176:179]\n"
"v_mfma_f32_16x16x32_bf16 v[180:183], a[172:175], a[132:135], v[180:183]\n"
"v_mfma_f32_16x16x32_bf16 v[184:187], a[172:175], a[136:139], v[184:187]\n"
"s_add_u32 s51, s51, 32768\n"
"s_and_b32 s51, s51, 131071\n"
"v_mfma_f32_16x16x32_bf16 v[188:191], a[172:175], a[140:143], v[188:191]\n"
"s_waitcnt lgkmcnt(0) vmcnt(0)\n"
"s_barrier\n"
"v_mfma_f32_16x16x32_bf16 v[128:131], a[224:227], a[192:195], v[128:131]\n"
"v_add_u32 v40, s51, v46\n"
"v_add_u32 v41, s51, v47\n"
"v_mfma_f32_16x16x32_bf16 v[132:135], a[224:227], a[196:199], v[132:135]\n"
"v_add_u32 v42, s51, v48\n"
"v_add_u32 v43, s51, v49\n"
"v_mfma_f32_16x16x32_bf16 v[136:139], a[224:227], a[200:203], v[136:139]\n"
"ds_read_b64_tr_b16 a[160:161], v42 offset:16384\n"
"v_mfma_f32_16x16x32_bf16 v[140:143], a[224:227], a[204:207], v[140:143]\n"
"ds_read_b64_tr_b16 a[162:163], v42 offset:16896\n"
"v_mfma_f32_16x16x32_bf16 v[144:147], a[228:231], a[192:195], v[144:147]\n"
"ds_read_b128 a[128:131], v40 offset:0\n"
"v_mfma_f32_16x16x32_bf16 v[148:151], a[228:231], a[196:199], v[148:151]\n"
"ds_read_b128 a[132:135], v40 offset:2048\n"
"v_mfma_f32_16x16x32_bf16 v[152:155], a[228:231], a[200:203], v[152:155]\n"
"ds_read_b128 a[136:139], v40 offset:4096\n"
"v_mfma_f32_16x16x32_bf16 v[156:159], a[228:231], a[204:207], v[156:159]\n"
"ds_read_b128 a[140:143], v40 offset:6144\n"
"v_mfma_f32_16x16x32_bf16 v[160:163], a[232:235], a[192:195], v[160:163]\n"
"ds_read_b64_tr_b16 a[164:165], v43 offset:16384\n"
"v_mfma_f32_16x16x32_bf16 v[164:167], a[232:235], a[196:199], v[164:167]\n"
"ds_read_b64_tr_b16 a[166:167], v43 offset:16896\n"
"v_mfma_f32_16x16x32_bf16 v[168:171], a[232:235], a[200:203], v[168:171]\n"
"ds_read_b64_tr_b16 a[168:169], v42 offset:24576\n"
"v_mfma_f32_16x16x32_bf16 v[172:175], a[232:235], a[204:207], v[172:175]\n"
"ds_read_b64_tr_b16 a[170:171], v42 offset:25088\n"
"v_mfma_f32_16x16x32_bf16 v[176:179], a[236:239], a[192:195], v[176:179]\n"
"ds_read_b64_tr_b16 a[172:173], v43 offset:24576\n"
"v_mfma_f32_16x16x32_bf16 v[180:183], a[236:239], a[196:199], v[180:183]\n"
"ds_read_b64_tr_b16 a[174:175], v43 offset:25088\n"
"v_mfma_f32_16x16x32_bf16 v[184:187], a[236:239], a[200:203], v[184:187]\n"
"v_mfma_f32_16x16x32_bf16 v[188:191], a[236:239], a[204:207], v[188:191]\n"
"s_waitcnt lgkmcnt(0)\n"
"v_mfma_f32_16x16x32_bf16 v[128:131], a[160:163], a[128:131], v[128:131]\n"
"ds_read_b64_tr_b16 a[224:225], v42 offset:20480\n"
"v_mfma_f32_16x16x32_bf16 v[132:135], a[160:163], a[132:135], v[132:135]\n"
"ds_read_b64_tr_b16 a[226:227], v42 offset:20992\n"
"v_mfma_f32_16x16x32_bf16 v[136:139], a[160:163], a[136:139], v[136:139]\n"
"ds_read_b128 a[192:195], v41 offset:0\n"
"v_mfma_f32_16x16x32_bf16 v[140:143], a[160:163], a[140:143], v[140:143]\n"
"ds_read_b128 a[196:199], v41 offset:2048\n"
"v_mfma_f32_16x16x32_bf16 v[144:147], a[164:167], a[128:131], v[144:147]\n"
"ds_read_b128 a[200:203], v41 offset:4096\n"
"v_mfma_f32_16x16x32_bf16 v[148:151], a[164:167], a[132:135], v[148:151]\n"
"ds_read_b128 a[204:207], v41 offset:6144\n"
"v_mfma_f32_16x16x32_bf16 v[152:155], a[164:167], a[136:139], v[152:155]\n"
"ds_read_b64_tr_b16 a[228:229], v43 offset:20480\n"
"v_mfma_f32_16x16x32_bf16 v[156:159], a[164:167], a[140:143], v[156:159]\n"
"ds_read_b64_tr_b16 a[230:231], v43 offset:20992\n"
"v_mfma_f32_16x16x32_bf16 v[160:163], a[168:171], a[128:131], v[160:163]\n"
"ds_read_b64_tr_b16 a[232:233], v42 offset:28672\n"
"v_mfma_f32_16x16x32_bf16 v[164:167], a[168:171], a[132:135], v[164:167]\n"
"ds_read_b64_tr_b16 a[234:235], v42 offset:29184\n"
"v_mfma_f32_16x16x32_bf16 v[168:171], a[168:171], a[136:139], v[168:171]\n"
"ds_read_b64_tr_b16 a[236:237], v43 offset:28672\n"
"v_mfma_f32_16x16x32_bf16 v[172:175], a[168:171], a[140:143], v[172:175]\n"
"ds_read_b64_tr_b16 a[238:239], v43 offset:29184\n"
"v_mfma_f32_16x16x32_bf16 v[176:179], a[172:175], a[128:131], v[176:179]\n"
"v_mfma_f32_16x16x32_bf16 v[180:183], a[172:175], a[132:135], v[180:183]\n"
"v_mfma_f32_16x16x32_bf16 v[184:187], a[172:175], a[136:139], v[184:187]\n"
"s_add_u32 s51, s51, 32768\n"
"s_and_b32 s51, s51, 131071\n"
"v_mfma_f32_16x16x32_bf16 v[188:191], a[172:175], a[140:143], v[188:191]\n"
"s_waitcnt lgkmcnt(0)\n"
"v_mfma_f32_16x16x32_bf16 v[128:131], a[224:227], a[192:195], v[128:131]\n"
"v_mfma_f32_16x16x32_bf16 v[132:135], a[224:227], a[196:199], v[132:135]\n"
"v_mfma_f32_16x16x32_bf16 v[136:139], a[224:227], a[200:203], v[136:139]\n"
"v_mfma_f32_16x16x32_bf16 v[140:143], a[224:227], a[204:207], v[140:143]\n"
"v_mfma_f32_16x16x32_bf16 v[144:147], a[228:231], a[192:195], v[144:147]\n"
"v_mfma_f32_16x16x32_bf16 v[148:151], a[228:231], a[196:199], v[148:151]\n"
"v_mfma_f32_16x16x32_bf16 v[152:155], a[228:231], a[200:203], v[152:155]\n"
"v_mfma_f32_16x16x32_bf16 v[156:159], a[228:231], a[204:207], v[156:159]\n"
"v_mfma_f32_16x16x32_bf16 v[160:163], a[232:235], a[192:195], v[160:163]\n"
"v_mfma_f32_16x16x32_bf16 v[164:167], a[232:235], a[196:199], v[164:167]\n"
"v_mfma_f32_16x16x32_bf16 v[168:171], a[232:235], a[200:203], v[168:171]\n"
"v_mfma_f32_16x16x32_bf16 v[172:175], a[232:235], a[204:207], v[172:175]\n"
"v_mfma_f32_16x16x32_bf16 v[176:179], a[236:239], a[192:195], v[176:179]\n"
"v_mfma_f32_16x16x32_bf16 v[180:183], a[236:239], a[196:199], v[180:183]\n"
"v_mfma_f32_16x16x32_bf16 v[184:187], a[236:239], a[200:203], v[184:187]\n"
"v_mfma_f32_16x16x32_bf16 v[188:191], a[236:239], a[204:207], v[188:191]\n"
"s_barrier\n"
"s_cmp_lg_u32 %[kind], 0\n"
"s_cbranch_scc1 NX0_%=\n"
"s_add_u32 s54, %[o0], 0\n"
"v_cvt_pk_bf16_f32 v60, v128, v129\n"
"v_cvt_pk_bf16_f32 v61, v130, v131\n"
"v_cvt_pk_bf16_f32 v62, v144, v145\n"
"v_cvt_pk_bf16_f32 v63, v146, v147\n"
"s_nop 0\n"
"v_permlane16_swap_b32 v60, v62\n"
"v_permlane16_swap_b32 v61, v63\n"
"buffer_store_dwordx4 v[60:63], %[vst], s[56:59], s54 offen offset:0 sc1\n"
"s_add_u32 s54, %[o0], 0\n"
"s_add_u32 s54, s54, %[o1]\n"
"v_cvt_pk_bf16_f32 v64, v160, v161\n"
"v_cvt_pk_bf16_f32 v65, v162, v163\n"
"v_cvt_pk_bf16_f32 v66, v176, v177\n"
"v_cvt_pk_bf16_f32 v67, v178, v179\n"
"s_nop 0\n"
"v_permlane16_swap_b32 v64, v66\n"
"v_permlane16_swap_b32 v65, v67\n"
"buffer_store_dwordx4 v[64:67], %[vst], s[56:59], s54 offen offset:0 sc1\n"
"s_add_u32 s54, %[o0], 4096\n"
"v_cvt_pk_bf16_f32 v68, v132, v133\n"
"v_cvt_pk_bf16_f32 v69, v134, v135\n"
"v_cvt_pk_bf16_f32 v70, v148, v149\n"
"v_cvt_pk_bf16_f32 v71, v150, v151\n"
"s_nop 0\n"
"v_permlane16_swap_b32 v68, v70\n"
"v_permlane16_swap_b32 v69, v71\n"
"buffer_store_dwordx4 v[68:71], %[vst], s[56:59], s54 offen offset:0 sc1\n"
"s_add_u32 s54, %[o0], 4096\n"
"s_add_u32 s54, s54, %[o1]\n"
"v_cvt_pk_bf16_f32 v72, v164, v165\n"
"v_cvt_pk_bf16_f32 v73, v166, v167\n"
"v_cvt_pk_bf16_f32 v74, v180, v181\n"
"v_cvt_pk_bf16_f32 v75, v182, v183\n"
"s_nop 0\n"
"v_permlane16_swap_b32 v72, v74\n"
"v_permlane16_swap_b32 v73, v75\n"
"buffer_store_dwordx4 v[72:75], %[vst], s[56:59], s54 offen offset:0 sc1\n"
"s_add_u32 s54, %[o0], 262144\n"
"v_cvt_pk_bf16_f32 v60, v136, v137\n"
"v_cvt_pk_bf16_f32 v61, v138, v139\n"
"v_cvt_pk_bf16_f32 v62, v152, v153\n"
"v_cvt_pk_bf16_f32 v63, v154, v155\n"
"s_nop 0\n"
"v_permlane16_swap_b32 v60, v62\n"
"v_permlane16_swap_b32 v61, v63\n"
"buffer_store_dwordx4 v[60:63], %[vst], s[56:59], s54 offen offset:0 sc1\n"
"s_add_u32 s54, %[o0], 262144\n"
"s_add_u32 s54, s54, %[o1]\n"
"v_cvt_pk_bf16_f32 v64, v168, v169\n"
"v_cvt_pk_bf16_f32 v65, v170, v171\n"
"v_cvt_pk_bf16_f32 v66, v184, v185\n"
"v_cvt_pk_bf16_f32 v67, v186, v187\n"
"s_nop 0\n"
"v_permlane16_swap_b32 v64, v66\n"
"v_permlane16_swap_b32 v65, v67\n"
"buffer_store_dwordx4 v[64:67], %[vst], s[56:59], s54 offen offset:0 sc1\n"
"s_add_u32 s54, %[o0], 266240\n"
"v_cvt_pk_bf16_f32 v68, v140, v141\n"
"v_cvt_pk_bf16_f32 v69, v142, v143\n"
"v_cvt_pk_bf16_f32 v70, v156, v157\n"
"v_cvt_pk_bf16_f32 v71, v158, v159\n"
"s_nop 0\n"
"v_permlane16_swap_b32 v68, v70\n"
"v_permlane16_swap_b32 v69, v71\n"
"buffer_store_dwordx4 v[68:71], %[vst], s[56:59], s54 offen offset:0 sc1\n"
"s_add_u32 s54, %[o0], 266240\n"
"s_add_u32 s54, s54, %[o1]\n"
"v_cvt_pk_bf16_f32 v72, v172, v173\n"
"v_cvt_pk_bf16_f32 v73, v174, v175\n"
"v_cvt_pk_bf16_f32 v74, v188, v189\n"
"v_cvt_pk_bf16_f32 v75, v190, v191\n"
"s_nop 0\n"
"v_permlane16_swap_b32 v72, v74\n"
"v_permlane16_swap_b32 v73, v75\n"
"buffer_store_dwordx4 v[72:75], %[vst], s[56:59], s54 offen offset:0 sc1\n"
"s_branch END_%=\n"
"NX0_%=:\n"
"s_cmp_lg_u32 %[kind], 2\n"
"s_cbranch_scc1 NX2_%=\n"
"s_add_u32 s54, %[o0], 0\n"
"v_cvt_pk_bf16_f32 v60, v128, v129\n"
"v_cvt_pk_bf16_f32 v61, v130, v131\n"
"v_cvt_pk_bf16_f32 v62, v144, v145\n"
"v_cvt_pk_bf16_f32 v63, v146, v147\n"
"s_nop 0\n"
"v_permlane16_swap_b32 v60, v62\n"
"v_permlane16_swap_b32 v61, v63\n"
"buffer_store_dwordx4 v[60:63], %[vst], s[56:59], s54 offen offset:0 sc1\n"
"s_add_u32 s54, %[o0], 0\n"
"s_add_u32 s54, s54, %[o1]\n"
"v_cvt_pk_bf16_f32 v64, v160, v161\n"
"v_cvt_pk_bf16_f32 v65, v162, v163\n"
"v_cvt_pk_bf16_f32 v66, v176, v177\n"
"v_cvt_pk_bf16_f32 v67, v178, v179\n"
"s_nop 0\n"
"v_permlane16_swap_b32 v64, v66\n"
"v_permlane16_swap_b32 v65, v67\n"
"buffer_store_dwordx4 v[64:67], %[vst], s[56:59], s54 offen offset:0 sc1\n"
"s_add_u32 s54, %[o0], 32768\n"
"v_cvt_pk_bf16_f32 v68, v132, v133\n"
"v_cvt_pk_bf16_f32 v69, v134, v135\n"
"v_cvt_pk_bf16_f32 v70, v148, v149\n"
"v_cvt_pk_bf16_f32 v71, v150, v151\n"
"s_nop 0\n"
"v_permlane16_swap_b32 v68, v70\n"
"v_permlane16_swap_b32 v69, v71\n"
"buffer_store_dwordx4 v[68:71], %[vst], s[56:59], s54 offen offset:0 sc1\n"
"s_add_u32 s54, %[o0], 32768\n"
"s_add_u32 s54, s54, %[o1]\n"
"v_cvt_pk_bf16_f32 v72, v164, v165\n"
"v_cvt_pk_bf16_f32 v73, v166, v167\n"
"v_cvt_pk_bf16_f32 v74, v180, v181\n"
"v_cvt_pk_bf16_f32 v75, v182, v183\n"
"s_nop 0\n"
"v_permlane16_swap_b32 v72, v74\n"
"v_permlane16_swap_b32 v73, v75\n"
"buffer_store_dwordx4 v[72:75], %[vst], s[56:59], s54 offen offset:0 sc1\n"
"s_add_u32 s54, %[o0], 65536\n"
"v_cvt_pk_bf16_f32 v60, v136, v137\n"
"v_cvt_pk_bf16_f32 v61, v138, v139\n"
"v_cvt_pk_bf16_f32 v62, v152, v153\n"
"v_cvt_pk_bf16_f32 v63, v154, v155\n"
"s_nop 0\n"
"v_permlane16_swap_b32 v60, v62\n"
"v_permlane16_swap_b32 v61, v63\n"
"buffer_store_dwordx4 v[60:63], %[vst], s[56:59], s54 offen offset:0 sc1\n"
"s_add_u32 s54, %[o0], 65536\n"
"s_add_u32 s54, s54, %[o1]\n"
"v_cvt_pk_bf16_f32 v64, v168, v169\n"
"v_cvt_pk_bf16_f32 v65, v170, v171\n"
"v_cvt_pk_bf16_f32 v66, v184, v185\n"
"v_cvt_pk_bf16_f32 v67, v186, v187\n"
"s_nop 0\n"
"v_permlane16_swap_b32 v64, v66\n"
"v_permlane16_swap_b32 v65, v67\n"
"buffer_store_dwordx4 v[64:67], %[vst], s[56:59], s54 offen offset:0 sc1\n"
"s_add_u32 s54, %[o0], 98304\n"
"v_cvt_pk_bf16_f32 v68, v140, v141\n"
"v_cvt_pk_bf16_f32 v69, v142, v143\n"
"v_cvt_pk_bf16_f32 v70, v156, v157\n"
"v_cvt_pk_bf16_f32 v71, v158, v159\n"
"s_nop 0\n"
"v_permlane16_swap_b32 v68, v70\n"
"v_permlane16_swap_b32 v69, v71\n"
"buffer_store_dwordx4 v[68:71], %[vst], s[56:59], s54 offen offset:0 sc1\n"
"s_add_u32 s54, %[o0], 98304\n"
"s_add_u32 s54, s54, %[o1]\n"
"v_cvt_pk_bf16_f32 v72, v172, v173\n"
"v_cvt_pk_bf16_f32 v73, v174, v175\n"
"v_cvt_pk_bf16_f32 v74, v188, v189\n"
"v_cvt_pk_bf16_f32 v75, v190, v191\n"
"s_nop 0\n"
"v_permlane16_swap_b32 v72, v74\n"
"v_permlane16_swap_b32 v73, v75\n"
"buffer_store_dwordx4 v[72:75], %[vst], s[56:59], s54 offen offset:0 sc1\n"
"s_branch END_%=\n"
"NX2_%=:\n"
"s_mov_b32 s64, 0xbfb8aa3b\n"
"s_mov_b32 s65, s64\n"
"s_mov_b32 s66, 1.0\n"
"s_mov_b32 s67, 1.0\n"
"s_add_u32 s54, %[o0], 0\n"
"v_pk_mul_f32 v[52:53], v[128:129], s[64:65]\n"
"v_pk_mul_f32 v[54:55], v[130:131], s[64:65]\n"
"v_exp_f32 v52, v52\n"
"v_exp_f32 v53, v53\n"
"v_exp_f32 v54, v54\n"
"v_exp_f32 v55, v55\n"
"s_nop 0\n"
"v_pk_add_f32 v[52:53], v[52:53], s[66:67]\n"
"v_pk_add_f32 v[54:55], v[54:55], s[66:67]\n"
"v_rcp_f32 v52, v52\n"
"v_rcp_f32 v53, v53\n"
"v_rcp_f32 v54, v54\n"
"v_rcp_f32 v55, v55\n"
"v_pk_mul_f32 v[128:129], v[128:129], v[160:161]\n"
"v_pk_mul_f32 v[130:131], v[130:131], v[162:163]\n"
"v_pk_mul_f32 v[128:129], v[128:129], v[52:53]\n"
"v_pk_mul_f32 v[130:131], v[130:131], v[54:55]\n"
"v_cvt_pk_bf16_f32 v68, v128, v129\n"
"v_cvt_pk_bf16_f32 v69, v130, v131\n"
"v_pk_mul_f32 v[52:53], v[144:145], s[64:65]\n"
"v_pk_mul_f32 v[54:55], v[146:147], s[64:65]\n"
"v_exp_f32 v52, v52\n"
"v_exp_f32 v53, v53\n"
"v_exp_f32 v54, v54\n"
"v_exp_f32 v55, v55\n"
"s_nop 0\n"
"v_pk_add_f32 v[52:53], v[52:53], s[66:67]\n"
"v_pk_add_f32 v[54:55], v[54:55], s[66:67]\n"
"v_rcp_f32 v52, v52\n"
"v_rcp_f32 v53, v53\n"
"v_rcp_f32 v54, v54\n"
"v_rcp_f32 v55, v55\n"
"v_pk_mul_f32 v[144:145], v[144:145], v[176:177]\n"
"v_pk_mul_f32 v[146:147], v[146:147], v[178:179]\n"
"v_pk_mul_f32 v[144:145], v[144:145], v[52:53]\n"
"v_pk_mul_f32 v[146:147], v[146:147], v[54:55]\n"
"v_cvt_pk_bf16_f32 v70, v144, v145\n"
"v_cvt_pk_bf16_f32 v71, v146, v147\n"
"s_nop 0\n"
"v_permlane16_swap_b32 v68, v70\n"
"v_permlane16_swap_b32 v69, v71\n"
"buffer_store_dwordx4 v[68:71], %[vst], s[56:59], s54 offen offset:0 sc1\n"
"s_add_u32 s54, %[o0], 114688\n"
"v_pk_mul_f32 v[52:53], v[132:133], s[64:65]\n"
"v_pk_mul_f32 v[54:55], v[134:135], s[64:65]\n"
"v_exp_f32 v52, v52\n"
"v_exp_f32 v53, v53\n"
"v_exp_f32 v54, v54\n"
"v_exp_f32 v55, v55\n"
"s_nop 0\n"
"v_pk_add_f32 v[52:53], v[52:53], s[66:67]\n"
"v_pk_add_f32 v[54:55], v[54:55], s[66:67]\n"
"v_rcp_f32 v52, v52\n"
"v_rcp_f32 v53, v53\n"
"v_rcp_f32 v54, v54\n"
"v_rcp_f32 v55, v55\n"
"v_pk_mul_f32 v[132:133], v[132:133], v[164:165]\n"
"v_pk_mul_f32 v[134:135], v[134:135], v[166:167]\n"
"v_pk_mul_f32 v[132:133], v[132:133], v[52:53]\n"
"v_pk_mul_f32 v[134:135], v[134:135], v[54:55]\n"
"v_cvt_pk_bf16_f32 v72, v132, v133\n"
"v_cvt_pk_bf16_f32 v73, v134, v135\n"
"v_pk_mul_f32 v[52:53], v[148:149], s[64:65]\n"
"v_pk_mul_f32 v[54:55], v[150:151], s[64:65]\n"
"v_exp_f32 v52, v52\n"
"v_exp_f32 v53, v53\n"
"v_exp_f32 v54, v54\n"
"v_exp_f32 v55, v55\n"
"s_nop 0\n"
"v_pk_add_f32 v[52:53], v[52:53], s[66:67]\n"
"v_pk_add_f32 v[54:55], v[54:55], s[66:67]\n"
"v_rcp_f32 v52, v52\n"
"v_rcp_f32 v53, v53\n"
"v_rcp_f32 v54, v54\n"
"v_rcp_f32 v55, v55\n"
"v_pk_mul_f32 v[148:149], v[148:149], v[180:181]\n"
"v_pk_mul_f32 v[150:151], v[150:151], v[182:183]\n"
"v_pk_mul_f32 v[148:149], v[148:149], v[52:53]\n"
"v_pk_mul_f32 v[150:151], v[150:151], v[54:55]\n"
"v_cvt_pk_bf16_f32 v74, v148, v149\n"
"v_cvt_pk_bf16_f32 v75, v150, v151\n"
"s_nop 0\n"
"v_permlane16_swap_b32 v72, v74\n"
"v_permlane16_swap_b32 v73, v75\n"
"buffer_store_dwordx4 v[72:75], %[vst], s[56:59], s54 offen offset:0 sc1\n"
"s_add_u32 s54, %[o0], 229376\n"
"v_pk_mul_f32 v[52:53], v[136:137], s[64:65]\n"
"v_pk_mul_f32 v[54:55], v[138:139], s[64:65]\n"
"v_exp_f32 v52, v52\n"
"v_exp_f32 v53, v53\n"
"v_exp_f32 v54, v54\n"
"v_exp_f32 v55, v55\n"
"s_nop 0\n"
"v_pk_add_f32 v[52:53], v[52:53], s[66:67]\n"
"v_pk_add_f32 v[54:55], v[54:55], s[66:67]\n"
"v_rcp_f32 v52, v52\n"
"v_rcp_f32 v53, v53\n"
"v_rcp_f32 v54, v54\n"
"v_rcp_f32 v55, v55\n"
"v_pk_mul_f32 v[136:137], v[136:137], v[168:169]\n"
"v_pk_mul_f32 v[138:139], v[138:139], v[170:171]\n"
"v_pk_mul_f32 v[136:137], v[136:137], v[52:53]\n"
"v_pk_mul_f32 v[138:139], v[138:139], v[54:55]\n"
"v_cvt_pk_bf16_f32 v76, v136, v137\n"
"v_cvt_pk_bf16_f32 v77, v138, v139\n"
"v_pk_mul_f32 v[52:53], v[152:153], s[64:65]\n"
"v_pk_mul_f32 v[54:55], v[154:155], s[64:65]\n"
"v_exp_f32 v52, v52\n"
"v_exp_f32 v53, v53\n"
"v_exp_f32 v54, v54\n"
"v_exp_f32 v55, v55\n"
"s_nop 0\n"
"v_pk_add_f32 v[52:53], v[52:53], s[66:67]\n"
"v_pk_add_f32 v[54:55], v[54:55], s[66:67]\n"
"v_rcp_f32 v52, v52\n"
"v_rcp_f32 v53, v53\n"
"v_rcp_f32 v54, v54\n"
"v_rcp_f32 v55, v55\n"
"v_pk_mul_f32 v[152:153], v[152:153], v[184:185]\n"
"v_pk_mul_f32 v[154:155], v[154:155], v[186:187]\n"
"v_pk_mul_f32 v[152:153], v[152:153], v[52:53]\n"
"v_pk_mul_f32 v[154:155], v[154:155], v[54:55]\n"
"v_cvt_pk_bf16_f32 v78, v152, v153\n"
"v_cvt_pk_bf16_f32 v79, v154, v155\n"
"s_nop 0\n"
"v_permlane16_swap_b32 v76, v78\n"
"v_permlane16_swap_b32 v77, v79\n"
"buffer_store_dwordx4 v[76:79], %[vst], s[56:59], s54 offen offset:0 sc1\n"
"s_add_u32 s54, %[o0], 344064\n"
"v_pk_mul_f32 v[52:53], v[140:141], s[64:65]\n"
"v_pk_mul_f32 v[54:55], v[142:143], s[64:65]\n"
"v_exp_f32 v52, v52\n"
"v_exp_f32 v53, v53\n"
"v_exp_f32 v54, v54\n"
"v_exp_f32 v55, v55\n"
"s_nop 0\n"
"v_pk_add_f32 v[52:53], v[52:53], s[66:67]\n"
"v_pk_add_f32 v[54:55], v[54:55], s[66:67]\n"
"v_rcp_f32 v52, v52\n"
"v_rcp_f32 v53, v53\n"
"v_rcp_f32 v54, v54\n"
"v_rcp_f32 v55, v55\n"
"v_pk_mul_f32 v[140:141], v[140:141], v[172:173]\n"
"v_pk_mul_f32 v[142:143], v[142:143], v[174:175]\n"
"v_pk_mul_f32 v[140:141], v[140:141], v[52:53]\n"
"v_pk_mul_f32 v[142:143], v[142:143], v[54:55]\n"
"v_cvt_pk_bf16_f32 v80, v140, v141\n"
"v_cvt_pk_bf16_f32 v81, v142, v143\n"
"v_pk_mul_f32 v[52:53], v[156:157], s[64:65]\n"
"v_pk_mul_f32 v[54:55], v[158:159], s[64:65]\n"
"v_exp_f32 v52, v52\n"
"v_exp_f32 v53, v53\n"
"v_exp_f32 v54, v54\n"
"v_exp_f32 v55, v55\n"
"s_nop 0\n"
"v_pk_add_f32 v[52:53], v[52:53], s[66:67]\n"
"v_pk_add_f32 v[54:55], v[54:55], s[66:67]\n"
"v_rcp_f32 v52, v52\n"
"v_rcp_f32 v53, v53\n"
"v_rcp_f32 v54, v54\n"
"v_rcp_f32 v55, v55\n"
"v_pk_mul_f32 v[156:157], v[156:157], v[188:189]\n"
"v_pk_mul_f32 v[158:159], v[158:159], v[190:191]\n"
"v_pk_mul_f32 v[156:157], v[156:157], v[52:53]\n"
"v_pk_mul_f32 v[158:159], v[158:159], v[54:55]\n"
"v_cvt_pk_bf16_f32 v82, v156, v157\n"
"v_cvt_pk_bf16_f32 v83, v158, v159\n"
"s_nop 0\n"
"v_permlane16_swap_b32 v80, v82\n"
"v_permlane16_swap_b32 v81, v83\n"
"buffer_store_dwordx4 v[80:83], %[vst], s[56:59], s54 offen offset:0 sc1\n"
"END_%=:\n"
"s_waitcnt vmcnt(0)\n"
  :: [ws] "s"(ws), [kind] "s"(kind), [wave] "s"(wave), [aoff] "s"(aoff), [lda] "s"(lda), [bp] "s"(bp), [bn] "s"(bn), [ldb] "s"(ldb), [nk] "s"(nk), [o0] "s"(o0), [o1] "s"(o1),
     [va0] "v"(va0), [vb0] "v"(vb0), [vx0] "v"(vx0), [vw0] "v"(vw0), [vst] "v"(vst)
  : "v24","v25","v26","v27","v28","v29","v30","v31","v32","v33","v34","v35","v36","v37","v38","v39","v40","v41","v42","v43","v44","v45","v46","v47","v48","v49","v50","v51","v52","v53","v54","v55",
"v56","v57","v58","v59","v60","v61","v62","v63","v64","v65","v66","v67","v68","v69","v70","v71","v72","v73","v74","v75","v76","v77","v78","v79","v80","v81","v82","v83","v84","v85","v86","v87",
"v88","v89","v90","v91","v92","v93","v94","v95","v96","v97","v98","v99","v100","v101","v102","v103","v104","v105","v106","v107","v108","v109","v110","v111","v112","v113","v114","v115","v116","v117","v118","v119",
"v120","v121","v122","v123","v124","v125","v126","v127","v128","v129","v130","v131","v132","v133","v134","v135","v136","v137","v138","v139","v140","v141","v142","v143","v144","v145","v146","v147","v148","v149","v150","v151",
"v152","v153","v154","v155","v156","v157","v158","v159","v160","v161","v162","v163","v164","v165","v166","v167","v168","v169","v170","v171","v172","v173","v174","v175","v176","v177","v178","v179","v180","v181","v182","v183",
"v184","v185","v186","v187","v188","v189","v190","v191","v192","v193","v194","v195","v196","v197","v198","v199","v200","v201","v202","v203","v204","v205","v206","v207","v208","v209","v210","v211","v212","v213","v214","v215",
"v216","v217","v218","v219","v220","v221","v222","v223","v224","v225","v226","v227","v228","v229","v230","v231","v232","v233","v234","v235","v236","v237","v238","v239","v240","v241","v242","v243","v244","v245","v246","v247",
"v248","v249","v250","v251","v252","v253","v254","v255","a0","a1","a2","a3","a4","a5","a6","a7","a8","a9","a10","a11","a12","a13","a14","a15","a16","a17","a18","a19","a20","a21","a22","a23",
"a24","a25","a26","a27","a28","a29","a30","a31","a32","a33","a34","a35","a36","a37","a38","a39","a40","a41","a42","a43","a44","a45","a46","a47","a48","a49","a50","a51","a52","a53","a54","a55",
"a56","a57","a58","a59","a60","a61","a62","a63","a64","a65","a66","a67","a68","a69","a70","a71","a72","a73","a74","a75","a76","a77","a78","a79","a80","a81","a82","a83","a84","a85","a86","a87",
"a88","a89","a90","a91","a92","a93","a94","a95","a96","a97","a98","a99","a100","a101","a102","a103","a104","a105","a106","a107","a108","a109","a110","a111","a112","a113","a114","a115","a116","a117","a118","a119",
"a120","a121","a122","a123","a124","a125","a126","a127","a128","a129","a130","a131","a132","a133","a134","a135","a136","a137","a138","a139","a140","a141","a142","a143","a144","a145","a146","a147","a148","a149","a150","a151",
"a152","a153","a154","a155","a156","a157","a158","a159","a160","a161","a162","a163","a164","a165","a166","a167","a168","a169","a170","a171","a172","a173","a174","a175","a176","a177","a178","a179","a180","a181","a182","a183",
"a184","a185","a186","a187","a188","a189","a190","a191","a192","a193","a194","a195","a196","a197","a198","a199","a200","a201","a202","a203","a204","a205","a206","a207","a208","a209","a210","a211","a212","a213","a214","a215",
"a216","a217","a218","a219","a220","a221","a222","a223","a224","a225","a226","a227","a228","a229","a230","a231","a232","a233","a234","a235","a236","a237","a238","a239","a240","a241","a242","a243","a244","a245","a246","a247",
"a248","a249","a250","a251","a252","a253","a254","a255","s36","s37","s38","s39","s40","s41","s42","s43","s44","s45","s46","s47","s48","s49","s50","s51","s52","s53","s54","s55","s56","s57","s58","s59",
"s60","s61","s62","s63","s64","s65","s66","s67","vcc","scc","memory");
}

__device__ __forceinline__ void gemm_X(char* ws, int wave, const GP& P) {
  const unsigned kind = RFL(P.kind), aoff = RFL(P.aoff), lda = RFL(P.lda), bn = RFL(P.bn), ldb = RFL(P.ldb), nk = RFL(P.nk), o0 = RFL(P.o0), o1 = RFL(P.o1);
  const unsigned long long bp = ((unsigned long long)RFL((unsigned)(P.bp >> 32)) << 32) | RFL((unsigned)P.bp);
  const unsigned l = lane_id(), w = wave, wm = w >> 1, wn = w & 1;
  const unsigned i16 = l & 15, g = l >> 4, q = (l >> 2) & 3, p = l & 3, r = l / 8;
  const unsigned c0 = (l & 7) ^ g, va0 = 2048 + 1 * w * lda + (l >> 3) * 64 + (c0 >> 2) * 1024 + (c0 & 3) * 16;
  const unsigned vb0 = (32 * wn + r) * ldb + (((l % 8) ^ (2 * ((r >> 1) & 1))) * 16);
  const unsigned vx0 = wm * 4096 + i16 * 128 + ((g ^ (i16 >> 1)) * 16);
  const unsigned vw0 = (8 * g + q) * 128 + (((2 * wn) ^ ((q >> 1) | ((g & 1) << 1))) * 32) + 8 * p;
  const unsigned gp = ((g & 1) << 1) | (g >> 1);
  const unsigned vst = kind == 0 ? gp * 256 + i16 * 16 : kind == 1 ? l * 16 : i16 * 64 + gp * 16;
  asm volatile(
"s_mov_b64 s[36:37], %[ws]\n"
"s_and_b32 s37, s37, 0xffff\n"
"s_mov_b32 s38, 0xf000000\n"
"s_mov_b32 s39, 0x20000\n"
"s_mov_b64 s[40:41], %[bp]\n"
"s_and_b32 s41, s41, 0xffff\n"
"s_mov_b32 s42, %[bn]\n"
"s_mov_b32 s43, 0x20000\n"
"s_mov_b64 s[56:57], %[ws]\n"
"s_and_b32 s57, s57, 0xffff\n"
"s_mov_b32 s58, 0xf000000\n"
"s_mov_b32 s59, 0x20000\n"
"s_sub_u32 s44, %[aoff], 0x800\n"
"s_mov_b32 s45, 0\n"
"s_mov_b32 s47, %[nk]\n"
"s_lshl_b32 s48, %[ldb], 6\n"
"s_mov_b32 s49, %[wave]\n"
"s_mul_i32 s60, s49, 2048\n"
"s_add_u32 s61, s60, 4096\n"
"s_mul_i32 s62, s49, 4096\n"
"s_add_u32 s62, s62, 8192\n"
"s_add_u32 s63, s62, 4096\n"
"v_mov_b32 v24, %[va0]\n"
"v_xor_b32 v25, 0x400, v24\n"
"v_add_u32 v25, 0x200, v25\n"
"v_subrev_u32 v25, 1024, v25\n"
"s_mul_i32 s55, %[ldb], 8\n"
"v_mov_b32 v32, %[vb0]\n"
"v_add_u32 v33, s55, v32\n"
"v_xor_b32 v33, 64, v33\n"
"v_add_u32 v34, s55, v33\n"
"v_xor_b32 v34, 64, v34\n"
"v_add_u32 v35, s55, v34\n"
"v_xor_b32 v35, 64, v35\n"
"v_subrev_u32 v33, 1024, v33\n"
"v_subrev_u32 v34, 2048, v34\n"
"v_subrev_u32 v35, 3072, v35\n"
"v_mov_b32 v46, %[vx0]\n"
"v_xor_b32 v47, 64, %[vx0]\n"
"v_xor_b32 v48, 0, %[vw0]\n"
"v_xor_b32 v49, 32, %[vw0]\n"
"s_cmp_lg_u32 %[kind], 1\n"
"s_cbranch_scc1 SW_%=\n"
"s_mov_b32 s50, 0\n"
"s_mov_b32 s51, 0\n"
"s_add_u32 m0, s50, s60\n"
"s_nop 0\n"
"buffer_load_dwordx4 v24, s[36:39], s44 offen offset:0 sc1 lds\n"
"buffer_load_dwordx4 v25, s[36:39], s44 offen offset:1024 sc1 lds\n"
"s_add_u32 m0, s50, s62\n"
"s_nop 0\n"
"buffer_load_dwordx4 v32, s[40:43], s45 offen offset:0 lds\n"
"buffer_load_dwordx4 v33, s[40:43], s45 offen offset:1024 lds\n"
"buffer_load_dwordx4 v34, s[40:43], s45 offen offset:2048 lds\n"
"buffer_load_dwordx4 v35, s[40:43], s45 offen offset:3072 lds\n"
"s_add_u32 s44, s44, 2048\n"
"s_add_u32 s45, s45, s48\n"
"s_add_u32 s50, s50, 24576\n"
"s_cmp_eq_u32 s50, 122880\n"
"s_cselect_b32 s50, 0, s50\n"
"v_mov_b64 v[128:129], 0\n"
"v_mov_b64 v[130:131], 0\n"
"v_mov_b64 v[136:137], 0\n"
"v_mov_b64 v[138:139], 0\n"
"v_mov_b64 v[144:145], 0\n"
"v_mov_b64 v[146:147], 0\n"
"v_mov_b64 v[152:153], 0\n"
"v_mov_b64 v[154:155], 0\n"
"v_mov_b64 v[132:133], 0\n"
"v_mov_b64 v[134:135], 0\n"
"v_mov_b64 v[140:141], 0\n"
"v_mov_b64 v[142:143], 0\n"
"v_mov_b64 v[148:149], 0\n"
"v_mov_b64 v[150:151], 0\n"
"v_mov_b64 v[156:157], 0\n"
"v_mov_b64 v[158:159], 0\n"
"v_add_u32 v40, s51, v46\n"
"v_add_u32 v41, s51, v47\n"
"v_add_u32 v42, s51, v48\n"
"v_add_u32 v43, s51, v49\n"
"s_add_u32 m0, s50, s60\n"
"s_nop 0\n"
"buffer_load_dwordx4 v24, s[36:39], s44 offen offset:0 sc1 lds\n"
"buffer_load_dwordx4 v25, s[36:39], s44 offen offset:1024 sc1 lds\n"
"s_add_u32 m0, s50, s62\n"
"s_nop 0\n"
"buffer_load_dwordx4 v32, s[40:43], s45 offen offset:0 lds\n"
"buffer_load_dwordx4 v33, s[40:43], s45 offen offset:1024 lds\n"
"buffer_load_dwordx4 v34, s[40:43], s45 offen offset:2048 lds\n"
"buffer_load_dwordx4 v35, s[40:43], s45 offen offset:3072 lds\n"
"s_add_u32 s44, s44, 2048\n"
"s_add_u32 s45, s45, s48\n"
"s_add_u32 s50, s50, 24576\n"
"s_cmp_eq_u32 s50, 122880\n"
"s_cselect_b32 s50, 0, s50\n"
"s_add_u32 m0, s50, s60\n"
"s_nop 0\n"
"buffer_load_dwordx4 v24, s[36:39], s44 offen offset:0 sc1 lds\n"
"buffer_load_dwordx4 v25, s[36:39], s44 offen offset:1024 sc1 lds\n"
"s_add_u32 m0, s50, s62\n"
"s_nop 0\n"
"buffer_load_dwordx4 v32, s[40:43], s45 offen offset:0 lds\n"
"buffer_load_dwordx4 v33, s[40:43], s45 offen offset:1024 lds\n"
"buffer_load_dwordx4 v34, s[40:43], s45 offen offset:2048 lds\n"
"buffer_load_dwordx4 v35, s[40:43], s45 offen offset:3072 lds\n"
"s_add_u32 s44, s44, 2048\n"
"s_add_u32 s45, s45, s48\n"
"s_add_u32 s50, s50, 24576\n"
"s_cmp_eq_u32 s50, 122880\n"
"s_cselect_b32 s50, 0, s50\n"
"s_add_u32 m0, s50, s60\n"
"s_nop 0\n"
"buffer_load_dwordx4 v24, s[36:39], s44 offen offset:0 sc1 lds\n"
"buffer_load_dwordx4 v25, s[36:39], s44 offen offset:1024 sc1 lds\n"
"s_add_u32 m0, s50, s62\n"
"s_nop 0\n"
"buffer_load_dwordx4 v32, s[40:43], s45 offen offset:0 lds\n"
"buffer_load_dwordx4 v33, s[40:43], s45 offen offset:1024 lds\n"
"buffer_load_dwordx4 v34, s[40:43], s45 offen offset:2048 lds\n"
"buffer_load_dwordx4 v35, s[40:43], s45 offen offset:3072 lds\n"
"s_add_u32 s44, s44, 2048\n"
"s_add_u32 s45, s45, s48\n"
"s_add_u32 s50, s50, 24576\n"
"s_cmp_eq_u32 s50, 122880\n"
"s_cselect_b32 s50, 0, s50\n"
"s_add_u32 m0, s50, s60\n"
"s_nop 0\n"
"buffer_load_dwordx4 v24, s[36:39], s44 offen offset:0 sc1 lds\n"
"buffer_load_dwordx4 v25, s[36:39], s44 offen offset:1024 sc1 lds\n"
"s_add_u32 s44, s44, 2048\n"
"s_waitcnt vmcnt(20)\n"
"s_barrier\n"
"ds_read_b64_tr_b16 a[160:161], v42 offset:8192\n"
"ds_read_b64_tr_b16 a[162:163], v42 offset:8704\n"
"ds_read_b128 a[128:131], v40 offset:0\n"
"ds_read_b128 a[132:135], v40 offset:2048\n"
"ds_read_b64_tr_b16 a[164:165], v43 offset:8192\n"
"ds_read_b64_tr_b16 a[166:167], v43 offset:8704\n"
"ds_read_b64_tr_b16 a[168:169], v42 offset:16384\n"
"ds_read_b64_tr_b16 a[170:171], v42 offset:16896\n"
"ds_read_b64_tr_b16 a[172:173], v43 offset:16384\n"
"ds_read_b64_tr_b16 a[174:175], v43 offset:16896\n"
"s_sub_u32 s46, s47, 5\n"
"s_waitcnt lgkmcnt(0)\n"
"KL_u_%=:\n"
"v_mfma_f32_16x16x32_bf16 v[128:131], a[128:131], a[160:163], v[128:131]\n"
"ds_read_b64_tr_b16 a[224:225], v42 offset:12288\n"
"ds_read_b64_tr_b16 a[226:227], v42 offset:12800\n"
"s_add_u32 m0, s50, s62\n"
"v_mfma_f32_16x16x32_bf16 v[132:135], a[132:135], a[160:163], v[132:135]\n"
"ds_read_b128 a[192:195], v41 offset:0\n"
"ds_read_b128 a[196:199], v41 offset:2048\n"
"buffer_load_dwordx4 v32, s[40:43], s45 offen offset:0 lds\n"
"v_mfma_f32_16x16x32_bf16 v[136:139], a[128:131], a[164:167], v[136:139]\n"
"ds_read_b64_tr_b16 a[228:229], v43 offset:12288\n"
"ds_read_b64_tr_b16 a[230:231], v43 offset:12800\n"
"v_mfma_f32_16x16x32_bf16 v[140:143], a[132:135], a[164:167], v[140:143]\n"
"ds_read_b64_tr_b16 a[232:233], v42 offset:20480\n"
"ds_read_b64_tr_b16 a[234:235], v42 offset:20992\n"
"buffer_load_dwordx4 v33, s[40:43], s45 offen offset:1024 lds\n"
"v_mfma_f32_16x16x32_bf16 v[144:147], a[128:131], a[168:171], v[144:147]\n"
"ds_read_b64_tr_b16 a[236:237], v43 offset:20480\n"
"ds_read_b64_tr_b16 a[238:239], v43 offset:20992\n"
"v_mfma_f32_16x16x32_bf16 v[148:151], a[132:135], a[168:171], v[148:151]\n"
"buffer_load_dwordx4 v34, s[40:43], s45 offen offset:2048 lds\n"
"v_mfma_f32_16x16x32_bf16 v[152:155], a[128:131], a[172:175], v[152:155]\n"
"s_add_u32 s51, s51, 24576\n"
"s_cmp_eq_u32 s51, 122880\n"
"s_cselect_b32 s51, 0, s51\n"
"v_mfma_f32_16x16x32_bf16 v[156:159], a[132:135], a[172:175], v[156:159]\n"
"buffer_load_dwordx4 v35, s[40:43], s45 offen offset:3072 lds\n"
"s_add_u32 s45, s45, s48\n"
"s_add_u32 s50, s50, 24576\n"
"s_cmp_eq_u32 s50, 122880\n"
"s_cselect_b32 s50, 0, s50\n"
"s_waitcnt lgkmcnt(0) vmcnt(18)\n"
"s_barrier\n"
"v_mfma_f32_16x16x32_bf16 v[128:131], a[192:195], a[224:227], v[128:131]\n"
"v_add_u32 v40, s51, v46\n"
"v_add_u32 v41, s51, v47\n"
"s_add_u32 m0, s50, s60\n"
"v_mfma_f32_16x16x32_bf16 v[132:135], a[196:199], a[224:227], v[132:135]\n"
"v_add_u32 v42, s51, v48\n"
"v_add_u32 v43, s51, v49\n"
"buffer_load_dwordx4 v24, s[36:39], s44 offen offset:0 sc1 lds\n"
"v_mfma_f32_16x16x32_bf16 v[136:139], a[192:195], a[228:231], v[136:139]\n"
"ds_read_b64_tr_b16 a[160:161], v42 offset:8192\n"
"ds_read_b64_tr_b16 a[162:163], v42 offset:8704\n"
"ds_read_b128 a[128:131], v40 offset:0\n"
"v_mfma_f32_16x16x32_bf16 v[140:143], a[196:199], a[228:231], v[140:143]\n"
"ds_read_b128 a[132:135], v40 offset:2048\n"
"ds_read_b64_tr_b16 a[164:165], v43 offset:8192\n"
"v_mfma_f32_16x16x32_bf16 v[144:147], a[192:195], a[232:235], v[144:147]\n"
"ds_read_b64_tr_b16 a[166:167], v43 offset:8704\n"
"ds_read_b64_tr_b16 a[168:169], v42 offset:16384\n"
"ds_read_b64_tr_b16 a[170:171], v42 offset:16896\n"
"v_mfma_f32_16x16x32_bf16 v[148:151], a[196:199], a[232:235], v[148:151]\n"
"ds_read_b64_tr_b16 a[172:173], v43 offset:16384\n"
"ds_read_b64_tr_b16 a[174:175], v43 offset:16896\n"
"buffer_load_dwordx4 v25, s[36:39], s44 offen offset:1024 sc1 lds\n"
"v_mfma_f32_16x16x32_bf16 v[152:155], a[192:195], a[236:239], v[152:155]\n"
"s_add_u32 s44, s44, 2048\n"
"v_mfma_f32_16x16x32_bf16 v[156:159], a[196:199], a[236:239], v[156:159]\n"
"s_sub_u32 s46, s46, 1\n"
"s_cmp_lg_u32 s46, 0\n"
"s_waitcnt lgkmcnt(0)\n"
"s_cbranch_scc1 KL_u_%=\n"
"v_mfma_f32_16x16x32_bf16 v[128:131], a[128:131], a[160:163], v[128:131]\n"
"ds_read_b64_tr_b16 a[224:225], v42 offset:12288\n"
"ds_read_b64_tr_b16 a[226:227], v42 offset:12800\n"
"s_add_u32 m0, s50, s62\n"
"v_mfma_f32_16x16x32_bf16 v[132:135], a[132:135], a[160:163], v[132:135]\n"
"ds_read_b128 a[192:195], v41 offset:0\n"
"ds_read_b128 a[196:199], v41 offset:2048\n"
"buffer_load_dwordx4 v32, s[40:43], s45 offen offset:0 lds\n"
"v_mfma_f32_16x16x32_bf16 v[136:139], a[128:131], a[164:167], v[136:139]\n"
"ds_read_b64_tr_b16 a[228:229], v43 offset:12288\n"
"ds_read_b64_tr_b16 a[230:231], v43 offset:12800\n"
"v_mfma_f32_16x16x32_bf16 v[140:143], a[132:135], a[164:167], v[140:143]\n"
"ds_read_b64_tr_b16 a[232:233], v42 offset:20480\n"
"ds_read_b64_tr_b16 a[234:235], v42 offset:20992\n"
"buffer_load_dwordx4 v33, s[40:43], s45 offen offset:1024 lds\n"
"v_mfma_f32_16x16x32_bf16 v[144:147], a[128:131], a[168:171], v[144:147]\n"
"ds_read_b64_tr_b16 a[236:237], v43 offset:20480\n"
"ds_read_b64_tr_b16 a[238:239], v43 offset:20992\n"
"v_mfma_f32_16x16x32_bf16 v[148:151], a[132:135], a[168:171], v[148:151]\n"
"buffer_load_dwordx4 v34, s[40:43], s45 offen offset:2048 lds\n"
"v_mfma_f32_16x16x32_bf16 v[152:155], a[128:131], a[172:175], v[152:155]\n"
"s_add_u32 s51, s51, 24576\n"
"s_cmp_eq_u32 s51, 122880\n"
"s_cselect_b32 s51, 0, s51\n"
"v_mfma_f32_16x16x32_bf16 v[156:159], a[132:135], a[172:175], v[156:159]\n"
"buffer_load_dwordx4 v35, s[40:43], s45 offen offset:3072 lds\n"
"s_add_u32 s45, s45, s48\n"
"s_add_u32 s50, s50, 24576\n"
"s_cmp_eq_u32 s50, 122880\n"
"s_cselect_b32 s50, 0, s50\n"
"s_waitcnt lgkmcnt(0) vmcnt(18)\n"
"s_barrier\n"
"v_mfma_f32_16x16x32_bf16 v[128:131], a[192:195], a[224:227], v[128:131]\n"
"v_add_u32 v40, s51, v46\n"
"v_add_u32 v41, s51, v47\n"
"v_mfma_f32_16x16x32_bf16 v[132:135], a[196:199], a[224:227], v[132:135]\n"
"v_add_u32 v42, s51, v48\n"
"v_add_u32 v43, s51, v49\n"
"v_mfma_f32_16x16x32_bf16 v[136:139], a[192:195], a[228:231], v[136:139]\n"
"ds_read_b64_tr_b16 a[160:161], v42 offset:8192\n"
"ds_read_b64_tr_b16 a[162:163], v42 offset:8704\n"
"ds_read_b128 a[128:131], v40 offset:0\n"
"v_mfma_f32_16x16x32_bf16 v[140:143], a[196:199], a[228:231], v[140:143]\n"
"ds_read_b128 a[132:135], v40 offset:2048\n"
"ds_read_b64_tr_b16 a[164:165], v43 offset:8192\n"
"v_mfma_f32_16x16x32_bf16 v[144:147], a[192:195], a[232:235], v[144:147]\n"
"ds_read_b64_tr_b16 a[166:167], v43 offset:8704\n"
"ds_read_b64_tr_b16 a[168:169], v42 offset:16384\n"
"ds_read_b64_tr_b16 a[170:171], v42 offset:16896\n"
"v_mfma_f32_16x16x32_bf16 v[148:151], a[196:199], a[232:235], v[148:151]\n"
"ds_read_b64_tr_b16 a[172:173], v43 offset:16384\n"
"ds_read_b64_tr_b16 a[174:175], v43 offset:16896\n"
"v_mfma_f32_16x16x32_bf16 v[152:155], a[192:195], a[236:239], v[152:155]\n"
"v_mfma_f32_16x16x32_bf16 v[156:159], a[196:199], a[236:239], v[156:159]\n"
"s_waitcnt lgkmcnt(0)\n"
"v_mfma_f32_16x16x32_bf16 v[128:131], a[128:131], a[160:163], v[128:131]\n"
"ds_read_b64_tr_b16 a[224:225], v42 offset:12288\n"
"ds_read_b64_tr_b16 a[226:227], v42 offset:12800\n"
"v_mfma_f32_16x16x32_bf16 v[132:135], a[132:135], a[160:163], v[132:135]\n"
"ds_read_b128 a[192:195], v41 offset:0\n"
"ds_read_b128 a[196:199], v41 offset:2048\n"
"v_mfma_f32_16x16x32_bf16 v[136:139], a[128:131], a[164:167], v[136:139]\n"
"ds_read_b64_tr_b16 a[228:229], v43 offset:12288\n"
"ds_read_b64_tr_b16 a[230:231], v43 offset:12800\n"
"v_mfma_f32_16x16x32_bf16 v[140:143], a[132:135], a[164:167], v[140:143]\n"
"ds_read_b64_tr_b16 a[232:233], v42 offset:20480\n"
"ds_read_b64_tr_b16 a[234:235], v42 offset:20992\n"
"v_mfma_f32_16x16x32_bf16 v[144:147], a[128:131], a[168:171], v[144:147]\n"
"ds_read_b64_tr_b16 a[236:237], v43 offset:20480\n"
"ds_read_b64_tr_b16 a[238:239], v43 offset:20992\n"
"v_mfma_f32_16x16x32_bf16 v[148:151], a[132:135], a[168:171], v[148:151]\n"
"v_mfma_f32_16x16x32_bf16 v[152:155], a[128:131], a[172:175], v[152:155]\n"
"s_add_u32 s51, s51, 24576\n"
"s_cmp_eq_u32 s51, 122880\n"
"s_cselect_b32 s51, 0, s51\n"
"v_mfma_f32_16x16x32_bf16 v[156:159], a[132:135], a[172:175], v[156:159]\n"
"s_waitcnt lgkmcnt(0) vmcnt(12)\n"
"s_barrier\n"
"v_mfma_f32_16x16x32_bf16 v[128:131], a[192:195], a[224:227], v[128:131]\n"
"v_add_u32 v40, s51, v46\n"
"v_add_u32 v41, s51, v47\n"
"v_mfma_f32_16x16x32_bf16 v[132:135], a[196:199], a[224:227], v[132:135]\n"
"v_add_u32 v42, s51, v48\n"
"v_add_u32 v43, s51, v49\n"
"v_mfma_f32_16x16x32_bf16 v[136:139], a[192:195], a[228:231], v[136:139]\n"
"ds_read_b64_tr_b16 a[160:161], v42 offset:8192\n"
"ds_read_b64_tr_b16 a[162:163], v42 offset:8704\n"
"ds_read_b128 a[128:131], v40 offset:0\n"
"v_mfma_f32_16x16x32_bf16 v[140:143], a[196:199], a[228:231], v[140:143]\n"
"ds_read_b128 a[132:135], v40 offset:2048\n"
"ds_read_b64_tr_b16 a[164:165], v43 offset:8192\n"
"v_mfma_f32_16x16x32_bf16 v[144:147], a[192:195], a[232:235], v[144:147]\n"
"ds_read_b64_tr_b16 a[166:167], v43 offset:8704\n"
"ds_read_b64_tr_b16 a[168:169], v42 offset:16384\n"
"ds_read_b64_tr_b16 a[170:171], v42 offset:16896\n"
"v_mfma_f32_16x16x32_bf16 v[148:151], a[196:199], a[232:235], v[148:151]\n"
"ds_read_b64_tr_b16 a[172:173], v43 offset:16384\n"
"ds_read_b64_tr_b16 a[174:175], v43 offset:16896\n"
"v_mfma_f32_16x16x32_bf16 v[152:155], a[192:195], a[236:239], v[152:155]\n"
"v_mfma_f32_16x16x32_bf16 v[156:159], a[196:199], a[236:239], v[156:159]\n"
"s_waitcnt lgkmcnt(0)\n"
"v_mfma_f32_16x16x32_bf16 v[128:131], a[128:131], a[160:163], v[128:131]\n"
"ds_read_b64_tr_b16 a[224:225], v42 offset:12288\n"
"ds_read_b64_tr_b16 a[226:227], v42 offset:12800\n"
"v_mfma_f32_16x16x32_bf16 v[132:135], a[132:135], a[160:163], v[132:135]\n"
"ds_read_b128 a[192:195], v41 offset:0\n"
"ds_read_b128 a[196:199], v41 offset:2048\n"
"v_mfma_f32_16x16x32_bf16 v[136:139], a[128:131], a[164:167], v[136:139]\n"
"ds_read_b64_tr_b16 a[228:229], v43 offset:12288\n"
"ds_read_b64_tr_b16 a[230:231], v43 offset:12800\n"
"v_mfma_f32_16x16x32_bf16 v[140:143], a[132:135], a[164:167], v[140:143]\n"
"ds_read_b64_tr_b16 a[232:233], v42 offset:20480\n"
"ds_read_b64_tr_b16 a[234:235], v42 offset:20992\n"
"v_mfma_f32_16x16x32_bf16 v[144:147], a[128:131], a[168:171], v[144:147]\n"
"ds_read_b64_tr_b16 a[236:237], v43 offset:20480\n"
"ds_read_b64_tr_b16 a[238:239], v43 offset:20992\n"
"v_mfma_f32_16x16x32_bf16 v[148:151], a[132:135], a[168:171], v[148:151]\n"
"v_mfma_f32_16x16x32_bf16 v[152:155], a[128:131], a[172:175], v[152:155]\n"
"s_add_u32 s51, s51, 24576\n"
"s_cmp_eq_u32 s51, 122880\n"
"s_cselect_b32 s51, 0, s51\n"
"v_mfma_f32_16x16x32_bf16 v[156:159], a[132:135], a[172:175], v[156:159]\n"
"s_waitcnt lgkmcnt(0) vmcnt(6)\n"
"s_barrier\n"
"v_mfma_f32_16x16x32_bf16 v[128:131], a[192:195], a[224:227], v[128:131]\n"
"v_add_u32 v40, s51, v46\n"
"v_add_u32 v41, s51, v47\n"
"v_mfma_f32_16x16x32_bf16 v[132:135], a[196:199], a[224:227], v[132:135]\n"
"v_add_u32 v42, s51, v48\n"
"v_add_u32 v43, s51, v49\n"
"v_mfma_f32_16x16x32_bf16 v[136:139], a[192:195], a[228:231], v[136:139]\n"
"ds_read_b64_tr_b16 a[160:161], v42 offset:8192\n"
"ds_read_b64_tr_b16 a[162:163], v42 offset:8704\n"
"ds_read_b128 a[128:131], v40 offset:0\n"
"v_mfma_f32_16x16x32_bf16 v[140:143], a[196:199], a[228:231], v[140:143]\n"
"ds_read_b128 a[132:135], v40 offset:2048\n"
"ds_read_b64_tr_b16 a[164:165], v43 offset:8192\n"
"v_mfma_f32_16x16x32_bf16 v[144:147], a[192:195], a[232:235], v[144:147]\n"
"ds_read_b64_tr_b16 a[166:167], v43 offset:8704\n"
"ds_read_b64_tr_b16 a[168:169], v42 offset:16384\n"
"ds_read_b64_tr_b16 a[170:171], v42 offset:16896\n"
"v_mfma_f32_16x16x32_bf16 v[148:151], a[196:199], a[232:235], v[148:151]\n"
"ds_read_b64_tr_b16 a[172:173], v43 offset:16384\n"
"ds_read_b64_tr_b16 a[174:175], v43 offset:16896\n"
"v_mfma_f32_16x16x32_bf16 v[152:155], a[192:195], a[236:239], v[152:155]\n"
"v_mfma_f32_16x16x32_bf16 v[156:159], a[196:199], a[236:239], v[156:159]\n"
"s_waitcnt lgkmcnt(0)\n"
"v_mfma_f32_16x16x32_bf16 v[128:131], a[128:131], a[160:163], v[128:131]\n"
"ds_read_b64_tr_b16 a[224:225], v42 offset:12288\n"
"ds_read_b64_tr_b16 a[226:227], v42 offset:12800\n"
"v_mfma_f32_16x16x32_bf16 v[132:135], a[132:135], a[160:163], v[132:135]\n"
"ds_read_b128 a[192:195], v41 offset:0\n"
"ds_read_b128 a[196:199], v41 offset:2048\n"
"v_mfma_f32_16x16x32_bf16 v[136:139], a[128:131], a[164:167], v[136:139]\n"
"ds_read_b64_tr_b16 a[228:229], v43 offset:12288\n"
"ds_read_b64_tr_b16 a[230:231], v43 offset:12800\n"
"v_mfma_f32_16x16x32_bf16 v[140:143], a[132:135], a[164:167], v[140:143]\n"
"ds_read_b64_tr_b16 a[232:233], v42 offset:20480\n"
"ds_read_b64_tr_b16 a[234:235], v42 offset:20992\n"
"v_mfma_f32_16x16x32_bf16 v[144:147], a[128:131], a[168:171], v[144:147]\n"
"ds_read_b64_tr_b16 a[236:237], v43 offset:20480\n"
"ds_read_b64_tr_b16 a[238:239], v43 offset:20992\n"
"v_mfma_f32_16x16x32_bf16 v[148:151], a[132:135], a[168:171], v[148:151]\n"
"v_mfma_f32_16x16x32_bf16 v[152:155], a[128:131], a[172:175], v[152:155]\n"
"s_add_u32 s51, s51, 24576\n"
"s_cmp_eq_u32 s51, 122880\n"
"s_cselect_b32 s51, 0, s51\n"
"v_mfma_f32_16x16x32_bf16 v[156:159], a[132:135], a[172:175], v[156:159]\n"
"s_waitcnt lgkmcnt(0) vmcnt(0)\n"
"s_barrier\n"
"v_mfma_f32_16x16x32_bf16 v[128:131], a[192:195], a[224:227], v[128:131]\n"
"v_add_u32 v40, s51, v46\n"
"v_add_u32 v41, s51, v47\n"
"v_mfma_f32_16x16x32_bf16 v[132:135], a[196:199], a[224:227], v[132:135]\n"
"v_add_u32 v42, s51, v48\n"
"v_add_u32 v43, s51, v49\n"
"v_mfma_f32_16x16x32_bf16 v[136:139], a[192:195], a[228:231], v[136:139]\n"
"ds_read_b64_tr_b16 a[160:161], v42 offset:8192\n"
"ds_read_b64_tr_b16 a[162:163], v42 offset:8704\n"
"ds_read_b128 a[128:131], v40 offset:0\n"
"v_mfma_f32_16x16x32_bf16 v[140:143], a[196:199], a[228:231], v[140:143]\n"
"ds_read_b128 a[132:135], v40 offset:2048\n"
"ds_read_b64_tr_b16 a[164:165], v43 offset:8192\n"
"v_mfma_f32_16x16x32_bf16 v[144:147], a[192:195], a[232:235], v[144:147]\n"
"ds_read_b64_tr_b16 a[166:167], v43 offset:8704\n"
"ds_read_b64_tr_b16 a[168:169], v42 offset:16384\n"
"ds_read_b64_tr_b16 a[170:171], v42 offset:16896\n"
"v_mfma_f32_16x16x32_bf16 v[148:151], a[196:199], a[232:235], v[148:151]\n"
"ds_read_b64_tr_b16 a[172:173], v43 offset:16384\n"
"ds_read_b64_tr_b16 a[174:175], v43 offset:16896\n"
"v_mfma_f32_16x16x32_bf16 v[152:155], a[192:195], a[236:239], v[152:155]\n"
"v_mfma_f32_16x16x32_bf16 v[156:159], a[196:199], a[236:239], v[156:159]\n"
"s_waitcnt lgkmcnt(0)\n"
"v_mfma_f32_16x16x32_bf16 v[128:131], a[128:131], a[160:163], v[128:131]\n"
"ds_read_b64_tr_b16 a[224:225], v42 offset:12288\n"
"ds_read_b64_tr_b16 a[226:227], v42 offset:12800\n"
"v_mfma_f32_16x16x32_bf16 v[132:135], a[132:135], a[160:163], v[132:135]\n"
"ds_read_b128 a[192:195], v41 offset:0\n"
"ds_read_b128 a[196:199], v41 offset:2048\n"
"v_mfma_f32_16x16x32_bf16 v[136:139], a[128:131], a[164:167], v[136:139]\n"
"ds_read_b64_tr_b16 a[228:229], v43 offset:12288\n"
"ds_read_b64_tr_b16 a[230:231], v43 offset:12800\n"
"v_mfma_f32_16x16x32_bf16 v[140:143], a[132:135], a[164:167], v[140:143]\n"
"ds_read_b64_tr_b16 a[232:233], v42 offset:20480\n"
"ds_read_b64_tr_b16 a[234:235], v42 offset:20992\n"
"v_mfma_f32_16x16x32_bf16 v[144:147], a[128:131], a[168:171], v[144:147]\n"
"ds_read_b64_tr_b16 a[236:237], v43 offset:20480\n"
"ds_read_b64_tr_b16 a[238:239], v43 offset:20992\n"
"v_mfma_f32_16x16x32_bf16 v[148:151], a[132:135], a[168:171], v[148:151]\n"
"v_mfma_f32_16x16x32_bf16 v[152:155], a[128:131], a[172:175], v[152:155]\n"
"s_add_u32 s51, s51, 24576\n"
"s_cmp_eq_u32 s51, 122880\n"
"s_cselect_b32 s51, 0, s51\n"
"v_mfma_f32_16x16x32_bf16 v[156:159], a[132:135], a[172:175], v[156:159]\n"
"s_waitcnt lgkmcnt(0)\n"
"v_mfma_f32_16x16x32_bf16 v[128:131], a[192:195], a[224:227], v[128:131]\n"
"v_mfma_f32_16x16x32_bf16 v[132:135], a[196:199], a[224:227], v[132:135]\n"
"v_mfma_f32_16x16x32_bf16 v[136:139], a[192:195], a[228:231], v[136:139]\n"
"v_mfma_f32_16x16x32_bf16 v[140:143], a[196:199], a[228:231], v[140:143]\n"
"v_mfma_f32_16x16x32_bf16 v[144:147], a[192:195], a[232:235], v[144:147]\n"
"v_mfma_f32_16x16x32_bf16 v[148:151], a[196:199], a[232:235], v[148:151]\n"
"v_mfma_f32_16x16x32_bf16 v[152:155], a[192:195], a[236:239], v[152:155]\n"
"v_mfma_f32_16x16x32_bf16 v[156:159], a[196:199], a[236:239], v[156:159]\n"
"s_barrier\n"
"s_add_u32 s54, %[o0], 0\n"
"v_cvt_pk_bf16_f32 v60, v128, v129\n"
"v_cvt_pk_bf16_f32 v61, v130, v131\n"
"v_cvt_pk_bf16_f32 v62, v132, v133\n"
"v_cvt_pk_bf16_f32 v63, v134, v135\n"
"buffer_store_dwordx4 v[60:63], %[vst], s[56:59], s54 offen offset:0 sc1\n"
"v_cvt_pk_bf16_f32 v64, v136, v137\n"
"v_cvt_pk_bf16_f32 v65, v138, v139\n"
"v_cvt_pk_bf16_f32 v66, v140, v141\n"
"v_cvt_pk_bf16_f32 v67, v142, v143\n"
"buffer_store_dwordx4 v[64:67], %[vst], s[56:59], s54 offen offset:1024 sc1\n"
"s_add_u32 s54, %[o0], 0\n"
"s_add_u32 s54, s54, %[o1]\n"
"v_cvt_pk_bf16_f32 v68, v144, v145\n"
"v_cvt_pk_bf16_f32 v69, v146, v147\n"
"v_cvt_pk_bf16_f32 v70, v148, v149\n"
"v_cvt_pk_bf16_f32 v71, v150, v151\n"
"buffer_store_dwordx4 v[68:71], %[vst], s[56:59], s54 offen offset:0 sc1\n"
"v_cvt_pk_bf16_f32 v72, v152, v153\n"
"v_cvt_pk_bf16_f32 v73, v154, v155\n"
"v_cvt_pk_bf16_f32 v74, v156, v157\n"
"v_cvt_pk_bf16_f32 v75, v158, v159\n"
"buffer_store_dwordx4 v[72:75], %[vst], s[56:59], s54 offen offset:1024 sc1\n"
"s_branch END_%=\n"
"SW_%=:\n"
"s_mov_b32 s50, 0\n"
"s_mov_b32 s51, 0\n"
"s_add_u32 m0, s50, s60\n"
"s_nop 0\n"
"buffer_load_dwordx4 v24, s[36:39], s44 offen offset:0 sc1 lds\n"
"buffer_load_dwordx4 v25, s[36:39], s44 offen offset:1024 sc1 lds\n"
"s_add_u32 m0, s50, s62\n"
"s_nop 0\n"
"buffer_load_dwordx4 v32, s[40:43], s45 offen offset:0 lds\n"
"buffer_load_dwordx4 v33, s[40:43], s45 offen offset:1024 lds\n"
"buffer_load_dwordx4 v34, s[40:43], s45 offen offset:2048 lds\n"
"buffer_load_dwordx4 v35, s[40:43], s45 offen offset:3072 lds\n"
"s_add_u32 s44, s44, 2048\n"
"s_add_u32 s45, s45, s48\n"
"s_add_u32 s50, s50, 24576\n"
"s_cmp_eq_u32 s50, 122880\n"
"s_cselect_b32 s50, 0, s50\n"
"v_mov_b64 v[128:129], 0\n"
"v_mov_b64 v[130:131], 0\n"
"v_mov_b64 v[136:137], 0\n"
"v_mov_b64 v[138:139], 0\n"
"v_mov_b64 v[144:145], 0\n"
"v_mov_b64 v[146:147], 0\n"
"v_mov_b64 v[152:153], 0\n"
"v_mov_b64 v[154:155], 0\n"
"v_mov_b64 v[132:133], 0\n"
"v_mov_b64 v[134:135], 0\n"
"v_mov_b64 v[140:141], 0\n"
"v_mov_b64 v[142:143], 0\n"
"v_mov_b64 v[148:149], 0\n"
"v_mov_b64 v[150:151], 0\n"
"v_mov_b64 v[156:157], 0\n"
"v_mov_b64 v[158:159], 0\n"
"v_add_u32 v40, s51, v46\n"
"v_add_u32 v41, s51, v47\n"
"v_add_u32 v42, s51, v48\n"
"v_add_u32 v43, s51, v49\n"
"s_add_u32 m0, s50, s60\n"
"s_nop 0\n"
"buffer_load_dwordx4 v24, s[36:39], s44 offen offset:0 sc1 lds\n"
"buffer_load_dwordx4 v25, s[36:39], s44 offen offset:1024 sc1 lds\n"
"s_add_u32 m0, s50, s62\n"
"s_nop 0\n"
"buffer_load_dwordx4 v32, s[40:43], s45 offen offset:0 lds\n"
"buffer_load_dwordx4 v33, s[40:43], s45 offen offset:1024 lds\n"
"buffer_load_dwordx4 v34, s[40:43], s45 offen offset:2048 lds\n"
"buffer_load_dwordx4 v35, s[40:43], s45 offen offset:3072 lds\n"
"s_add_u32 s44, s44, 2048\n"
"s_add_u32 s45, s45, s48\n"
"s_add_u32 s50, s50, 24576\n"
"s_cmp_eq_u32 s50, 122880\n"
"s_cselect_b32 s50, 0, s50\n"
"s_add_u32 m0, s50, s60\n"
"s_nop 0\n"
"buffer_load_dwordx4 v24, s[36:39], s44 offen offset:0 sc1 lds\n"
"buffer_load_dwordx4 v25, s[36:39], s44 offen offset:1024 sc1 lds\n"
"s_add_u32 m0, s50, s62\n"
"s_nop 0\n"
"buffer_load_dwordx4 v32, s[40:43], s45 offen offset:0 lds\n"
"buffer_load_dwordx4 v33, s[40:43], s45 offen offset:1024 lds\n"
"buffer_load_dwordx4 v34, s[40:43], s45 offen offset:2048 lds\n"
"buffer_load_dwordx4 v35, s[40:43], s45 offen offset:3072 lds\n"
"s_add_u32 s44, s44, 2048\n"
"s_add_u32 s45, s45, s48\n"
"s_add_u32 s50, s50, 24576\n"
"s_cmp_eq_u32 s50, 122880\n"
"s_cselect_b32 s50, 0, s50\n"
"s_add_u32 m0, s50, s60\n"
"s_nop 0\n"
"buffer_load_dwordx4 v24, s[36:39], s44 offen offset:0 sc1 lds\n"
"buffer_load_dwordx4 v25, s[36:39], s44 offen offset:1024 sc1 lds\n"
"s_add_u32 m0, s50, s62\n"
"s_nop 0\n"
"buffer_load_dwordx4 v32, s[40:43], s45 offen offset:0 lds\n"
"buffer_load_dwordx4 v33, s[40:43], s45 offen offset:1024 lds\n"
"buffer_load_dwordx4 v34, s[40:43], s45 offen offset:2048 lds\n"
"buffer_load_dwordx4 v35, s[40:43], s45 offen offset:3072 lds\n"
"s_add_u32 s44, s44, 2048\n"
"s_add_u32 s45, s45, s48\n"
"s_add_u32 s50, s50, 24576\n"
"s_cmp_eq_u32 s50, 122880\n"
"s_cselect_b32 s50, 0, s50\n"
"s_add_u32 m0, s50, s60\n"
"s_nop 0\n"
"buffer_load_dwordx4 v24, s[36:39], s44 offen offset:0 sc1 lds\n"
"buffer_load_dwordx4 v25, s[36:39], s44 offen offset:1024 sc1 lds\n"
"s_add_u32 s44, s44, 2048\n"
"s_waitcnt vmcnt(20)\n"
"s_barrier\n"
"ds_read_b64_tr_b16 a[160:161], v42 offset:8192\n"
"ds_read_b64_tr_b16 a[162:163], v42 offset:8704\n"
"ds_read_b128 a[128:131], v40 offset:0\n"
"ds_read_b128 a[132:135], v40 offset:2048\n"
"ds_read_b64_tr_b16 a[164:165], v43 offset:8192\n"
"ds_read_b64_tr_b16 a[166:167], v43 offset:8704\n"
"ds_read_b64_tr_b16 a[168:169], v42 offset:16384\n"
"ds_read_b64_tr_b16 a[170:171], v42 offset:16896\n"
"ds_read_b64_tr_b16 a[172:173], v43 offset:16384\n"
"ds_read_b64_tr_b16 a[174:175], v43 offset:16896\n"
"s_sub_u32 s46, s47, 5\n"
"s_waitcnt lgkmcnt(0)\n"
"KL_s_%=:\n"
"v_mfma_f32_16x16x32_bf16 v[128:131], a[160:163], a[128:131], v[128:131]\n"
"ds_read_b64_tr_b16 a[224:225], v42 offset:12288\n"
"ds_read_b64_tr_b16 a[226:227], v42 offset:12800\n"
"s_add_u32 m0, s50, s62\n"
"v_mfma_f32_16x16x32_bf16 v[132:135], a[160:163], a[132:135], v[132:135]\n"
"ds_read_b128 a[192:195], v41 offset:0\n"
"ds_read_b128 a[196:199], v41 offset:2048\n"
"buffer_load_dwordx4 v32, s[40:43], s45 offen offset:0 lds\n"
"v_mfma_f32_16x16x32_bf16 v[136:139], a[164:167], a[128:131], v[136:139]\n"
"ds_read_b64_tr_b16 a[228:229], v43 offset:12288\n"
"ds_read_b64_tr_b16 a[230:231], v43 offset:12800\n"
"v_mfma_f32_16x16x32_bf16 v[140:143], a[164:167], a[132:135], v[140:143]\n"
"ds_read_b64_tr_b16 a[232:233], v42 offset:20480\n"
"ds_read_b64_tr_b16 a[234:235], v42 offset:20992\n"
"buffer_load_dwordx4 v33, s[40:43], s45 offen offset:1024 lds\n"
"v_mfma_f32_16x16x32_bf16 v[144:147], a[168:171], a[128:131], v[144:147]\n"
"ds_read_b64_tr_b16 a[236:237], v43 offset:20480\n"
"ds_read_b64_tr_b16 a[238:239], v43 offset:20992\n"
"v_mfma_f32_16x16x32_bf16 v[148:151], a[168:171], a[132:135], v[148:151]\n"
"buffer_load_dwordx4 v34, s[40:43], s45 offen offset:2048 lds\n"
"v_mfma_f32_16x16x32_bf16 v[152:155], a[172:175], a[128:131], v[152:155]\n"
"s_add_u32 s51, s51, 24576\n"
"s_cmp_eq_u32 s51, 122880\n"
"s_cselect_b32 s51, 0, s51\n"
"v_mfma_f32_16x16x32_bf16 v[156:159], a[172:175], a[132:135], v[156:159]\n"
"buffer_load_dwordx4 v35, s[40:43], s45 offen offset:3072 lds\n"
"s_add_u32 s45, s45, s48\n"
"s_add_u32 s50, s50, 24576\n"
"s_cmp_eq_u32 s50, 122880\n"
"s_cselect_b32 s50, 0, s50\n"
"s_waitcnt lgkmcnt(0) vmcnt(18)\n"
"s_barrier\n"
"v_mfma_f32_16x16x32_bf16 v[128:131], a[224:227], a[192:195], v[128:131]\n"
"v_add_u32 v40, s51, v46\n"
"v_add_u32 v41, s51, v47\n"
"s_add_u32 m0, s50, s60\n"
"v_mfma_f32_16x16x32_bf16 v[132:135], a[224:227], a[196:199], v[132:135]\n"
"v_add_u32 v42, s51, v48\n"
"v_add_u32 v43, s51, v49\n"
"buffer_load_dwordx4 v24, s[36:39], s44 offen offset:0 sc1 lds\n"
"v_mfma_f32_16x16x32_bf16 v[136:139], a[228:231], a[192:195], v[136:139]\n"
"ds_read_b64_tr_b16 a[160:161], v42 offset:8192\n"
"ds_read_b64_tr_b16 a[162:163], v42 offset:8704\n"
"ds_read_b128 a[128:131], v40 offset:0\n"
"v_mfma_f32_16x16x32_bf16 v[140:143], a[228:231], a[196:199], v[140:143]\n"
"ds_read_b128 a[132:135], v40 offset:2048\n"
"ds_read_b64_tr_b16 a[164:165], v43 offset:8192\n"
"v_mfma_f32_16x16x32_bf16 v[144:147], a[232:235], a[192:195], v[144:147]\n"
"ds_read_b64_tr_b16 a[166:167], v43 offset:8704\n"
"ds_read_b64_tr_b16 a[168:169], v42 offset:16384\n"
"ds_read_b64_tr_b16 a[170:171], v42 offset:16896\n"
"v_mfma_f32_16x16x32_bf16 v[148:151], a[232:235], a[196:199], v[148:151]\n"
"ds_read_b64_tr_b16 a[172:173], v43 offset:16384\n"
"ds_read_b64_tr_b16 a[174:175], v43 offset:16896\n"
"buffer_load_dwordx4 v25, s[36:39], s44 offen offset:1024 sc1 lds\n"
"v_mfma_f32_16x16x32_bf16 v[152:155], a[236:239], a[192:195], v[152:155]\n"
"s_add_u32 s44, s44, 2048\n"
"v_mfma_f32_16x16x32_bf16 v[156:159], a[236:239], a[196:199], v[156:159]\n"
"s_sub_u32 s46, s46, 1\n"
"s_cmp_lg_u32 s46, 0\n"
"s_waitcnt lgkmcnt(0)\n"
"s_cbranch_scc1 KL_s_%=\n"
"v_mfma_f32_16x16x32_bf16 v[128:131], a[160:163], a[128:131], v[128:131]\n"
"ds_read_b64_tr_b16 a[224:225], v42 offset:12288\n"
"ds_read_b64_tr_b16 a[226:227], v42 offset:12800\n"
"s_add_u32 m0, s50, s62\n"
"v_mfma_f32_16x16x32_bf16 v[132:135], a[160:163], a[132:135], v[132:135]\n"
"ds_read_b128 a[192:195], v41 offset:0\n"
"ds_read_b128 a[196:199], v41 offset:2048\n"
"buffer_load_dwordx4 v32, s[40:43], s45 offen offset:0 lds\n"
"v_mfma_f32_16x16x32_bf16 v[136:139], a[164:167], a[128:131], v[136:139]\n"
"ds_read_b64_tr_b16 a[228:229], v43 offset:12288\n"
"ds_read_b64_tr_b16 a[230:231], v43 offset:12800\n"
"v_mfma_f32_16x16x32_bf16 v[140:143], a[164:167], a[132:135], v[140:143]\n"
"ds_read_b64_tr_b16 a[232:233], v42 offset:20480\n"
"ds_read_b64_tr_b16 a[234:235], v42 offset:20992\n"
"buffer_load_dwordx4 v33, s[40:43], s45 offen offset:1024 lds\n"
"v_mfma_f32_16x16x32_bf16 v[144:147], a[168:171], a[128:131], v[144:147]\n"
"ds_read_b64_tr_b16 a[236:237], v43 offset:20480\n"
"ds_read_b64_tr_b16 a[238:239], v43 offset:20992\n"
"v_mfma_f32_16x16x32_bf16 v[148:151], a[168:171], a[132:135], v[148:151]\n"
"buffer_load_dwordx4 v34, s[40:43], s45 offen offset:2048 lds\n"
"v_mfma_f32_16x16x32_bf16 v[152:155], a[172:175], a[128:131], v[152:155]\n"
"s_add_u32 s51, s51, 24576\n"
"s_cmp_eq_u32 s51, 122880\n"
"s_cselect_b32 s51, 0, s51\n"
"v_mfma_f32_16x16x32_bf16 v[156:159], a[172:175], a[132:135], v[156:159]\n"
"buffer_load_dwordx4 v35, s[40:43], s45 offen offset:3072 lds\n"
"s_add_u32 s45, s45, s48\n"
"s_add_u32 s50, s50, 24576\n"
"s_cmp_eq_u32 s50, 122880\n"
"s_cselect_b32 s50, 0, s50\n"
"s_waitcnt lgkmcnt(0) vmcnt(18)\n"
"s_barrier\n"
"v_mfma_f32_16x16x32_bf16 v[128:131], a[224:227], a[192:195], v[128:131]\n"
"v_add_u32 v40, s51, v46\n"
"v_add_u32 v41, s51, v47\n"
"v_mfma_f32_16x16x32_bf16 v[132:135], a[224:227], a[196:199], v[132:135]\n"
"v_add_u32 v42, s51, v48\n"
"v_add_u32 v43, s51, v49\n"
"v_mfma_f32_16x16x32_bf16 v[136:139], a[228:231], a[192:195], v[136:139]\n"
"ds_read_b64_tr_b16 a[160:161], v42 offset:8192\n"
"ds_read_b64_tr_b16 a[162:163], v42 offset:8704\n"
"ds_read_b128 a[128:131], v40 offset:0\n"
"v_mfma_f32_16x16x32_bf16 v[140:143], a[228:231], a[196:199], v[140:143]\n"
"ds_read_b128 a[132:135], v40 offset:2048\n"
"ds_read_b64_tr_b16 a[164:165], v43 offset:8192\n"
"v_mfma_f32_16x16x32_bf16 v[144:147], a[232:235], a[192:195], v[144:147]\n"
"ds_read_b64_tr_b16 a[166:167], v43 offset:8704\n"
"ds_read_b64_tr_b16 a[168:169], v42 offset:16384\n"
"ds_read_b64_tr_b16 a[170:171], v42 offset:16896\n"
"v_mfma_f32_16x16x32_bf16 v[148:151], a[232:235], a[196:199], v[148:151]\n"
"ds_read_b64_tr_b16 a[172:173], v43 offset:16384\n"
"ds_read_b64_tr_b16 a[174:175], v43 offset:16896\n"
"v_mfma_f32_16x16x32_bf16 v[152:155], a[236:239], a[192:195], v[152:155]\n"
"v_mfma_f32_16x16x32_bf16 v[156:159], a[236:239], a[196:199], v[156:159]\n"
"s_waitcnt lgkmcnt(0)\n"
"v_mfma_f32_16x16x32_bf16 v[128:131], a[160:163], a[128:131], v[128:131]\n"
"ds_read_b64_tr_b16 a[224:225], v42 offset:12288\n"
"ds_read_b64_tr_b16 a[226:227], v42 offset:12800\n"
"v_mfma_f32_16x16x32_bf16 v[132:135], a[160:163], a[132:135], v[132:135]\n"
"ds_read_b128 a[192:195], v41 offset:0\n"
"ds_read_b128 a[196:199], v41 offset:2048\n"
"v_mfma_f32_16x16x32_bf16 v[136:139], a[164:167], a[128:131], v[136:139]\n"
"ds_read_b64_tr_b16 a[228:229], v43 offset:12288\n"
"ds_read_b64_tr_b16 a[230:231], v43 offset:12800\n"
"v_mfma_f32_16x16x32_bf16 v[140:143], a[164:167], a[132:135], v[140:143]\n"
"ds_read_b64_tr_b16 a[232:233], v42 offset:20480\n"
"ds_read_b64_tr_b16 a[234:235], v42 offset:20992\n"
"v_mfma_f32_16x16x32_bf16 v[144:147], a[168:171], a[128:131], v[144:147]\n"
"ds_read_b64_tr_b16 a[236:237], v43 offset:20480\n"
"ds_read_b64_tr_b16 a[238:239], v43 offset:20992\n"
"v_mfma_f32_16x16x32_bf16 v[148:151], a[168:171], a[132:135], v[148:151]\n"
"v_mfma_f32_16x16x32_bf16 v[152:155], a[172:175], a[128:131], v[152:155]\n"
"s_add_u32 s51, s51, 24576\n"
"s_cmp_eq_u32 s51, 122880\n"
"s_cselect_b32 s51, 0, s51\n"
"v_mfma_f32_16x16x32_bf16 v[156:159], a[172:175], a[132:135], v[156:159]\n"
"s_waitcnt lgkmcnt(0) vmcnt(12)\n"
"s_barrier\n"
"v_mfma_f32_16x16x32_bf16 v[128:131], a[224:227], a[192:195], v[128:131]\n"
"v_add_u32 v40, s51, v46\n"
"v_add_u32 v41, s51, v47\n"
"v_mfma_f32_16x16x32_bf16 v[132:135], a[224:227], a[196:199], v[132:135]\n"
"v_add_u32 v42, s51, v48\n"
"v_add_u32 v43, s51, v49\n"
"v_mfma_f32_16x16x32_bf16 v[136:139], a[228:231], a[192:195], v[136:139]\n"
"ds_read_b64_tr_b16 a[160:161], v42 offset:8192\n"
"ds_read_b64_tr_b16 a[162:163], v42 offset:8704\n"
"ds_read_b128 a[128:131], v40 offset:0\n"
"v_mfma_f32_16x16x32_bf16 v[140:143], a[228:231], a[196:199], v[140:143]\n"
"ds_read_b128 a[132:135], v40 offset:2048\n"
"ds_read_b64_tr_b16 a[164:165], v43 offset:8192\n"
"v_mfma_f32_16x16x32_bf16 v[144:147], a[232:235], a[192:195], v[144:147]\n"
"ds_read_b64_tr_b16 a[166:167], v43 offset:8704\n"
"ds_read_b64_tr_b16 a[168:169], v42 offset:16384\n"
"ds_read_b64_tr_b16 a[170:171], v42 offset:16896\n"
"v_mfma_f32_16x16x32_bf16 v[148:151], a[232:235], a[196:199], v[148:151]\n"
"ds_read_b64_tr_b16 a[172:173], v43 offset:16384\n"
"ds_read_b64_tr_b16 a[174:175], v43 offset:16896\n"
"v_mfma_f32_16x16x32_bf16 v[152:155], a[236:239], a[192:195], v[152:155]\n"
"v_mfma_f32_16x16x32_bf16 v[156:159], a[236:239], a[196:199], v[156:159]\n"
"s_waitcnt lgkmcnt(0)\n"
"v_mfma_f32_16x16x32_bf16 v[128:131], a[160:163], a[128:131], v[128:131]\n"
"ds_read_b64_tr_b16 a[224:225], v42 offset:12288\n"
"ds_read_b64_tr_b16 a[226:227], v42 offset:12800\n"
"v_mfma_f32_16x16x32_bf16 v[132:135], a[160:163], a[132:135], v[132:135]\n"
"ds_read_b128 a[192:195], v41 offset:0\n"
"ds_read_b128 a[196:199], v41 offset:2048\n"
"v_mfma_f32_16x16x32_bf16 v[136:139], a[164:167], a[128:131], v[136:139]\n"
"ds_read_b64_tr_b16 a[228:229], v43 offset:12288\n"
"ds_read_b64_tr_b16 a[230:231], v43 offset:12800\n"
"v_mfma_f32_16x16x32_bf16 v[140:143], a[164:167], a[132:135], v[140:143]\n"
"ds_read_b64_tr_b16 a[232:233], v42 offset:20480\n"
"ds_read_b64_tr_b16 a[234:235], v42 offset:20992\n"
"v_mfma_f32_16x16x32_bf16 v[144:147], a[168:171], a[128:131], v[144:147]\n"
"ds_read_b64_tr_b16 a[236:237], v43 offset:20480\n"
"ds_read_b64_tr_b16 a[238:239], v43 offset:20992\n"
"v_mfma_f32_16x16x32_bf16 v[148:151], a[168:171], a[132:135], v[148:151]\n"
"v_mfma_f32_16x16x32_bf16 v[152:155], a[172:175], a[128:131], v[152:155]\n"
"s_add_u32 s51, s51, 24576\n"
"s_cmp_eq_u32 s51, 122880\n"
"s_cselect_b32 s51, 0, s51\n"
"v_mfma_f32_16x16x32_bf16 v[156:159], a[172:175], a[132:135], v[156:159]\n"
"s_waitcnt lgkmcnt(0) vmcnt(6)\n"
"s_barrier\n"
"v_mfma_f32_16x16x32_bf16 v[128:131], a[224:227], a[192:195], v[128:131]\n"
"v_add_u32 v40, s51, v46\n"
"v_add_u32 v41, s51, v47\n"
"v_mfma_f32_16x16x32_bf16 v[132:135], a[224:227], a[196:199], v[132:135]\n"
"v_add_u32 v42, s51, v48\n"
"v_add_u32 v43, s51, v49\n"
"v_mfma_f32_16x16x32_bf16 v[136:139], a[228:231], a[192:195], v[136:139]\n"
"ds_read_b64_tr_b16 a[160:161], v42 offset:8192\n"
"ds_read_b64_tr_b16 a[162:163], v42 offset:8704\n"
"ds_read_b128 a[128:131], v40 offset:0\n"
"v_mfma_f32_16x16x32_bf16 v[140:143], a[228:231], a[196:199], v[140:143]\n"
"ds_read_b128 a[132:135], v40 offset:2048\n"
"ds_read_b64_tr_b16 a[164:165], v43 offset:8192\n"
"v_mfma_f32_16x16x32_bf16 v[144:147], a[232:235], a[192:195], v[144:147]\n"
"ds_read_b64_tr_b16 a[166:167], v43 offset:8704\n"
"ds_read_b64_tr_b16 a[168:169], v42 offset:16384\n"
"ds_read_b64_tr_b16 a[170:171], v42 offset:16896\n"
"v_mfma_f32_16x16x32_bf16 v[148:151], a[232:235], a[196:199], v[148:151]\n"
"ds_read_b64_tr_b16 a[172:173], v43 offset:16384\n"
"ds_read_b64_tr_b16 a[174:175], v43 offset:16896\n"
"v_mfma_f32_16x16x32_bf16 v[152:155], a[236:239], a[192:195], v[152:155]\n"
"v_mfma_f32_16x16x32_bf16 v[156:159], a[236:239], a[196:199], v[156:159]\n"
"s_waitcnt lgkmcnt(0)\n"
"v_mfma_f32_16x16x32_bf16 v[128:131], a[160:163], a[128:131], v[128:131]\n"
"ds_read_b64_tr_b16 a[224:225], v42 offset:12288\n"
"ds_read_b64_tr_b16 a[226:227], v42 offset:12800\n"
"v_mfma_f32_16x16x32_bf16 v[132:135], a[160:163], a[132:135], v[132:135]\n"
"ds_read_b128 a[192:195], v41 offset:0\n"
"ds_read_b128 a[196:199], v41 offset:2048\n"
"v_mfma_f32_16x16x32_bf16 v[136:139], a[164:167], a[128:131], v[136:139]\n"
"ds_read_b64_tr_b16 a[228:229], v43 offset:12288\n"
"ds_read_b64_tr_b16 a[230:231], v43 offset:12800\n"
"v_mfma_f32_16x16x32_bf16 v[140:143], a[164:167], a[132:135], v[140:143]\n"
"ds_read_b64_tr_b16 a[232:233], v42 offset:20480\n"
"ds_read_b64_tr_b16 a[234:235], v42 offset:20992\n"
"v_mfma_f32_16x16x32_bf16 v[144:147], a[168:171], a[128:131], v[144:147]\n"
"ds_read_b64_tr_b16 a[236:237], v43 offset:20480\n"
"ds_read_b64_tr_b16 a[238:239], v43 offset:20992\n"
"v_mfma_f32_16x16x32_bf16 v[148:151], a[168:171], a[132:135], v[148:151]\n"
"v_mfma_f32_16x16x32_bf16 v[152:155], a[172:175], a[128:131], v[152:155]\n"
"s_add_u32 s51, s51, 24576\n"
"s_cmp_eq_u32 s51, 122880\n"
"s_cselect_b32 s51, 0, s51\n"
"v_mfma_f32_16x16x32_bf16 v[156:159], a[172:175], a[132:135], v[156:159]\n"
"s_waitcnt lgkmcnt(0) vmcnt(0)\n"
"s_barrier\n"
"v_mfma_f32_16x16x32_bf16 v[128:131], a[224:227], a[192:195], v[128:131]\n"
"v_add_u32 v40, s51, v46\n"
"v_add_u32 v41, s51, v47\n"
"v_mfma_f32_16x16x32_bf16 v[132:135], a[224:227], a[196:199], v[132:135]\n"
"v_add_u32 v42, s51, v48\n"
"v_add_u32 v43, s51, v49\n"
"v_mfma_f32_16x16x32_bf16 v[136:139], a[228:231], a[192:195], v[136:139]\n"
"ds_read_b64_tr_b16 a[160:161], v42 offset:8192\n"
"ds_read_b64_tr_b16 a[162:163], v42 offset:8704\n"
"ds_read_b128 a[128:131], v40 offset:0\n"
"v_mfma_f32_16x16x32_bf16 v[140:143], a[228:231], a[196:199], v[140:143]\n"
"ds_read_b128 a[132:135], v40 offset:2048\n"
"ds_read_b64_tr_b16 a[164:165], v43 offset:8192\n"
"v_mfma_f32_16x16x32_bf16 v[144:147], a[232:235], a[192:195], v[144:147]\n"
"ds_read_b64_tr_b16 a[166:167], v43 offset:8704\n"
"ds_read_b64_tr_b16 a[168:169], v42 offset:16384\n"
"ds_read_b64_tr_b16 a[170:171], v42 offset:16896\n"
"v_mfma_f32_16x16x32_bf16 v[148:151], a[232:235], a[196:199], v[148:151]\n"
"ds_read_b64_tr_b16 a[172:173], v43 offset:16384\n"
"ds_read_b64_tr_b16 a[174:175], v43 offset:16896\n"
"v_mfma_f32_16x16x32_bf16 v[152:155], a[236:239], a[192:195], v[152:155]\n"
"v_mfma_f32_16x16x32_bf16 v[156:159], a[236:239], a[196:199], v[156:159]\n"
"s_waitcnt lgkmcnt(0)\n"
"v_mfma_f32_16x16x32_bf16 v[128:131], a[160:163], a[128:131], v[128:131]\n"
"ds_read_b64_tr_b16 a[224:225], v42 offset:12288\n"
"ds_read_b64_tr_b16 a[226:227], v42 offset:12800\n"
"v_mfma_f32_16x16x32_bf16 v[132:135], a[160:163], a[132:135], v[132:135]\n"
"ds_read_b128 a[192:195], v41 offset:0\n"
"ds_read_b128 a[196:199], v41 offset:2048\n"
"v_mfma_f32_16x16x32_bf16 v[136:139], a[164:167], a[128:131], v[136:139]\n"
"ds_read_b64_tr_b16 a[228:229], v43 offset:12288\n"
"ds_read_b64_tr_b16 a[230:231], v43 offset:12800\n"
"v_mfma_f32_16x16x32_bf16 v[140:143], a[164:167], a[132:135], v[140:143]\n"
"ds_read_b64_tr_b16 a[232:233], v42 offset:20480\n"
"ds_read_b64_tr_b16 a[234:235], v42 offset:20992\n"
"v_mfma_f32_16x16x32_bf16 v[144:147], a[168:171], a[128:131], v[144:147]\n"
"ds_read_b64_tr_b16 a[236:237], v43 offset:20480\n"
"ds_read_b64_tr_b16 a[238:239], v43 offset:20992\n"
"v_mfma_f32_16x16x32_bf16 v[148:151], a[168:171], a[132:135], v[148:151]\n"
"v_mfma_f32_16x16x32_bf16 v[152:155], a[172:175], a[128:131], v[152:155]\n"
"s_add_u32 s51, s51, 24576\n"
"s_cmp_eq_u32 s51, 122880\n"
"s_cselect_b32 s51, 0, s51\n"
"v_mfma_f32_16x16x32_bf16 v[156:159], a[172:175], a[132:135], v[156:159]\n"
"s_waitcnt lgkmcnt(0)\n"
"v_mfma_f32_16x16x32_bf16 v[128:131], a[224:227], a[192:195], v[128:131]\n"
"v_mfma_f32_16x16x32_bf16 v[132:135], a[224:227], a[196:199], v[132:135]\n"
"v_mfma_f32_16x16x32_bf16 v[136:139], a[228:231], a[192:195], v[136:139]\n"
"v_mfma_f32_16x16x32_bf16 v[140:143], a[228:231], a[196:199], v[140:143]\n"
"v_mfma_f32_16x16x32_bf16 v[144:147], a[232:235], a[192:195], v[144:147]\n"
"v_mfma_f32_16x16x32_bf16 v[148:151], a[232:235], a[196:199], v[148:151]\n"
"v_mfma_f32_16x16x32_bf16 v[152:155], a[236:239], a[192:195], v[152:155]\n"
"v_mfma_f32_16x16x32_bf16 v[156:159], a[236:239], a[196:199], v[156:159]\n"
"s_barrier\n"
"s_cmp_lg_u32 %[kind], 0\n"
"s_cbranch_scc1 NX0_%=\n"
"s_add_u32 s54, %[o0], 0\n"
"v_cvt_pk_bf16_f32 v60, v128, v129\n"
"v_cvt_pk_bf16_f32 v61, v130, v131\n"
"v_cvt_pk_bf16_f32 v62, v136, v137\n"
"v_cvt_pk_bf16_f32 v63, v138, v139\n"
"s_nop 0\n"
"v_permlane16_swap_b32 v60, v62\n"
"v_permlane16_swap_b32 v61, v63\n"
"buffer_store_dwordx4 v[60:63], %[vst], s[56:59], s54 offen offset:0 sc1\n"
"s_add_u32 s54, %[o0], 0\n"
"s_add_u32 s54, s54, %[o1]\n"
"v_cvt_pk_bf16_f32 v64, v144, v145\n"
"v_cvt_pk_bf16_f32 v65, v146, v147\n"
"v_cvt_pk_bf16_f32 v66, v152, v153\n"
"v_cvt_pk_bf16_f32 v67, v154, v155\n"
"s_nop 0\n"
"v_permlane16_swap_b32 v64, v66\n"
"v_permlane16_swap_b32 v65, v67\n"
"buffer_store_dwordx4 v[64:67], %[vst], s[56:59], s54 offen offset:0 sc1\n"
"s_add_u32 s54, %[o0], 4096\n"
"v_cvt_pk_bf16_f32 v68, v132, v133\n"
"v_cvt_pk_bf16_f32 v69, v134, v135\n"
"v_cvt_pk_bf16_f32 v70, v140, v141\n"
"v_cvt_pk_bf16_f32 v71, v142, v143\n"
"s_nop 0\n"
"v_permlane16_swap_b32 v68, v70\n"
"v_permlane16_swap_b32 v69, v71\n"
"buffer_store_dwordx4 v[68:71], %[vst], s[56:59], s54 offen offset:0 sc1\n"
"s_add_u32 s54, %[o0], 4096\n"
"s_add_u32 s54, s54, %[o1]\n"
"v_cvt_pk_bf16_f32 v72, v148, v149\n"
"v_cvt_pk_bf16_f32 v73, v150, v151\n"
"v_cvt_pk_bf16_f32 v74, v156, v157\n"
"v_cvt_pk_bf16_f32 v75, v158, v159\n"
"s_nop 0\n"
"v_permlane16_swap_b32 v72, v74\n"
"v_permlane16_swap_b32 v73, v75\n"
"buffer_store_dwordx4 v[72:75], %[vst], s[56:59], s54 offen offset:0 sc1\n"
"s_branch END_%=\n"
"NX0_%=:\n"
"s_cmp_lg_u32 %[kind], 2\n"
"s_cbranch_scc1 NX2_%=\n"
"s_add_u32 s54, %[o0], 0\n"
"v_cvt_pk_bf16_f32 v60, v128, v129\n"
"v_cvt_pk_bf16_f32 v61, v130, v131\n"
"v_cvt_pk_bf16_f32 v62, v136, v137\n"
"v_cvt_pk_bf16_f32 v63, v138, v139\n"
"s_nop 0\n"
"v_permlane16_swap_b32 v60, v62\n"
"v_permlane16_swap_b32 v61, v63\n"
"buffer_store_dwordx4 v[60:63], %[vst], s[56:59], s54 offen offset:0 sc1\n"
"s_add_u32 s54, %[o0], 0\n"
"s_add_u32 s54, s54, %[o1]\n"
"v_cvt_pk_bf16_f32 v64, v144, v145\n"
"v_cvt_pk_bf16_f32 v65, v146, v147\n"
"v_cvt_pk_bf16_f32 v66, v152, v153\n"
"v_cvt_pk_bf16_f32 v67, v154, v155\n"
"s_nop 0\n"
"v_permlane16_swap_b32 v64, v66\n"
"v_permlane16_swap_b32 v65, v67\n"
"buffer_store_dwordx4 v[64:67], %[vst], s[56:59], s54 offen offset:0 sc1\n"
"s_add_u32 s54, %[o0], 32768\n"
"v_cvt_pk_bf16_f32 v68, v132, v133\n"
"v_cvt_pk_bf16_f32 v69, v134, v135\n"
"v_cvt_pk_bf16_f32 v70, v140, v141\n"
"v_cvt_pk_bf16_f32 v71, v142, v143\n"
"s_nop 0\n"
"v_permlane16_swap_b32 v68, v70\n"
"v_permlane16_swap_b32 v69, v71\n"
"buffer_store_dwordx4 v[68:71], %[vst], s[56:59], s54 offen offset:0 sc1\n"
"s_add_u32 s54, %[o0], 32768\n"
"s_add_u32 s54, s54, %[o1]\n"
"v_cvt_pk_bf16_f32 v72, v148, v149\n"
"v_cvt_pk_bf16_f32 v73, v150, v151\n"
"v_cvt_pk_bf16_f32 v74, v156, v157\n"
"v_cvt_pk_bf16_f32 v75, v158, v159\n"
"s_nop 0\n"
"v_permlane16_swap_b32 v72, v74\n"
"v_permlane16_swap_b32 v73, v75\n"
"buffer_store_dwordx4 v[72:75], %[vst], s[56:59], s54 offen offset:0 sc1\n"
"s_branch END_%=\n"
"NX2_%=:\n"
"s_mov_b32 s64, 0xbfb8aa3b\n"
"s_mov_b32 s65, s64\n"
"s_mov_b32 s66, 1.0\n"
"s_mov_b32 s67, 1.0\n"
"s_add_u32 s54, %[o0], 0\n"
"v_pk_mul_f32 v[52:53], v[128:129], s[64:65]\n"
"v_pk_mul_f32 v[54:55], v[130:131], s[64:65]\n"
"v_exp_f32 v52, v52\n"
"v_exp_f32 v53, v53\n"
"v_exp_f32 v54, v54\n"
"v_exp_f32 v55, v55\n"
"s_nop 0\n"
"v_pk_add_f32 v[52:53], v[52:53], s[66:67]\n"
"v_pk_add_f32 v[54:55], v[54:55], s[66:67]\n"
"v_rcp_f32 v52, v52\n"
"v_rcp_f32 v53, v53\n"
"v_rcp_f32 v54, v54\n"
"v_rcp_f32 v55, v55\n"
"v_pk_mul_f32 v[128:129], v[128:129], v[144:145]\n"
"v_pk_mul_f32 v[130:131], v[130:131], v[146:147]\n"
"v_pk_mul_f32 v[128:129], v[128:129], v[52:53]\n"
"v_pk_mul_f32 v[130:131], v[130:131], v[54:55]\n"
"v_cvt_pk_bf16_f32 v68, v128, v129\n"
"v_cvt_pk_bf16_f32 v69, v130, v131\n"
"v_pk_mul_f32 v[52:53], v[136:137], s[64:65]\n"
"v_pk_mul_f32 v[54:55], v[138:139], s[64:65]\n"
"v_exp_f32 v52, v52\n"
"v_exp_f32 v53, v53\n"
"v_exp_f32 v54, v54\n"
"v_exp_f32 v55, v55\n"
"s_nop 0\n"
"v_pk_add_f32 v[52:53], v[52:53], s[66:67]\n"
"v_pk_add_f32 v[54:55], v[54:55], s[66:67]\n"
"v_rcp_f32 v52, v52\n"
"v_rcp_f32 v53, v53\n"
"v_rcp_f32 v54, v54\n"
"v_rcp_f32 v55, v55\n"
"v_pk_mul_f32 v[136:137], v[136:137], v[152:153]\n"
"v_pk_mul_f32 v[138:139], v[138:139], v[154:155]\n"
"v_pk_mul_f32 v[136:137], v[136:137], v[52:53]\n"
"v_pk_mul_f32 v[138:139], v[138:139], v[54:55]\n"
"v_cvt_pk_bf16_f32 v70, v136, v137\n"
"v_cvt_pk_bf16_f32 v71, v138, v139\n"
"s_nop 0\n"
"v_permlane16_swap_b32 v68, v70\n"
"v_permlane16_swap_b32 v69, v71\n"
"buffer_store_dwordx4 v[68:71], %[vst], s[56:59], s54 offen offset:0 sc1\n"
"s_add_u32 s54, %[o0], 114688\n"
"v_pk_mul_f32 v[52:53], v[132:133], s[64:65]\n"
"v_pk_mul_f32 v[54:55], v[134:135], s[64:65]\n"
"v_exp_f32 v52, v52\n"
"v_exp_f32 v53, v53\n"
"v_exp_f32 v54, v54\n"
"v_exp_f32 v55, v55\n"
"s_nop 0\n"
"v_pk_add_f32 v[52:53], v[52:53], s[66:67]\n"
"v_pk_add_f32 v[54:55], v[54:55], s[66:67]\n"
"v_rcp_f32 v52, v52\n"
"v_rcp_f32 v53, v53\n"
"v_rcp_f32 v54, v54\n"
"v_rcp_f32 v55, v55\n"
"v_pk_mul_f32 v[132:133], v[132:133], v[148:149]\n"
"v_pk_mul_f32 v[134:135], v[134:135], v[150:151]\n"
"v_pk_mul_f32 v[132:133], v[132:133], v[52:53]\n"
"v_pk_mul_f32 v[134:135], v[134:135], v[54:55]\n"
"v_cvt_pk_bf16_f32 v72, v132, v133\n"
"v_cvt_pk_bf16_f32 v73, v134, v135\n"
"v_pk_mul_f32 v[52:53], v[140:141], s[64:65]\n"
"v_pk_mul_f32 v[54:55], v[142:143], s[64:65]\n"
"v_exp_f32 v52, v52\n"
"v_exp_f32 v53, v53\n"
"v_exp_f32 v54, v54\n"
"v_exp_f32 v55, v55\n"
"s_nop 0\n"
"v_pk_add_f32 v[52:53], v[52:53], s[66:67]\n"
"v_pk_add_f32 v[54:55], v[54:55], s[66:67]\n"
"v_rcp_f32 v52, v52\n"
"v_rcp_f32 v53, v53\n"
"v_rcp_f32 v54, v54\n"
"v_rcp_f32 v55, v55\n"
"v_pk_mul_f32 v[140:141], v[140:141], v[156:157]\n"
"v_pk_mul_f32 v[142:143], v[142:143], v[158:159]\n"
"v_pk_mul_f32 v[140:141], v[140:141], v[52:53]\n"
"v_pk_mul_f32 v[142:143], v[142:143], v[54:55]\n"
"v_cvt_pk_bf16_f32 v74, v140, v141\n"
"v_cvt_pk_bf16_f32 v75, v142, v143\n"
"s_nop 0\n"
"v_permlane16_swap_b32 v72, v74\n"
"v_permlane16_swap_b32 v73, v75\n"
"buffer_store_dwordx4 v[72:75], %[vst], s[56:59], s54 offen offset:0 sc1\n"
"END_%=:\n"
"s_waitcnt vmcnt(0)\n"
  :: [ws] "s"(ws), [kind] "s"(kind), [wave] "s"(wave), [aoff] "s"(aoff), [lda] "s"(lda), [bp] "s"(bp), [bn] "s"(bn), [ldb] "s"(ldb), [nk] "s"(nk), [o0] "s"(o0), [o1] "s"(o1),
     [va0] "v"(va0), [vb0] "v"(vb0), [vx0] "v"(vx0), [vw0] "v"(vw0), [vst] "v"(vst)
  : "v24","v25","v26","v27","v28","v29","v30","v31","v32","v33","v34","v35","v36","v37","v38","v39","v40","v41","v42","v43","v44","v45","v46","v47","v48","v49","v50","v51","v52","v53","v54","v55",
"v56","v57","v58","v59","v60","v61","v62","v63","v64","v65","v66","v67","v68","v69","v70","v71","v72","v73","v74","v75","v76","v77","v78","v79","v80","v81","v82","v83","v84","v85","v86","v87",
"v88","v89","v90","v91","v92","v93","v94","v95","v96","v97","v98","v99","v100","v101","v102","v103","v104","v105","v106","v107","v108","v109","v110","v111","v112","v113","v114","v115","v116","v117","v118","v119",
"v120","v121","v122","v123","v124","v125","v126","v127","v128","v129","v130","v131","v132","v133","v134","v135","v136","v137","v138","v139","v140","v141","v142","v143","v144","v145","v146","v147","v148","v149","v150","v151",
"v152","v153","v154","v155","v156","v157","v158","v159","v160","v161","v162","v163","v164","v165","v166","v167","v168","v169","v170","v171","v172","v173","v174","v175","v176","v177","v178","v179","v180","v181","v182","v183",
"v184","v185","v186","v187","v188","v189","v190","v191","v192","v193","v194","v195","v196","v197","v198","v199","v200","v201","v202","v203","v204","v205","v206","v207","v208","v209","v210","v211","v212","v213","v214","v215",
"v216","v217","v218","v219","v220","v221","v222","v223","v224","v225","v226","v227","v228","v229","v230","v231","v232","v233","v234","v235","v236","v237","v238","v239","v240","v241","v242","v243","v244","v245","v246","v247",
"v248","v249","v250","v251","v252","v253","v254","v255","a0","a1","a2","a3","a4","a5","a6","a7","a8","a9","a10","a11","a12","a13","a14","a15","a16","a17","a18","a19","a20","a21","a22","a23",
"a24","a25","a26","a27","a28","a29","a30","a31","a32","a33","a34","a35","a36","a37","a38","a39","a40","a41","a42","a43","a44","a45","a46","a47","a48","a49","a50","a51","a52","a53","a54","a55",
"a56","a57","a58","a59","a60","a61","a62","a63","a64","a65","a66","a67","a68","a69","a70","a71","a72","a73","a74","a75","a76","a77","a78","a79","a80","a81","a82","a83","a84","a85","a86","a87",
"a88","a89","a90","a91","a92","a93","a94","a95","a96","a97","a98","a99","a100","a101","a102","a103","a104","a105","a106","a107","a108","a109","a110","a111","a112","a113","a114","a115","a116","a117","a118","a119",
"a120","a121","a122","a123","a124","a125","a126","a127","a128","a129","a130","a131","a132","a133","a134","a135","a136","a137","a138","a139","a140","a141","a142","a143","a144","a145","a146","a147","a148","a149","a150","a151",
"a152","a153","a154","a155","a156","a157","a158","a159","a160","a161","a162","a163","a164","a165","a166","a167","a168","a169","a170","a171","a172","a173","a174","a175","a176","a177","a178","a179","a180","a181","a182","a183",
"a184","a185","a186","a187","a188","a189","a190","a191","a192","a193","a194","a195","a196","a197","a198","a199","a200","a201","a202","a203","a204","a205","a206","a207","a208","a209","a210","a211","a212","a213","a214","a215",
"a216","a217","a218","a219","a220","a221","a222","a223","a224","a225","a226","a227","a228","a229","a230","a231","a232","a233","a234","a235","a236","a237","a238","a239","a240","a241","a242","a243","a244","a245","a246","a247",
"a248","a249","a250","a251","a252","a253","a254","a255","s36","s37","s38","s39","s40","s41","s42","s43","s44","s45","s46","s47","s48","s49","s50","s51","s52","s53","s54","s55","s56","s57","s58","s59",
"s60","s61","s62","s63","s64","s65","s66","s67","vcc","scc","memory");
}

__device__ __forceinline__ void gemm_M(char* ws, int wave, const GP& P) {
  const unsigned kind = RFL(P.kind), aoff = RFL(P.aoff), lda = RFL(P.lda), bn = RFL(P.bn), ldb = RFL(P.ldb), nk = RFL(P.nk), o0 = RFL(P.o0), o1 = RFL(P.o1);
  const unsigned long long bp = ((unsigned long long)RFL((unsigned)(P.bp >> 32)) << 32) | RFL((unsigned)P.bp);
  const unsigned l = lane_id(), w = wave, wm = w >> 1, wn = w & 1;
  const unsigned i16 = l & 15, g = l >> 4, q = (l >> 2) & 3, p = l & 3, r = l / 16;
  const unsigned c0 = (l & 7) ^ g, va0 = 2048 + 2 * w * lda + (l >> 3) * 64 + (c0 >> 2) * 1024 + (c0 & 3) * 16;
  const unsigned vb0 = (32 * wn + r) * ldb + (((l % 16) ^ (2 * (r & 3))) * 16);
  const unsigned vx0 = wm * 8192 + i16 * 128 + ((g ^ (i16 >> 1)) * 16);
  const unsigned vw0 = (8 * g + q) * 256 + (((4 * wn) ^ (q | ((g & 1) << 2))) * 32) + 8 * p;
  const unsigned gp = ((g & 1) << 1) | (g >> 1);
  const unsigned vst = kind == 0 ? gp * 256 + i16 * 16 : kind == 1 ? l * 16 : i16 * 64 + gp * 16;
  asm volatile(
"s_mov_b64 s[36:37], %[ws]\n"
"s_and_b32 s37, s37, 0xffff\n"
"s_mov_b32 s38, 0xf000000\n"
"s_mov_b32 s39, 0x20000\n"
"s_mov_b64 s[40:41], %[bp]\n"
"s_and_b32 s41, s41, 0xffff\n"
"s_mov_b32 s42, %[bn]\n"
"s_mov_b32 s43, 0x20000\n"
"s_mov_b64 s[56:57], %[ws]\n"
"s_and_b32 s57, s57, 0xffff\n"
"s_mov_b32 s58, 0xf000000\n"
"s_mov_b32 s59, 0x20000\n"
"s_sub_u32 s44, %[aoff], 0x800\n"
"s_mov_b32 s45, 0\n"
"s_mov_b32 s47, %[nk]\n"
"s_lshl_b32 s48, %[ldb], 6\n"
"s_mov_b32 s49, %[wave]\n"
"s_mul_i32 s60, s49, 4096\n"
"s_add_u32 s61, s60, 4096\n"
"s_mul_i32 s62, s49, 8192\n"
"s_add_u32 s62, s62, 16384\n"
"s_add_u32 s63, s62, 4096\n"
"v_mov_b32 v24, %[va0]\n"
"v_add_u32 v26, %[lda], v24\n"
"v_xor_b32 v25, 0x400, v24\n"
"v_add_u32 v25, 0x200, v25\n"
"v_xor_b32 v27, 0x400, v26\n"
"v_add_u32 v27, 0x200, v27\n"
"v_subrev_u32 v25, 1024, v25\n"
"v_subrev_u32 v26, 2048, v26\n"
"v_subrev_u32 v27, 3072, v27\n"
"s_mul_i32 s55, %[ldb], 4\n"
"v_mov_b32 v32, %[vb0]\n"
"v_add_u32 v33, s55, v32\n"
"v_add_u32 v34, s55, v33\n"
"v_xor_b32 v34, 128, v34\n"
"v_add_u32 v35, s55, v34\n"
"v_add_u32 v36, s55, v35\n"
"v_xor_b32 v36, 128, v36\n"
"v_add_u32 v37, s55, v36\n"
"v_add_u32 v38, s55, v37\n"
"v_xor_b32 v38, 128, v38\n"
"v_add_u32 v39, s55, v38\n"
"v_subrev_u32 v33, 1024, v33\n"
"v_subrev_u32 v34, 2048, v34\n"
"v_subrev_u32 v35, 3072, v35\n"
"v_subrev_u32 v37, 1024, v37\n"
"v_subrev_u32 v38, 2048, v38\n"
"v_subrev_u32 v39, 3072, v39\n"
"v_mov_b32 v46, %[vx0]\n"
"v_xor_b32 v47, 64, %[vx0]\n"
"v_xor_b32 v48, 0, %[vw0]\n"
"v_xor_b32 v49, 32, %[vw0]\n"
"v_xor_b32 v50, 64, %[vw0]\n"
"v_xor_b32 v51, 96, %[vw0]\n"
"s_cmp_lg_u32 %[kind], 1\n"
"s_cbranch_scc1 SW_%=\n"
"s_mov_b32 s50, 0\n"
"s_mov_b32 s51, 0\n"
"s_add_u32 m0, s50, s60\n"
"s_nop 0\n"
"buffer_load_dwordx4 v24, s[36:39], s44 offen offset:0 sc1 lds\n"
"buffer_load_dwordx4 v25, s[36:39], s44 offen offset:1024 sc1 lds\n"
"buffer_load_dwordx4 v26, s[36:39], s44 offen offset:2048 sc1 lds\n"
"buffer_load_dwordx4 v27, s[36:39], s44 offen offset:3072 sc1 lds\n"
"s_add_u32 m0, s50, s62\n"
"s_nop 0\n"
"buffer_load_dwordx4 v32, s[40:43], s45 offen offset:0 lds\n"
"buffer_load_dwordx4 v33, s[40:43], s45 offen offset:1024 lds\n"
"buffer_load_dwordx4 v34, s[40:43], s45 offen offset:2048 lds\n"
"buffer_load_dwordx4 v35, s[40:43], s45 offen offset:3072 lds\n"
"s_add_u32 m0, s50, s63\n"
"s_nop 0\n"
"buffer_load_dwordx4 v36, s[40:43], s45 offen offset:0 lds\n"
"buffer_load_dwordx4 v37, s[40:43], s45 offen offset:1024 lds\n"
"buffer_load_dwordx4 v38, s[40:43], s45 offen offset:2048 lds\n"
"buffer_load_dwordx4 v39, s[40:43], s45 offen offset:3072 lds\n"
"s_add_u32 s44, s44, 2048\n"
"s_add_u32 s45, s45, s48\n"
"s_add_u32 s50, s50, 49152\n"
"s_cmp_eq_u32 s50, 147456\n"
"s_cselect_b32 s50, 0, s50\n"
"v_mov_b64 v[128:129], 0\n"
"v_mov_b64 v[130:131], 0\n"
"v_mov_b64 v[136:137], 0\n"
"v_mov_b64 v[138:139], 0\n"
"v_mov_b64 v[144:145], 0\n"
"v_mov_b64 v[146:147], 0\n"
"v_mov_b64 v[152:153], 0\n"
"v_mov_b64 v[154:155], 0\n"
"v_mov_b64 v[160:161], 0\n"
"v_mov_b64 v[162:163], 0\n"
"v_mov_b64 v[168:169], 0\n"
"v_mov_b64 v[170:171], 0\n"
"v_mov_b64 v[176:177], 0\n"
"v_mov_b64 v[178:179], 0\n"
"v_mov_b64 v[184:185], 0\n"
"v_mov_b64 v[186:187], 0\n"
"v_mov_b64 v[192:193], 0\n"
"v_mov_b64 v[194:195], 0\n"
"v_mov_b64 v[200:201], 0\n"
"v_mov_b64 v[202:203], 0\n"
"v_mov_b64 v[208:209], 0\n"
"v_mov_b64 v[210:211], 0\n"
"v_mov_b64 v[216:217], 0\n"
"v_mov_b64 v[218:219], 0\n"
"v_mov_b64 v[224:225], 0\n"
"v_mov_b64 v[226:227], 0\n"
"v_mov_b64 v[232:233], 0\n"
"v_mov_b64 v[234:235], 0\n"
"v_mov_b64 v[240:241], 0\n"
"v_mov_b64 v[242:243], 0\n"
"v_mov_b64 v[248:249], 0\n"
"v_mov_b64 v[250:251], 0\n"
"v_mov_b64 v[132:133], 0\n"
"v_mov_b64 v[134:135], 0\n"
"v_mov_b64 v[140:141], 0\n"
"v_mov_b64 v[142:143], 0\n"
"v_mov_b64 v[148:149], 0\n"
"v_mov_b64 v[150:151], 0\n"
"v_mov_b64 v[156:157], 0\n"
"v_mov_b64 v[158:159], 0\n"
"v_mov_b64 v[164:165], 0\n"
"v_mov_b64 v[166:167], 0\n"
"v_mov_b64 v[172:173], 0\n"
"v_mov_b64 v[174:175], 0\n"
"v_mov_b64 v[180:181], 0\n"
"v_mov_b64 v[182:183], 0\n"
"v_mov_b64 v[188:189], 0\n"
"v_mov_b64 v[190:191], 0\n"
"v_mov_b64 v[196:197], 0\n"
"v_mov_b64 v[198:199], 0\n"
"v_mov_b64 v[204:205], 0\n"
"v_mov_b64 v[206:207], 0\n"
"v_mov_b64 v[212:213], 0\n"
"v_mov_b64 v[214:215], 0\n"
"v_mov_b64 v[220:221], 0\n"
"v_mov_b64 v[222:223], 0\n"
"v_mov_b64 v[228:229], 0\n"
"v_mov_b64 v[230:231], 0\n"
"v_mov_b64 v[236:237], 0\n"
"v_mov_b64 v[238:239], 0\n"
"v_mov_b64 v[244:245], 0\n"
"v_mov_b64 v[246:247], 0\n"
"v_mov_b64 v[252:253], 0\n"
"v_mov_b64 v[254:255], 0\n"
"v_add_u32 v40, s51, v46\n"
"v_add_u32 v41, s51, v47\n"
"v_add_u32 v42, s51, v48\n"
"v_add_u32 v43, s51, v49\n"
"v_add_u32 v44, s51, v50\n"
"v_add_u32 v45, s51, v51\n"
"s_add_u32 m0, s50, s60\n"
"s_nop 0\n"
"buffer_load_dwordx4 v24, s[36:39], s44 offen offset:0 sc1 lds\n"
"buffer_load_dwordx4 v25, s[36:39], s44 offen offset:1024 sc1 lds\n"
"buffer_load_dwordx4 v26, s[36:39], s44 offen offset:2048 sc1 lds\n"
"buffer_load_dwordx4 v27, s[36:39], s44 offen offset:3072 sc1 lds\n"
"s_add_u32 m0, s50, s62\n"
"s_nop 0\n"
"buffer_load_dwordx4 v32, s[40:43], s45 offen offset:0 lds\n"
"buffer_load_dwordx4 v33, s[40:43], s45 offen offset:1024 lds\n"
"buffer_load_dwordx4 v34, s[40:43], s45 offen offset:2048 lds\n"
"buffer_load_dwordx4 v35, s[40:43], s45 offen offset:3072 lds\n"
"s_add_u32 m0, s50, s63\n"
"s_nop 0\n"
"buffer_load_dwordx4 v36, s[40:43], s45 offen offset:0 lds\n"
"buffer_load_dwordx4 v37, s[40:43], s45 offen offset:1024 lds\n"
"buffer_load_dwordx4 v38, s[40:43], s45 offen offset:2048 lds\n"
"buffer_load_dwordx4 v39, s[40:43], s45 offen offset:3072 lds\n"
"s_add_u32 s44, s44, 2048\n"
"s_add_u32 s45, s45, s48\n"
"s_add_u32 s50, s50, 49152\n"
"s_cmp_eq_u32 s50, 147456\n"
"s_cselect_b32 s50, 0, s50\n"
"s_add_u32 m0, s50, s60\n"
"s_nop 0\n"
"buffer_load_dwordx4 v24, s[36:39], s44 offen offset:0 sc1 lds\n"
"buffer_load_dwordx4 v25, s[36:39], s44 offen offset:1024 sc1 lds\n"
"buffer_load_dwordx4 v26, s[36:39], s44 offen offset:2048 sc1 lds\n"
"buffer_load_dwordx4 v27, s[36:39], s44 offen offset:3072 sc1 lds\n"
"s_add_u32 s44, s44, 2048\n"
"s_waitcnt vmcnt(16)\n"
"s_barrier\n"
"ds_read_b64_tr_b16 a[160:161], v42 offset:16384\n"
"ds_read_b64_tr_b16 a[162:163], v42 offset:17408\n"
"ds_read_b128 a[128:131], v40 offset:0\n"
"ds_read_b128 a[132:135], v40 offset:2048\n"
"ds_read_b128 a[136:139], v40 offset:4096\n"
"ds_read_b128 a[140:143], v40 offset:6144\n"
"ds_read_b64_tr_b16 a[164:165], v43 offset:16384\n"
"ds_read_b64_tr_b16 a[166:167], v43 offset:17408\n"
"ds_read_b64_tr_b16 a[168:169], v44 offset:16384\n"
"ds_read_b64_tr_b16 a[170:171], v44 offset:17408\n"
"ds_read_b64_tr_b16 a[172:173], v45 offset:16384\n"
"ds_read_b64_tr_b16 a[174:175], v45 offset:17408\n"
"ds_read_b64_tr_b16 a[176:177], v42 offset:32768\n"
"ds_read_b64_tr_b16 a[178:179], v42 offset:33792\n"
"ds_read_b64_tr_b16 a[180:181], v43 offset:32768\n"
"ds_read_b64_tr_b16 a[182:183], v43 offset:33792\n"
"ds_read_b64_tr_b16 a[184:185], v44 offset:32768\n"
"ds_read_b64_tr_b16 a[186:187], v44 offset:33792\n"
"ds_read_b64_tr_b16 a[188:189], v45 offset:32768\n"
"ds_read_b64_tr_b16 a[190:191], v45 offset:33792\n"
"s_sub_u32 s46, s47, 3\n"
"s_waitcnt lgkmcnt(0)\n"
"KL_u_%=:\n"
"v_mfma_f32_16x16x32_bf16 v[128:131], a[128:131], a[160:163], v[128:131]\n"
"ds_read_b64_tr_b16 a[224:225], v42 offset:24576\n"
"s_add_u32 m0, s50, s62\n"
"v_mfma_f32_16x16x32_bf16 v[132:135], a[132:135], a[160:163], v[132:135]\n"
"ds_read_b64_tr_b16 a[226:227], v42 offset:25600\n"
"buffer_load_dwordx4 v32, s[40:43], s45 offen offset:0 lds\n"
"v_mfma_f32_16x16x32_bf16 v[136:139], a[136:139], a[160:163], v[136:139]\n"
"ds_read_b128 a[192:195], v41 offset:0\n"
"v_mfma_f32_16x16x32_bf16 v[140:143], a[140:143], a[160:163], v[140:143]\n"
"v_mfma_f32_16x16x32_bf16 v[144:147], a[128:131], a[164:167], v[144:147]\n"
"ds_read_b128 a[196:199], v41 offset:2048\n"
"v_mfma_f32_16x16x32_bf16 v[148:151], a[132:135], a[164:167], v[148:151]\n"
"ds_read_b128 a[200:203], v41 offset:4096\n"
"buffer_load_dwordx4 v33, s[40:43], s45 offen offset:1024 lds\n"
"v_mfma_f32_16x16x32_bf16 v[152:155], a[136:139], a[164:167], v[152:155]\n"
"v_mfma_f32_16x16x32_bf16 v[156:159], a[140:143], a[164:167], v[156:159]\n"
"ds_read_b128 a[204:207], v41 offset:6144\n"
"v_mfma_f32_16x16x32_bf16 v[160:163], a[128:131], a[168:171], v[160:163]\n"
"ds_read_b64_tr_b16 a[228:229], v43 offset:24576\n"
"v_mfma_f32_16x16x32_bf16 v[164:167], a[132:135], a[168:171], v[164:167]\n"
"buffer_load_dwordx4 v34, s[40:43], s45 offen offset:2048 lds\n"
"v_mfma_f32_16x16x32_bf16 v[168:171], a[136:139], a[168:171], v[168:171]\n"
"ds_read_b64_tr_b16 a[230:231], v43 offset:25600\n"
"v_mfma_f32_16x16x32_bf16 v[172:175], a[140:143], a[168:171], v[172:175]\n"
"ds_read_b64_tr_b16 a[232:233], v44 offset:24576\n"
"v_mfma_f32_16x16x32_bf16 v[176:179], a[128:131], a[172:175], v[176:179]\n"
"v_mfma_f32_16x16x32_bf16 v[180:183], a[132:135], a[172:175], v[180:183]\n"
"ds_read_b64_tr_b16 a[234:235], v44 offset:25600\n"
"buffer_load_dwordx4 v35, s[40:43], s45 offen offset:3072 lds\n"
"v_mfma_f32_16x16x32_bf16 v[184:187], a[136:139], a[172:175], v[184:187]\n"
"ds_read_b64_tr_b16 a[236:237], v45 offset:24576\n"
"v_mfma_f32_16x16x32_bf16 v[188:191], a[140:143], a[172:175], v[188:191]\n"
"ds_read_b64_tr_b16 a[238:239], v45 offset:25600\n"
"v_mfma_f32_16x16x32_bf16 v[192:195], a[128:131], a[176:179], v[192:195]\n"
"s_add_u32 m0, s50, s63\n"
"v_mfma_f32_16x16x32_bf16 v[196:199], a[132:135], a[176:179], v[196:199]\n"
"ds_read_b64_tr_b16 a[240:241], v42 offset:40960\n"
"buffer_load_dwordx4 v36, s[40:43], s45 offen offset:0 lds\n"
"v_mfma_f32_16x16x32_bf16 v[200:203], a[136:139], a[176:179], v[200:203]\n"
"ds_read_b64_tr_b16 a[242:243], v42 offset:41984\n"
"v_mfma_f32_16x16x32_bf16 v[204:207], a[140:143], a[176:179], v[204:207]\n"
"v_mfma_f32_16x16x32_bf16 v[208:211], a[128:131], a[180:183], v[208:211]\n"
"ds_read_b64_tr_b16 a[244:245], v43 offset:40960\n"
"v_mfma_f32_16x16x32_bf16 v[212:215], a[132:135], a[180:183], v[212:215]\n"
"ds_read_b64_tr_b16 a[246:247], v43 offset:41984\n"
"buffer_load_dwordx4 v37, s[40:43], s45 offen offset:1024 lds\n"
"v_mfma_f32_16x16x32_bf16 v[216:219], a[136:139], a[180:183], v[216:219]\n"
"v_mfma_f32_16x16x32_bf16 v[220:223], a[140:143], a[180:183], v[220:223]\n"
"ds_read_b64_tr_b16 a[248:249], v44 offset:40960\n"
"v_mfma_f32_16x16x32_bf16 v[224:227], a[128:131], a[184:187], v[224:227]\n"
"ds_read_b64_tr_b16 a[250:251], v44 offset:41984\n"
"v_mfma_f32_16x16x32_bf16 v[228:231], a[132:135], a[184:187], v[228:231]\n"
"buffer_load_dwordx4 v38, s[40:43], s45 offen offset:2048 lds\n"
"v_mfma_f32_16x16x32_bf16 v[232:235], a[136:139], a[184:187], v[232:235]\n"
"ds_read_b64_tr_b16 a[252:253], v45 offset:40960\n"
"v_mfma_f32_16x16x32_bf16 v[236:239], a[140:143], a[184:187], v[236:239]\n"
"ds_read_b64_tr_b16 a[254:255], v45 offset:41984\n"
"v_mfma_f32_16x16x32_bf16 v[240:243], a[128:131], a[188:191], v[240:243]\n"
"v_mfma_f32_16x16x32_bf16 v[244:247], a[132:135], a[188:191], v[244:247]\n"
"buffer_load_dwordx4 v39, s[40:43], s45 offen offset:3072 lds\n"
"v_mfma_f32_16x16x32_bf16 v[248:251], a[136:139], a[188:191], v[248:251]\n"
"s_add_u32 s45, s45, s48\n"
"s_add_u32 s50, s50, 49152\n"
"s_cmp_eq_u32 s50, 147456\n"
"s_cselect_b32 s50, 0, s50\n"
"s_add_u32 s51, s51, 49152\n"
"s_cmp_eq_u32 s51, 147456\n"
"s_cselect_b32 s51, 0, s51\n"
"v_mfma_f32_16x16x32_bf16 v[252:255], a[140:143], a[188:191], v[252:255]\n"
"s_waitcnt lgkmcnt(0) vmcnt(12)\n"
"s_barrier\n"
"v_mfma_f32_16x16x32_bf16 v[128:131], a[192:195], a[224:227], v[128:131]\n"
"v_add_u32 v40, s51, v46\n"
"v_add_u32 v41, s51, v47\n"
"s_add_u32 m0, s50, s60\n"
"v_mfma_f32_16x16x32_bf16 v[132:135], a[196:199], a[224:227], v[132:135]\n"
"v_add_u32 v42, s51, v48\n"
"v_add_u32 v43, s51, v49\n"
"buffer_load_dwordx4 v24, s[36:39], s44 offen offset:0 sc1 lds\n"
"v_mfma_f32_16x16x32_bf16 v[136:139], a[200:203], a[224:227], v[136:139]\n"
"v_add_u32 v44, s51, v50\n"
"v_add_u32 v45, s51, v51\n"
"v_mfma_f32_16x16x32_bf16 v[140:143], a[204:207], a[224:227], v[140:143]\n"
"ds_read_b64_tr_b16 a[160:161], v42 offset:16384\n"
"v_mfma_f32_16x16x32_bf16 v[144:147], a[192:195], a[228:231], v[144:147]\n"
"ds_read_b64_tr_b16 a[162:163], v42 offset:17408\n"
"v_mfma_f32_16x16x32_bf16 v[148:151], a[196:199], a[228:231], v[148:151]\n"
"ds_read_b128 a[128:131], v40 offset:0\n"
"v_mfma_f32_16x16x32_bf16 v[152:155], a[200:203], a[228:231], v[152:155]\n"
"v_mfma_f32_16x16x32_bf16 v[156:159], a[204:207], a[228:231], v[156:159]\n"
"ds_read_b128 a[132:135], v40 offset:2048\n"
"v_mfma_f32_16x16x32_bf16 v[160:163], a[192:195], a[232:235], v[160:163]\n"
"ds_read_b128 a[136:139], v40 offset:4096\n"
"v_mfma_f32_16x16x32_bf16 v[164:167], a[196:199], a[232:235], v[164:167]\n"
"ds_read_b128 a[140:143], v40 offset:6144\n"
"buffer_load_dwordx4 v25, s[36:39], s44 offen offset:1024 sc1 lds\n"
"v_mfma_f32_16x16x32_bf16 v[168:171], a[200:203], a[232:235], v[168:171]\n"
"v_mfma_f32_16x16x32_bf16 v[172:175], a[204:207], a[232:235], v[172:175]\n"
"ds_read_b64_tr_b16 a[164:165], v43 offset:16384\n"
"v_mfma_f32_16x16x32_bf16 v[176:179], a[192:195], a[236:239], v[176:179]\n"
"ds_read_b64_tr_b16 a[166:167], v43 offset:17408\n"
"v_mfma_f32_16x16x32_bf16 v[180:183], a[196:199], a[236:239], v[180:183]\n"
"ds_read_b64_tr_b16 a[168:169], v44 offset:16384\n"
"v_mfma_f32_16x16x32_bf16 v[184:187], a[200:203], a[236:239], v[184:187]\n"
"v_mfma_f32_16x16x32_bf16 v[188:191], a[204:207], a[236:239], v[188:191]\n"
"ds_read_b64_tr_b16 a[170:171], v44 offset:17408\n"
"v_mfma_f32_16x16x32_bf16 v[192:195], a[192:195], a[240:243], v[192:195]\n"
"ds_read_b64_tr_b16 a[172:173], v45 offset:16384\n"
"v_mfma_f32_16x16x32_bf16 v[196:199], a[196:199], a[240:243], v[196:199]\n"
"ds_read_b64_tr_b16 a[174:175], v45 offset:17408\n"
"buffer_load_dwordx4 v26, s[36:39], s44 offen offset:2048 sc1 lds\n"
"v_mfma_f32_16x16x32_bf16 v[200:203], a[200:203], a[240:243], v[200:203]\n"
"v_mfma_f32_16x16x32_bf16 v[204:207], a[204:207], a[240:243], v[204:207]\n"
"ds_read_b64_tr_b16 a[176:177], v42 offset:32768\n"
"v_mfma_f32_16x16x32_bf16 v[208:211], a[192:195], a[244:247], v[208:211]\n"
"ds_read_b64_tr_b16 a[178:179], v42 offset:33792\n"
"v_mfma_f32_16x16x32_bf16 v[212:215], a[196:199], a[244:247], v[212:215]\n"
"ds_read_b64_tr_b16 a[180:181], v43 offset:32768\n"
"v_mfma_f32_16x16x32_bf16 v[216:219], a[200:203], a[244:247], v[216:219]\n"
"v_mfma_f32_16x16x32_bf16 v[220:223], a[204:207], a[244:247], v[220:223]\n"
"ds_read_b64_tr_b16 a[182:183], v43 offset:33792\n"
"v_mfma_f32_16x16x32_bf16 v[224:227], a[192:195], a[248:251], v[224:227]\n"
"ds_read_b64_tr_b16 a[184:185], v44 offset:32768\n"
"v_mfma_f32_16x16x32_bf16 v[228:231], a[196:199], a[248:251], v[228:231]\n"
"ds_read_b64_tr_b16 a[186:187], v44 offset:33792\n"
"buffer_load_dwordx4 v27, s[36:39], s44 offen offset:3072 sc1 lds\n"
"v_mfma_f32_16x16x32_bf16 v[232:235], a[200:203], a[248:251], v[232:235]\n"
"v_mfma_f32_16x16x32_bf16 v[236:239], a[204:207], a[248:251], v[236:239]\n"
"ds_read_b64_tr_b16 a[188:189], v45 offset:32768\n"
"v_mfma_f32_16x16x32_bf16 v[240:243], a[192:195], a[252:255], v[240:243]\n"
"ds_read_b64_tr_b16 a[190:191], v45 offset:33792\n"
"v_mfma_f32_16x16x32_bf16 v[244:247], a[196:199], a[252:255], v[244:247]\n"
"v_mfma_f32_16x16x32_bf16 v[248:251], a[200:203], a[252:255], v[248:251]\n"
"s_add_u32 s44, s44, 2048\n"
"v_mfma_f32_16x16x32_bf16 v[252:255], a[204:207], a[252:255], v[252:255]\n"
"s_sub_u32 s46, s46, 1\n"
"s_cmp_lg_u32 s46, 0\n"
"s_waitcnt lgkmcnt(0)\n"
"s_cbranch_scc1 KL_u_%=\n"
"v_mfma_f32_16x16x32_bf16 v[128:131], a[128:131], a[160:163], v[128:131]\n"
"ds_read_b64_tr_b16 a[224:225], v42 offset:24576\n"
"s_add_u32 m0, s50, s62\n"
"v_mfma_f32_16x16x32_bf16 v[132:135], a[132:135], a[160:163], v[132:135]\n"
"ds_read_b64_tr_b16 a[226:227], v42 offset:25600\n"
"buffer_load_dwordx4 v32, s[40:43], s45 offen offset:0 lds\n"
"v_mfma_f32_16x16x32_bf16 v[136:139], a[136:139], a[160:163], v[136:139]\n"
"ds_read_b128 a[192:195], v41 offset:0\n"
"v_mfma_f32_16x16x32_bf16 v[140:143], a[140:143], a[160:163], v[140:143]\n"
"v_mfma_f32_16x16x32_bf16 v[144:147], a[128:131], a[164:167], v[144:147]\n"
"ds_read_b128 a[196:199], v41 offset:2048\n"
"v_mfma_f32_16x16x32_bf16 v[148:151], a[132:135], a[164:167], v[148:151]\n"
"ds_read_b128 a[200:203], v41 offset:4096\n"
"buffer_load_dwordx4 v33, s[40:43], s45 offen offset:1024 lds\n"
"v_mfma_f32_16x16x32_bf16 v[152:155], a[136:139], a[164:167], v[152:155]\n"
"v_mfma_f32_16x16x32_bf16 v[156:159], a[140:143], a[164:167], v[156:159]\n"
"ds_read_b128 a[204:207], v41 offset:6144\n"
"v_mfma_f32_16x16x32_bf16 v[160:163], a[128:131], a[168:171], v[160:163]\n"
"ds_read_b64_tr_b16 a[228:229], v43 offset:24576\n"
"v_mfma_f32_16x16x32_bf16 v[164:167], a[132:135], a[168:171], v[164:167]\n"
"buffer_load_dwordx4 v34, s[40:43], s45 offen offset:2048 lds\n"
"v_mfma_f32_16x16x32_bf16 v[168:171], a[136:139], a[168:171], v[168:171]\n"
"ds_read_b64_tr_b16 a[230:231], v43 offset:25600\n"
"v_mfma_f32_16x16x32_bf16 v[172:175], a[140:143], a[168:171], v[172:175]\n"
"ds_read_b64_tr_b16 a[232:233], v44 offset:24576\n"
"v_mfma_f32_16x16x32_bf16 v[176:179], a[128:131], a[172:175], v[176:179]\n"
"v_mfma_f32_16x16x32_bf16 v[180:183], a[132:135], a[172:175], v[180:183]\n"
"ds_read_b64_tr_b16 a[234:235], v44 offset:25600\n"
"buffer_load_dwordx4 v35, s[40:43], s45 offen offset:3072 lds\n"
"v_mfma_f32_16x16x32_bf16 v[184:187], a[136:139], a[172:175], v[184:187]\n"
"ds_read_b64_tr_b16 a[236:237], v45 offset:24576\n"
"v_mfma_f32_16x16x32_bf16 v[188:191], a[140:143], a[172:175], v[188:191]\n"
"ds_read_b64_tr_b16 a[238:239], v45 offset:25600\n"
"v_mfma_f32_16x16x32_bf16 v[192:195], a[128:131], a[176:179], v[192:195]\n"
"s_add_u32 m0, s50, s63\n"
"v_mfma_f32_16x16x32_bf16 v[196:199], a[132:135], a[176:179], v[196:199]\n"
"ds_read_b64_tr_b16 a[240:241], v42 offset:40960\n"
"buffer_load_dwordx4 v36, s[40:43], s45 offen offset:0 lds\n"
"v_mfma_f32_16x16x32_bf16 v[200:203], a[136:139], a[176:179], v[200:203]\n"
"ds_read_b64_tr_b16 a[242:243], v42 offset:41984\n"
"v_mfma_f32_16x16x32_bf16 v[204:207], a[140:143], a[176:179], v[204:207]\n"
"v_mfma_f32_16x16x32_bf16 v[208:211], a[128:131], a[180:183], v[208:211]\n"
"ds_read_b64_tr_b16 a[244:245], v43 offset:40960\n"
"v_mfma_f32_16x16x32_bf16 v[212:215], a[132:135], a[180:183], v[212:215]\n"
"ds_read_b64_tr_b16 a[246:247], v43 offset:41984\n"
"buffer_load_dwordx4 v37, s[40:43], s45 offen offset:1024 lds\n"
"v_mfma_f32_16x16x32_bf16 v[216:219], a[136:139], a[180:183], v[216:219]\n"
"v_mfma_f32_16x16x32_bf16 v[220:223], a[140:143], a[180:183], v[220:223]\n"
"ds_read_b64_tr_b16 a[248:249], v44 offset:40960\n"
"v_mfma_f32_16x16x32_bf16 v[224:227], a[128:131], a[184:187], v[224:227]\n"
"ds_read_b64_tr_b16 a[250:251], v44 offset:41984\n"
"v_mfma_f32_16x16x32_bf16 v[228:231], a[132:135], a[184:187], v[228:231]\n"
"buffer_load_dwordx4 v38, s[40:43], s45 offen offset:2048 lds\n"
"v_mfma_f32_16x16x32_bf16 v[232:235], a[136:139], a[184:187], v[232:235]\n"
"ds_read_b64_tr_b16 a[252:253], v45 offset:40960\n"
"v_mfma_f32_16x16x32_bf16 v[236:239], a[140:143], a[184:187], v[236:239]\n"
"ds_read_b64_tr_b16 a[254:255], v45 offset:41984\n"
"v_mfma_f32_16x16x32_bf16 v[240:243], a[128:131], a[188:191], v[240:243]\n"
"v_mfma_f32_16x16x32_bf16 v[244:247], a[132:135], a[188:191], v[244:247]\n"
"buffer_load_dwordx4 v39, s[40:43], s45 offen offset:3072 lds\n"
"v_mfma_f32_16x16x32_bf16 v[248:251], a[136:139], a[188:191], v[248:251]\n"
"s_add_u32 s45, s45, s48\n"
"s_add_u32 s50, s50, 49152\n"
"s_cmp_eq_u32 s50, 147456\n"
"s_cselect_b32 s50, 0, s50\n"
"s_add_u32 s51, s51, 49152\n"
"s_cmp_eq_u32 s51, 147456\n"
"s_cselect_b32 s51, 0, s51\n"
"v_mfma_f32_16x16x32_bf16 v[252:255], a[140:143], a[188:191], v[252:255]\n"
"s_waitcnt lgkmcnt(0) vmcnt(12)\n"
"s_barrier\n"
"v_mfma_f32_16x16x32_bf16 v[128:131], a[192:195], a[224:227], v[128:131]\n"
"v_add_u32 v40, s51, v46\n"
"v_add_u32 v41, s51, v47\n"
"v_mfma_f32_16x16x32_bf16 v[132:135], a[196:199], a[224:227], v[132:135]\n"
"v_add_u32 v42, s51, v48\n"
"v_add_u32 v43, s51, v49\n"
"v_mfma_f32_16x16x32_bf16 v[136:139], a[200:203], a[224:227], v[136:139]\n"
"v_add_u32 v44, s51, v50\n"
"v_add_u32 v45, s51, v51\n"
"v_mfma_f32_16x16x32_bf16 v[140:143], a[204:207], a[224:227], v[140:143]\n"
"ds_read_b64_tr_b16 a[160:161], v42 offset:16384\n"
"v_mfma_f32_16x16x32_bf16 v[144:147], a[192:195], a[228:231], v[144:147]\n"
"ds_read_b64_tr_b16 a[162:163], v42 offset:17408\n"
"v_mfma_f32_16x16x32_bf16 v[148:151], a[196:199], a[228:231], v[148:151]\n"
"ds_read_b128 a[128:131], v40 offset:0\n"
"v_mfma_f32_16x16x32_bf16 v[152:155], a[200:203], a[228:231], v[152:155]\n"
"v_mfma_f32_16x16x32_bf16 v[156:159], a[204:207], a[228:231], v[156:159]\n"
"ds_read_b128 a[132:135], v40 offset:2048\n"
"v_mfma_f32_16x16x32_bf16 v[160:163], a[192:195], a[232:235], v[160:163]\n"
"ds_read_b128 a[136:139], v40 offset:4096\n"
"v_mfma_f32_16x16x32_bf16 v[164:167], a[196:199], a[232:235], v[164:167]\n"
"ds_read_b128 a[140:143], v40 offset:6144\n"
"v_mfma_f32_16x16x32_bf16 v[168:171], a[200:203], a[232:235], v[168:171]\n"
"v_mfma_f32_16x16x32_bf16 v[172:175], a[204:207], a[232:235], v[172:175]\n"
"ds_read_b64_tr_b16 a[164:165], v43 offset:16384\n"
"v_mfma_f32_16x16x32_bf16 v[176:179], a[192:195], a[236:239], v[176:179]\n"
"ds_read_b64_tr_b16 a[166:167], v43 offset:17408\n"
"v_mfma_f32_16x16x32_bf16 v[180:183], a[196:199], a[236:239], v[180:183]\n"
"ds_read_b64_tr_b16 a[168:169], v44 offset:16384\n"
"v_mfma_f32_16x16x32_bf16 v[184:187], a[200:203], a[236:239], v[184:187]\n"
"v_mfma_f32_16x16x32_bf16 v[188:191], a[204:207], a[236:239], v[188:191]\n"
"ds_read_b64_tr_b16 a[170:171], v44 offset:17408\n"
"v_mfma_f32_16x16x32_bf16 v[192:195], a[192:195], a[240:243], v[192:195]\n"
"ds_read_b64_tr_b16 a[172:173], v45 offset:16384\n"
"v_mfma_f32_16x16x32_bf16 v[196:199], a[196:199], a[240:243], v[196:199]\n"
"ds_read_b64_tr_b16 a[174:175], v45 offset:17408\n"
"v_mfma_f32_16x16x32_bf16 v[200:203], a[200:203], a[240:243], v[200:203]\n"
"v_mfma_f32_16x16x32_bf16 v[204:207], a[204:207], a[240:243], v[204:207]\n"
"ds_read_b64_tr_b16 a[176:177], v42 offset:32768\n"
"v_mfma_f32_16x16x32_bf16 v[208:211], a[192:195], a[244:247], v[208:211]\n"
"ds_read_b64_tr_b16 a[178:179], v42 offset:33792\n"
"v_mfma_f32_16x16x32_bf16 v[212:215], a[196:199], a[244:247], v[212:215]\n"
"ds_read_b64_tr_b16 a[180:181], v43 offset:32768\n"
"v_mfma_f32_16x16x32_bf16 v[216:219], a[200:203], a[244:247], v[216:219]\n"
"v_mfma_f32_16x16x32_bf16 v[220:223], a[204:207], a[244:247], v[220:223]\n"
"ds_read_b64_tr_b16 a[182:183], v43 offset:33792\n"
"v_mfma_f32_16x16x32_bf16 v[224:227], a[192:195], a[248:251], v[224:227]\n"
"ds_read_b64_tr_b16 a[184:185], v44 offset:32768\n"
"v_mfma_f32_16x16x32_bf16 v[228:231], a[196:199], a[248:251], v[228:231]\n"
"ds_read_b64_tr_b16 a[186:187], v44 offset:33792\n"
"v_mfma_f32_16x16x32_bf16 v[232:235], a[200:203], a[248:251], v[232:235]\n"
"v_mfma_f32_16x16x32_bf16 v[236:239], a[204:207], a[248:251], v[236:239]\n"
"ds_read_b64_tr_b16 a[188:189], v45 offset:32768\n"
"v_mfma_f32_16x16x32_bf16 v[240:243], a[192:195], a[252:255], v[240:243]\n"
"ds_read_b64_tr_b16 a[190:191], v45 offset:33792\n"
"v_mfma_f32_16x16x32_bf16 v[244:247], a[196:199], a[252:255], v[244:247]\n"
"v_mfma_f32_16x16x32_bf16 v[248:251], a[200:203], a[252:255], v[248:251]\n"
"v_mfma_f32_16x16x32_bf16 v[252:255], a[204:207], a[252:255], v[252:255]\n"
"s_waitcnt lgkmcnt(0)\n"
"v_mfma_f32_16x16x32_bf16 v[128:131], a[128:131], a[160:163], v[128:131]\n"
"ds_read_b64_tr_b16 a[224:225], v42 offset:24576\n"
"v_mfma_f32_16x16x32_bf16 v[132:135], a[132:135], a[160:163], v[132:135]\n"
"ds_read_b64_tr_b16 a[226:227], v42 offset:25600\n"
"v_mfma_f32_16x16x32_bf16 v[136:139], a[136:139], a[160:163], v[136:139]\n"
"ds_read_b128 a[192:195], v41 offset:0\n"
"v_mfma_f32_16x16x32_bf16 v[140:143], a[140:143], a[160:163], v[140:143]\n"
"v_mfma_f32_16x16x32_bf16 v[144:147], a[128:131], a[164:167], v[144:147]\n"
"ds_read_b128 a[196:199], v41 offset:2048\n"
"v_mfma_f32_16x16x32_bf16 v[148:151], a[132:135], a[164:167], v[148:151]\n"
"ds_read_b128 a[200:203], v41 offset:4096\n"
"v_mfma_f32_16x16x32_bf16 v[152:155], a[136:139], a[164:167], v[152:155]\n"
"v_mfma_f32_16x16x32_bf16 v[156:159], a[140:143], a[164:167], v[156:159]\n"
"ds_read_b128 a[204:207], v41 offset:6144\n"
"v_mfma_f32_16x16x32_bf16 v[160:163], a[128:131], a[168:171], v[160:163]\n"
"ds_read_b64_tr_b16 a[228:229], v43 offset:24576\n"
"v_mfma_f32_16x16x32_bf16 v[164:167], a[132:135], a[168:171], v[164:167]\n"
"v_mfma_f32_16x16x32_bf16 v[168:171], a[136:139], a[168:171], v[168:171]\n"
"ds_read_b64_tr_b16 a[230:231], v43 offset:25600\n"
"v_mfma_f32_16x16x32_bf16 v[172:175], a[140:143], a[168:171], v[172:175]\n"
"ds_read_b64_tr_b16 a[232:233], v44 offset:24576\n"
"v_mfma_f32_16x16x32_bf16 v[176:179], a[128:131], a[172:175], v[176:179]\n"
"v_mfma_f32_16x16x32_bf16 v[180:183], a[132:135], a[172:175], v[180:183]\n"
"ds_read_b64_tr_b16 a[234:235], v44 offset:25600\n"
"v_mfma_f32_16x16x32_bf16 v[184:187], a[136:139], a[172:175], v[184:187]\n"
"ds_read_b64_tr_b16 a[236:237], v45 offset:24576\n"
"v_mfma_f32_16x16x32_bf16 v[188:191], a[140:143], a[172:175], v[188:191]\n"
"ds_read_b64_tr_b16 a[238:239], v45 offset:25600\n"
"v_mfma_f32_16x16x32_bf16 v[192:195], a[128:131], a[176:179], v[192:195]\n"
"v_mfma_f32_16x16x32_bf16 v[196:199], a[132:135], a[176:179], v[196:199]\n"
"ds_read_b64_tr_b16 a[240:241], v42 offset:40960\n"
"v_mfma_f32_16x16x32_bf16 v[200:203], a[136:139], a[176:179], v[200:203]\n"
"ds_read_b64_tr_b16 a[242:243], v42 offset:41984\n"
"v_mfma_f32_16x16x32_bf16 v[204:207], a[140:143], a[176:179], v[204:207]\n"
"v_mfma_f32_16x16x32_bf16 v[208:211], a[128:131], a[180:183], v[208:211]\n"
"ds_read_b64_tr_b16 a[244:245], v43 offset:40960\n"
"v_mfma_f32_16x16x32_bf16 v[212:215], a[132:135], a[180:183], v[212:215]\n"
"ds_read_b64_tr_b16 a[246:247], v43 offset:41984\n"
"v_mfma_f32_16x16x32_bf16 v[216:219], a[136:139], a[180:183], v[216:219]\n"
"v_mfma_f32_16x16x32_bf16 v[220:223], a[140:143], a[180:183], v[220:223]\n"
"ds_read_b64_tr_b16 a[248:249], v44 offset:40960\n"
"v_mfma_f32_16x16x32_bf16 v[224:227], a[128:131], a[184:187], v[224:227]\n"
"ds_read_b64_tr_b16 a[250:251], v44 offset:41984\n"
"v_mfma_f32_16x16x32_bf16 v[228:231], a[132:135], a[184:187], v[228:231]\n"
"v_mfma_f32_16x16x32_bf16 v[232:235], a[136:139], a[184:187], v[232:235]\n"
"ds_read_b64_tr_b16 a[252:253], v45 offset:40960\n"
"v_mfma_f32_16x16x32_bf16 v[236:239], a[140:143], a[184:187], v[236:239]\n"
"ds_read_b64_tr_b16 a[254:255], v45 offset:41984\n"
"v_mfma_f32_16x16x32_bf16 v[240:243], a[128:131], a[188:191], v[240:243]\n"
"v_mfma_f32_16x16x32_bf16 v[244:247], a[132:135], a[188:191], v[244:247]\n"
"v_mfma_f32_16x16x32_bf16 v[248:251], a[136:139], a[188:191], v[248:251]\n"
"s_add_u32 s51, s51, 49152\n"
"s_cmp_eq_u32 s51, 147456\n"
"s_cselect_b32 s51, 0, s51\n"
"v_mfma_f32_16x16x32_bf16 v[252:255], a[140:143], a[188:191], v[252:255]\n"
"s_waitcnt lgkmcnt(0) vmcnt(0)\n"
"s_barrier\n"
"v_mfma_f32_16x16x32_bf16 v[128:131], a[192:195], a[224:227], v[128:131]\n"
"v_add_u32 v40, s51, v46\n"
"v_add_u32 v41, s51, v47\n"
"v_mfma_f32_16x16x32_bf16 v[132:135], a[196:199], a[224:227], v[132:135]\n"
"v_add_u32 v42, s51, v48\n"
"v_add_u32 v43, s51, v49\n"
"v_mfma_f32_16x16x32_bf16 v[136:139], a[200:203], a[224:227], v[136:139]\n"
"v_add_u32 v44, s51, v50\n"
"v_add_u32 v45, s51, v51\n"
"v_mfma_f32_16x16x32_bf16 v[140:143], a[204:207], a[224:227], v[140:143]\n"
"ds_read_b64_tr_b16 a[160:161], v42 offset:16384\n"
"v_mfma_f32_16x16x32_bf16 v[144:147], a[192:195], a[228:231], v[144:147]\n"
"ds_read_b64_tr_b16 a[162:163], v42 offset:17408\n"
"v_mfma_f32_16x16x32_bf16 v[148:151], a[196:199], a[228:231], v[148:151]\n"
"ds_read_b128 a[128:131], v40 offset:0\n"
"v_mfma_f32_16x16x32_bf16 v[152:155], a[200:203], a[228:231], v[152:155]\n"
"v_mfma_f32_16x16x32_bf16 v[156:159], a[204:207], a[228:231], v[156:159]\n"
"ds_read_b128 a[132:135], v40 offset:2048\n"
"v_mfma_f32_16x16x32_bf16 v[160:163], a[192:195], a[232:235], v[160:163]\n"
"ds_read_b128 a[136:139], v40 offset:4096\n"
"v_mfma_f32_16x16x32_bf16 v[164:167], a[196:199], a[232:235], v[164:167]\n"
"ds_read_b128 a[140:143], v40 offset:6144\n"
"v_mfma_f32_16x16x32_bf16 v[168:171], a[200:203], a[232:235], v[168:171]\n"
"v_mfma_f32_16x16x32_bf16 v[172:175], a[204:207], a[232:235], v[172:175]\n"
"ds_read_b64_tr_b16 a[164:165], v43 offset:16384\n"
"v_mfma_f32_16x16x32_bf16 v[176:179], a[192:195], a[236:239], v[176:179]\n"
"ds_read_b64_tr_b16 a[166:167], v43 offset:17408\n"
"v_mfma_f32_16x16x32_bf16 v[180:183], a[196:199], a[236:239], v[180:183]\n"
"ds_read_b64_tr_b16 a[168:169], v44 offset:16384\n"
"v_mfma_f32_16x16x32_bf16 v[184:187], a[200:203], a[236:239], v[184:187]\n"
"v_mfma_f32_16x16x32_bf16 v[188:191], a[204:207], a[236:239], v[188:191]\n"
"ds_read_b64_tr_b16 a[170:171], v44 offset:17408\n"
"v_mfma_f32_16x16x32_bf16 v[192:195], a[192:195], a[240:243], v[192:195]\n"
"ds_read_b64_tr_b16 a[172:173], v45 offset:16384\n"
"v_mfma_f32_16x16x32_bf16 v[196:199], a[196:199], a[240:243], v[196:199]\n"
"ds_read_b64_tr_b16 a[174:175], v45 offset:17408\n"
"v_mfma_f32_16x16x32_bf16 v[200:203], a[200:203], a[240:243], v[200:203]\n"
"v_mfma_f32_16x16x32_bf16 v[204:207], a[204:207], a[240:243], v[204:207]\n"
"ds_read_b64_tr_b16 a[176:177], v42 offset:32768\n"
"v_mfma_f32_16x16x32_bf16 v[208:211], a[192:195], a[244:247], v[208:211]\n"
"ds_read_b64_tr_b16 a[178:179], v42 offset:33792\n"
"v_mfma_f32_16x16x32_bf16 v[212:215], a[196:199], a[244:247], v[212:215]\n"
"ds_read_b64_tr_b16 a[180:181], v43 offset:32768\n"
"v_mfma_f32_16x16x32_bf16 v[216:219], a[200:203], a[244:247], v[216:219]\n"
"v_mfma_f32_16x16x32_bf16 v[220:223], a[204:207], a[244:247], v[220:223]\n"
"ds_read_b64_tr_b16 a[182:183], v43 offset:33792\n"
"v_mfma_f32_16x16x32_bf16 v[224:227], a[192:195], a[248:251], v[224:227]\n"
"ds_read_b64_tr_b16 a[184:185], v44 offset:32768\n"
"v_mfma_f32_16x16x32_bf16 v[228:231], a[196:199], a[248:251], v[228:231]\n"
"ds_read_b64_tr_b16 a[186:187], v44 offset:33792\n"
"v_mfma_f32_16x16x32_bf16 v[232:235], a[200:203], a[248:251], v[232:235]\n"
"v_mfma_f32_16x16x32_bf16 v[236:239], a[204:207], a[248:251], v[236:239]\n"
"ds_read_b64_tr_b16 a[188:189], v45 offset:32768\n"
"v_mfma_f32_16x16x32_bf16 v[240:243], a[192:195], a[252:255], v[240:243]\n"
"ds_read_b64_tr_b16 a[190:191], v45 offset:33792\n"
"v_mfma_f32_16x16x32_bf16 v[244:247], a[196:199], a[252:255], v[244:247]\n"
"v_mfma_f32_16x16x32_bf16 v[248:251], a[200:203], a[252:255], v[248:251]\n"
"v_mfma_f32_16x16x32_bf16 v[252:255], a[204:207], a[252:255], v[252:255]\n"
"s_waitcnt lgkmcnt(0)\n"
"v_mfma_f32_16x16x32_bf16 v[128:131], a[128:131], a[160:163], v[128:131]\n"
"ds_read_b64_tr_b16 a[224:225], v42 offset:24576\n"
"v_mfma_f32_16x16x32_bf16 v[132:135], a[132:135], a[160:163], v[132:135]\n"
"ds_read_b64_tr_b16 a[226:227], v42 offset:25600\n"
"v_mfma_f32_16x16x32_bf16 v[136:139], a[136:139], a[160:163], v[136:139]\n"
"ds_read_b128 a[192:195], v41 offset:0\n"
"v_mfma_f32_16x16x32_bf16 v[140:143], a[140:143], a[160:163], v[140:143]\n"
"v_mfma_f32_16x16x32_bf16 v[144:147], a[128:131], a[164:167], v[144:147]\n"
"ds_read_b128 a[196:199], v41 offset:2048\n"
"v_mfma_f32_16x16x32_bf16 v[148:151], a[132:135], a[164:167], v[148:151]\n"
"ds_read_b128 a[200:203], v41 offset:4096\n"
"v_mfma_f32_16x16x32_bf16 v[152:155], a[136:139], a[164:167], v[152:155]\n"
"v_mfma_f32_16x16x32_bf16 v[156:159], a[140:143], a[164:167], v[156:159]\n"
"ds_read_b128 a[204:207], v41 offset:6144\n"
"v_mfma_f32_16x16x32_bf16 v[160:163], a[128:131], a[168:171], v[160:163]\n"
"ds_read_b64_tr_b16 a[228:229], v43 offset:24576\n"
"v_mfma_f32_16x16x32_bf16 v[164:167], a[132:135], a[168:171], v[164:167]\n"
"v_mfma_f32_16x16x32_bf16 v[168:171], a[136:139], a[168:171], v[168:171]\n"
"ds_read_b64_tr_b16 a[230:231], v43 offset:25600\n"
"v_mfma_f32_16x16x32_bf16 v[172:175], a[140:143], a[168:171], v[172:175]\n"
"ds_read_b64_tr_b16 a[232:233], v44 offset:24576\n"
"v_mfma_f32_16x16x32_bf16 v[176:179], a[128:131], a[172:175], v[176:179]\n"
"v_mfma_f32_16x16x32_bf16 v[180:183], a[132:135], a[172:175], v[180:183]\n"
"ds_read_b64_tr_b16 a[234:235], v44 offset:25600\n"
"v_mfma_f32_16x16x32_bf16 v[184:187], a[136:139], a[172:175], v[184:187]\n"
"ds_read_b64_tr_b16 a[236:237], v45 offset:24576\n"
"v_mfma_f32_16x16x32_bf16 v[188:191], a[140:143], a[172:175], v[188:191]\n"
"ds_read_b64_tr_b16 a[238:239], v45 offset:25600\n"
"v_mfma_f32_16x16x32_bf16 v[192:195], a[128:131], a[176:179], v[192:195]\n"
"v_mfma_f32_16x16x32_bf16 v[196:199], a[132:135], a[176:179], v[196:199]\n"
"ds_read_b64_tr_b16 a[240:241], v42 offset:40960\n"
"v_mfma_f32_16x16x32_bf16 v[200:203], a[136:139], a[176:179], v[200:203]\n"
"ds_read_b64_tr_b16 a[242:243], v42 offset:41984\n"
"v_mfma_f32_16x16x32_bf16 v[204:207], a[140:143], a[176:179], v[204:207]\n"
"v_mfma_f32_16x16x32_bf16 v[208:211], a[128:131], a[180:183], v[208:211]\n"
"ds_read_b64_tr_b16 a[244:245], v43 offset:40960\n"
"v_mfma_f32_16x16x32_bf16 v[212:215], a[132:135], a[180:183], v[212:215]\n"
"ds_read_b64_tr_b16 a[246:247], v43 offset:41984\n"
"v_mfma_f32_16x16x32_bf16 v[216:219], a[136:139], a[180:183], v[216:219]\n"
"v_mfma_f32_16x16x32_bf16 v[220:223], a[140:143], a[180:183], v[220:223]\n"
"ds_read_b64_tr_b16 a[248:249], v44 offset:40960\n"
"v_mfma_f32_16x16x32_bf16 v[224:227], a[128:131], a[184:187], v[224:227]\n"
"ds_read_b64_tr_b16 a[250:251], v44 offset:41984\n"
"v_mfma_f32_16x16x32_bf16 v[228:231], a[132:135], a[184:187], v[228:231]\n"
"v_mfma_f32_16x16x32_bf16 v[232:235], a[136:139], a[184:187], v[232:235]\n"
"ds_read_b64_tr_b16 a[252:253], v45 offset:40960\n"
"v_mfma_f32_16x16x32_bf16 v[236:239], a[140:143], a[184:187], v[236:239]\n"
"ds_read_b64_tr_b16 a[254:255], v45 offset:41984\n"
"v_mfma_f32_16x16x32_bf16 v[240:243], a[128:131], a[188:191], v[240:243]\n"
"v_mfma_f32_16x16x32_bf16 v[244:247], a[132:135], a[188:191], v[244:247]\n"
"v_mfma_f32_16x16x32_bf16 v[248:251], a[136:139], a[188:191], v[248:251]\n"
"s_add_u32 s51, s51, 49152\n"
"s_cmp_eq_u32 s51, 147456\n"
"s_cselect_b32 s51, 0, s51\n"
"v_mfma_f32_16x16x32_bf16 v[252:255], a[140:143], a[188:191], v[252:255]\n"
"s_waitcnt lgkmcnt(0)\n"
"v_mfma_f32_16x16x32_bf16 v[128:131], a[192:195], a[224:227], v[128:131]\n"
"v_mfma_f32_16x16x32_bf16 v[132:135], a[196:199], a[224:227], v[132:135]\n"
"v_mfma_f32_16x16x32_bf16 v[136:139], a[200:203], a[224:227], v[136:139]\n"
"v_mfma_f32_16x16x32_bf16 v[140:143], a[204:207], a[224:227], v[140:143]\n"
"v_mfma_f32_16x16x32_bf16 v[144:147], a[192:195], a[228:231], v[144:147]\n"
"v_mfma_f32_16x16x32_bf16 v[148:151], a[196:199], a[228:231], v[148:151]\n"
"v_mfma_f32_16x16x32_bf16 v[152:155], a[200:203], a[228:231], v[152:155]\n"
"v_mfma_f32_16x16x32_bf16 v[156:159], a[204:207], a[228:231], v[156:159]\n"
"v_mfma_f32_16x16x32_bf16 v[160:163], a[192:195], a[232:235], v[160:163]\n"
"v_mfma_f32_16x16x32_bf16 v[164:167], a[196:199], a[232:235], v[164:167]\n"
"v_mfma_f32_16x16x32_bf16 v[168:171], a[200:203], a[232:235], v[168:171]\n"
"v_mfma_f32_16x16x32_bf16 v[172:175], a[204:207], a[232:235], v[172:175]\n"
"v_mfma_f32_16x16x32_bf16 v[176:179], a[192:195], a[236:239], v[176:179]\n"
"v_mfma_f32_16x16x32_bf16 v[180:183], a[196:199], a[236:239], v[180:183]\n"
"v_mfma_f32_16x16x32_bf16 v[184:187], a[200:203], a[236:239], v[184:187]\n"
"v_mfma_f32_16x16x32_bf16 v[188:191], a[204:207], a[236:239], v[188:191]\n"
"v_mfma_f32_16x16x32_bf16 v[192:195], a[192:195], a[240:243], v[192:195]\n"
"v_mfma_f32_16x16x32_bf16 v[196:199], a[196:199], a[240:243], v[196:199]\n"
"v_mfma_f32_16x16x32_bf16 v[200:203], a[200:203], a[240:243], v[200:203]\n"
"v_mfma_f32_16x16x32_bf16 v[204:207], a[204:207], a[240:243], v[204:207]\n"
"v_mfma_f32_16x16x32_bf16 v[208:211], a[192:195], a[244:247], v[208:211]\n"
"v_mfma_f32_16x16x32_bf16 v[212:215], a[196:199], a[244:247], v[212:215]\n"
"v_mfma_f32_16x16x32_bf16 v[216:219], a[200:203], a[244:247], v[216:219]\n"
"v_mfma_f32_16x16x32_bf16 v[220:223], a[204:207], a[244:247], v[220:223]\n"
"v_mfma_f32_16x16x32_bf16 v[224:227], a[192:195], a[248:251], v[224:227]\n"
"v_mfma_f32_16x16x32_bf16 v[228:231], a[196:199], a[248:251], v[228:231]\n"
"v_mfma_f32_16x16x32_bf16 v[232:235], a[200:203], a[248:251], v[232:235]\n"
"v_mfma_f32_16x16x32_bf16 v[236:239], a[204:207], a[248:251], v[236:239]\n"
"v_mfma_f32_16x16x32_bf16 v[240:243], a[192:195], a[252:255], v[240:243]\n"
"v_mfma_f32_16x16x32_bf16 v[244:247], a[196:199], a[252:255], v[244:247]\n"
"v_mfma_f32_16x16x32_bf16 v[248:251], a[200:203], a[252:255], v[248:251]\n"
"v_mfma_f32_16x16x32_bf16 v[252:255], a[204:207], a[252:255], v[252:255]\n"
"s_barrier\n"
"s_add_u32 s54, %[o0], 0\n"
"v_cvt_pk_bf16_f32 v60, v128, v129\n"
"v_cvt_pk_bf16_f32 v61, v130, v131\n"
"v_cvt_pk_bf16_f32 v62, v132, v133\n"
"v_cvt_pk_bf16_f32 v63, v134, v135\n"
"buffer_store_dwordx4 v[60:63], %[vst], s[56:59], s54 offen offset:0 sc1\n"
"v_cvt_pk_bf16_f32 v64, v144, v145\n"
"v_cvt_pk_bf16_f32 v65, v146, v147\n"
"v_cvt_pk_bf16_f32 v66, v148, v149\n"
"v_cvt_pk_bf16_f32 v67, v150, v151\n"
"buffer_store_dwordx4 v[64:67], %[vst], s[56:59], s54 offen offset:1024 sc1\n"
"v_cvt_pk_bf16_f32 v68, v160, v161\n"
"v_cvt_pk_bf16_f32 v69, v162, v163\n"
"v_cvt_pk_bf16_f32 v70, v164, v165\n"
"v_cvt_pk_bf16_f32 v71, v166, v167\n"
"buffer_store_dwordx4 v[68:71], %[vst], s[56:59], s54 offen offset:2048 sc1\n"
"v_cvt_pk_bf16_f32 v72, v176, v177\n"
"v_cvt_pk_bf16_f32 v73, v178, v179\n"
"v_cvt_pk_bf16_f32 v74, v180, v181\n"
"v_cvt_pk_bf16_f32 v75, v182, v183\n"
"buffer_store_dwordx4 v[72:75], %[vst], s[56:59], s54 offen offset:3072 sc1\n"
"s_add_u32 s54, %[o0], 0\n"
"s_add_u32 s54, s54, %[o1]\n"
"v_cvt_pk_bf16_f32 v60, v192, v193\n"
"v_cvt_pk_bf16_f32 v61, v194, v195\n"
"v_cvt_pk_bf16_f32 v62, v196, v197\n"
"v_cvt_pk_bf16_f32 v63, v198, v199\n"
"buffer_store_dwordx4 v[60:63], %[vst], s[56:59], s54 offen offset:0 sc1\n"
"v_cvt_pk_bf16_f32 v64, v208, v209\n"
"v_cvt_pk_bf16_f32 v65, v210, v211\n"
"v_cvt_pk_bf16_f32 v66, v212, v213\n"
"v_cvt_pk_bf16_f32 v67, v214, v215\n"
"buffer_store_dwordx4 v[64:67], %[vst], s[56:59], s54 offen offset:1024 sc1\n"
"v_cvt_pk_bf16_f32 v68, v224, v225\n"
"v_cvt_pk_bf16_f32 v69, v226, v227\n"
"v_cvt_pk_bf16_f32 v70, v228, v229\n"
"v_cvt_pk_bf16_f32 v71, v230, v231\n"
"buffer_store_dwordx4 v[68:71], %[vst], s[56:59], s54 offen offset:2048 sc1\n"
"v_cvt_pk_bf16_f32 v72, v240, v241\n"
"v_cvt_pk_bf16_f32 v73, v242, v243\n"
"v_cvt_pk_bf16_f32 v74, v244, v245\n"
"v_cvt_pk_bf16_f32 v75, v246, v247\n"
"buffer_store_dwordx4 v[72:75], %[vst], s[56:59], s54 offen offset:3072 sc1\n"
"s_add_u32 s54, %[o0], 262144\n"
"v_cvt_pk_bf16_f32 v60, v136, v137\n"
"v_cvt_pk_bf16_f32 v61, v138, v139\n"
"v_cvt_pk_bf16_f32 v62, v140, v141\n"
"v_cvt_pk_bf16_f32 v63, v142, v143\n"
"buffer_store_dwordx4 v[60:63], %[vst], s[56:59], s54 offen offset:0 sc1\n"
"v_cvt_pk_bf16_f32 v64, v152, v153\n"
"v_cvt_pk_bf16_f32 v65, v154, v155\n"
"v_cvt_pk_bf16_f32 v66, v156, v157\n"
"v_cvt_pk_bf16_f32 v67, v158, v159\n"
"buffer_store_dwordx4 v[64:67], %[vst], s[56:59], s54 offen offset:1024 sc1\n"
"v_cvt_pk_bf16_f32 v68, v168, v169\n"
"v_cvt_pk_bf16_f32 v69, v170, v171\n"
"v_cvt_pk_bf16_f32 v70, v172, v173\n"
"v_cvt_pk_bf16_f32 v71, v174, v175\n"
"buffer_store_dwordx4 v[68:71], %[vst], s[56:59], s54 offen offset:2048 sc1\n"
"v_cvt_pk_bf16_f32 v72, v184, v185\n"
"v_cvt_pk_bf16_f32 v73, v186, v187\n"
"v_cvt_pk_bf16_f32 v74, v188, v189\n"
"v_cvt_pk_bf16_f32 v75, v190, v191\n"
"buffer_store_dwordx4 v[72:75], %[vst], s[56:59], s54 offen offset:3072 sc1\n"
"s_add_u32 s54, %[o0], 262144\n"
"s_add_u32 s54, s54, %[o1]\n"
"v_cvt_pk_bf16_f32 v60, v200, v201\n"
"v_cvt_pk_bf16_f32 v61, v202, v203\n"
"v_cvt_pk_bf16_f32 v62, v204, v205\n"
"v_cvt_pk_bf16_f32 v63, v206, v207\n"
"buffer_store_dwordx4 v[60:63], %[vst], s[56:59], s54 offen offset:0 sc1\n"
"v_cvt_pk_bf16_f32 v64, v216, v217\n"
"v_cvt_pk_bf16_f32 v65, v218, v219\n"
"v_cvt_pk_bf16_f32 v66, v220, v221\n"
"v_cvt_pk_bf16_f32 v67, v222, v223\n"
"buffer_store_dwordx4 v[64:67], %[vst], s[56:59], s54 offen offset:1024 sc1\n"
"v_cvt_pk_bf16_f32 v68, v232, v233\n"
"v_cvt_pk_bf16_f32 v69, v234, v235\n"
"v_cvt_pk_bf16_f32 v70, v236, v237\n"
"v_cvt_pk_bf16_f32 v71, v238, v239\n"
"buffer_store_dwordx4 v[68:71], %[vst], s[56:59], s54 offen offset:2048 sc1\n"
"v_cvt_pk_bf16_f32 v72, v248, v249\n"
"v_cvt_pk_bf16_f32 v73, v250, v251\n"
"v_cvt_pk_bf16_f32 v74, v252, v253\n"
"v_cvt_pk_bf16_f32 v75, v254, v255\n"
"buffer_store_dwordx4 v[72:75], %[vst], s[56:59], s54 offen offset:3072 sc1\n"
"s_branch END_%=\n"
"SW_%=:\n"
"s_mov_b32 s50, 0\n"
"s_mov_b32 s51, 0\n"
"s_add_u32 m0, s50, s60\n"
"s_nop 0\n"
"buffer_load_dwordx4 v24, s[36:39], s44 offen offset:0 sc1 lds\n"
"buffer_load_dwordx4 v25, s[36:39], s44 offen offset:1024 sc1 lds\n"
"buffer_load_dwordx4 v26, s[36:39], s44 offen offset:2048 sc1 lds\n"
"buffer_load_dwordx4 v27, s[36:39], s44 offen offset:3072 sc1 lds\n"
"s_add_u32 m0, s50, s62\n"
"s_nop 0\n"
"buffer_load_dwordx4 v32, s[40:43], s45 offen offset:0 lds\n"
"buffer_load_dwordx4 v33, s[40:43], s45 offen offset:1024 lds\n"
"buffer_load_dwordx4 v34, s[40:43], s45 offen offset:2048 lds\n"
"buffer_load_dwordx4 v35, s[40:43], s45 offen offset:3072 lds\n"
"s_add_u32 m0, s50, s63\n"
"s_nop 0\n"
"buffer_load_dwordx4 v36, s[40:43], s45 offen offset:0 lds\n"
"buffer_load_dwordx4 v37, s[40:43], s45 offen offset:1024 lds\n"
"buffer_load_dwordx4 v38, s[40:43], s45 offen offset:2048 lds\n"
"buffer_load_dwordx4 v39, s[40:43], s45 offen offset:3072 lds\n"
"s_add_u32 s44, s44, 2048\n"
"s_add_u32 s45, s45, s48\n"
"s_add_u32 s50, s50, 49152\n"
"s_cmp_eq_u32 s50, 147456\n"
"s_cselect_b32 s50, 0, s50\n"
"v_mov_b64 v[128:129], 0\n"
"v_mov_b64 v[130:131], 0\n"
"v_mov_b64 v[136:137], 0\n"
"v_mov_b64 v[138:139], 0\n"
"v_mov_b64 v[144:145], 0\n"
"v_mov_b64 v[146:147], 0\n"
"v_mov_b64 v[152:153], 0\n"
"v_mov_b64 v[154:155], 0\n"
"v_mov_b64 v[160:161], 0\n"
"v_mov_b64 v[162:163], 0\n"
"v_mov_b64 v[168:169], 0\n"
"v_mov_b64 v[170:171], 0\n"
"v_mov_b64 v[176:177], 0\n"
"v_mov_b64 v[178:179], 0\n"
"v_mov_b64 v[184:185], 0\n"
"v_mov_b64 v[186:187], 0\n"
"v_mov_b64 v[192:193], 0\n"
"v_mov_b64 v[194:195], 0\n"
"v_mov_b64 v[200:201], 0\n"
"v_mov_b64 v[202:203], 0\n"
"v_mov_b64 v[208:209], 0\n"
"v_mov_b64 v[210:211], 0\n"
"v_mov_b64 v[216:217], 0\n"
"v_mov_b64 v[218:219], 0\n"
"v_mov_b64 v[224:225], 0\n"
"v_mov_b64 v[226:227], 0\n"
"v_mov_b64 v[232:233], 0\n"
"v_mov_b64 v[234:235], 0\n"
"v_mov_b64 v[240:241], 0\n"
"v_mov_b64 v[242:243], 0\n"
"v_mov_b64 v[248:249], 0\n"
"v_mov_b64 v[250:251], 0\n"
"v_mov_b64 v[132:133], 0\n"
"v_mov_b64 v[134:135], 0\n"
"v_mov_b64 v[140:141], 0\n"
"v_mov_b64 v[142:143], 0\n"
"v_mov_b64 v[148:149], 0\n"
"v_mov_b64 v[150:151], 0\n"
"v_mov_b64 v[156:157], 0\n"
"v_mov_b64 v[158:159], 0\n"
"v_mov_b64 v[164:165], 0\n"
"v_mov_b64 v[166:167], 0\n"
"v_mov_b64 v[172:173], 0\n"
"v_mov_b64 v[174:175], 0\n"
"v_mov_b64 v[180:181], 0\n"
"v_mov_b64 v[182:183], 0\n"
"v_mov_b64 v[188:189], 0\n"
"v_mov_b64 v[190:191], 0\n"
"v_mov_b64 v[196:197], 0\n"
"v_mov_b64 v[198:199], 0\n"
"v_mov_b64 v[204:205], 0\n"
"v_mov_b64 v[206:207], 0\n"
"v_mov_b64 v[212:213], 0\n"
"v_mov_b64 v[214:215], 0\n"
"v_mov_b64 v[220:221], 0\n"
"v_mov_b64 v[222:223], 0\n"
"v_mov_b64 v[228:229], 0\n"
"v_mov_b64 v[230:231], 0\n"
"v_mov_b64 v[236:237], 0\n"
"v_mov_b64 v[238:239], 0\n"
"v_mov_b64 v[244:245], 0\n"
"v_mov_b64 v[246:247], 0\n"
"v_mov_b64 v[252:253], 0\n"
"v_mov_b64 v[254:255], 0\n"
"v_add_u32 v40, s51, v46\n"
"v_add_u32 v41, s51, v47\n"
"v_add_u32 v42, s51, v48\n"
"v_add_u32 v43, s51, v49\n"
"v_add_u32 v44, s51, v50\n"
"v_add_u32 v45, s51, v51\n"
"s_add_u32 m0, s50, s60\n"
"s_nop 0\n"
"buffer_load_dwordx4 v24, s[36:39], s44 offen offset:0 sc1 lds\n"
"buffer_load_dwordx4 v25, s[36:39], s44 offen offset:1024 sc1 lds\n"
"buffer_load_dwordx4 v26, s[36:39], s44 offen offset:2048 sc1 lds\n"
"buffer_load_dwordx4 v27, s[36:39], s44 offen offset:3072 sc1 lds\n"
"s_add_u32 m0, s50, s62\n"
"s_nop 0\n"
"buffer_load_dwordx4 v32, s[40:43], s45 offen offset:0 lds\n"
"buffer_load_dwordx4 v33, s[40:43], s45 offen offset:1024 lds\n"
"buffer_load_dwordx4 v34, s[40:43], s45 offen offset:2048 lds\n"
"buffer_load_dwordx4 v35, s[40:43], s45 offen offset:3072 lds\n"
"s_add_u32 m0, s50, s63\n"
"s_nop 0\n"
"buffer_load_dwordx4 v36, s[40:43], s45 offen offset:0 lds\n"
"buffer_load_dwordx4 v37, s[40:43], s45 offen offset:1024 lds\n"
"buffer_load_dwordx4 v38, s[40:43], s45 offen offset:2048 lds\n"
"buffer_load_dwordx4 v39, s[40:43], s45 offen offset:3072 lds\n"
"s_add_u32 s44, s44, 2048\n"
"s_add_u32 s45, s45, s48\n"
"s_add_u32 s50, s50, 49152\n"
"s_cmp_eq_u32 s50, 147456\n"
"s_cselect_b32 s50, 0, s50\n"
"s_add_u32 m0, s50, s60\n"
"s_nop 0\n"
"buffer_load_dwordx4 v24, s[36:39], s44 offen offset:0 sc1 lds\n"
"buffer_load_dwordx4 v25, s[36:39], s44 offen offset:1024 sc1 lds\n"
"buffer_load_dwordx4 v26, s[36:39], s44 offen offset:2048 sc1 lds\n"
"buffer_load_dwordx4 v27, s[36:39], s44 offen offset:3072 sc1 lds\n"
"s_add_u32 s44, s44, 2048\n"
"s_waitcnt vmcnt(16)\n"
"s_barrier\n"
"ds_read_b64_tr_b16 a[160:161], v42 offset:16384\n"
"ds_read_b64_tr_b16 a[162:163], v42 offset:17408\n"
"ds_read_b128 a[128:131], v40 offset:0\n"
"ds_read_b128 a[132:135], v40 offset:2048\n"
"ds_read_b128 a[136:139], v40 offset:4096\n"
"ds_read_b128 a[140:143], v40 offset:6144\n"
"ds_read_b64_tr_b16 a[164:165], v43 offset:16384\n"
"ds_read_b64_tr_b16 a[166:167], v43 offset:17408\n"
"ds_read_b64_tr_b16 a[168:169], v44 offset:16384\n"
"ds_read_b64_tr_b16 a[170:171], v44 offset:17408\n"
"ds_read_b64_tr_b16 a[172:173], v45 offset:16384\n"
"ds_read_b64_tr_b16 a[174:175], v45 offset:17408\n"
"ds_read_b64_tr_b16 a[176:177], v42 offset:32768\n"
"ds_read_b64_tr_b16 a[178:179], v42 offset:33792\n"
"ds_read_b64_tr_b16 a[180:181], v43 offset:32768\n"
"ds_read_b64_tr_b16 a[182:183], v43 offset:33792\n"
"ds_read_b64_tr_b16 a[184:185], v44 offset:32768\n"
"ds_read_b64_tr_b16 a[186:187], v44 offset:33792\n"
"ds_read_b64_tr_b16 a[188:189], v45 offset:32768\n"
"ds_read_b64_tr_b16 a[190:191], v45 offset:33792\n"
"s_sub_u32 s46, s47, 3\n"
"s_waitcnt lgkmcnt(0)\n"
"KL_s_%=:\n"
"v_mfma_f32_16x16x32_bf16 v[128:131], a[160:163], a[128:131], v[128:131]\n"
"ds_read_b64_tr_b16 a[224:225], v42 offset:24576\n"
"s_add_u32 m0, s50, s62\n"
"v_mfma_f32_16x16x32_bf16 v[132:135], a[160:163], a[132:135], v[132:135]\n"
"ds_read_b64_tr_b16 a[226:227], v42 offset:25600\n"
"buffer_load_dwordx4 v32, s[40:43], s45 offen offset:0 lds\n"
"v_mfma_f32_16x16x32_bf16 v[136:139], a[160:163], a[136:139], v[136:139]\n"
"ds_read_b128 a[192:195], v41 offset:0\n"
"v_mfma_f32_16x16x32_bf16 v[140:143], a[160:163], a[140:143], v[140:143]\n"
"v_mfma_f32_16x16x32_bf16 v[144:147], a[164:167], a[128:131], v[144:147]\n"
"ds_read_b128 a[196:199], v41 offset:2048\n"
"v_mfma_f32_16x16x32_bf16 v[148:151], a[164:167], a[132:135], v[148:151]\n"
"ds_read_b128 a[200:203], v41 offset:4096\n"
"buffer_load_dwordx4 v33, s[40:43], s45 offen offset:1024 lds\n"
"v_mfma_f32_16x16x32_bf16 v[152:155], a[164:167], a[136:139], v[152:155]\n"
"v_mfma_f32_16x16x32_bf16 v[156:159], a[164:167], a[140:143], v[156:159]\n"
"ds_read_b128 a[204:207], v41 offset:6144\n"
"v_mfma_f32_16x16x32_bf16 v[160:163], a[168:171], a[128:131], v[160:163]\n"
"ds_read_b64_tr_b16 a[228:229], v43 offset:24576\n"
"v_mfma_f32_16x16x32_bf16 v[164:167], a[168:171], a[132:135], v[164:167]\n"
"buffer_load_dwordx4 v34, s[40:43], s45 offen offset:2048 lds\n"
"v_mfma_f32_16x16x32_bf16 v[168:171], a[168:171], a[136:139], v[168:171]\n"
"ds_read_b64_tr_b16 a[230:231], v43 offset:25600\n"
"v_mfma_f32_16x16x32_bf16 v[172:175], a[168:171], a[140:143], v[172:175]\n"
"ds_read_b64_tr_b16 a[232:233], v44 offset:24576\n"
"v_mfma_f32_16x16x32_bf16 v[176:179], a[172:175], a[128:131], v[176:179]\n"
"v_mfma_f32_16x16x32_bf16 v[180:183], a[172:175], a[132:135], v[180:183]\n"
"ds_read_b64_tr_b16 a[234:235], v44 offset:25600\n"
"buffer_load_dwordx4 v35, s[40:43], s45 offen offset:3072 lds\n"
"v_mfma_f32_16x16x32_bf16 v[184:187], a[172:175], a[136:139], v[184:187]\n"
"ds_read_b64_tr_b16 a[236:237], v45 offset:24576\n"
"v_mfma_f32_16x16x32_bf16 v[188:191], a[172:175], a[140:143], v[188:191]\n"
"ds_read_b64_tr_b16 a[238:239], v45 offset:25600\n"
"v_mfma_f32_16x16x32_bf16 v[192:195], a[176:179], a[128:131], v[192:195]\n"
"s_add_u32 m0, s50, s63\n"
"v_mfma_f32_16x16x32_bf16 v[196:199], a[176:179], a[132:135], v[196:199]\n"
"ds_read_b64_tr_b16 a[240:241], v42 offset:40960\n"
"buffer_load_dwordx4 v36, s[40:43], s45 offen offset:0 lds\n"
"v_mfma_f32_16x16x32_bf16 v[200:203], a[176:179], a[136:139], v[200:203]\n"
"ds_read_b64_tr_b16 a[242:243], v42 offset:41984\n"
"v_mfma_f32_16x16x32_bf16 v[204:207], a[176:179], a[140:143], v[204:207]\n"
"v_mfma_f32_16x16x32_bf16 v[208:211], a[180:183], a[128:131], v[208:211]\n"
"ds_read_b64_tr_b16 a[244:245], v43 offset:40960\n"
"v_mfma_f32_16x16x32_bf16 v[212:215], a[180:183], a[132:135], v[212:215]\n"
"ds_read_b64_tr_b16 a[246:247], v43 offset:41984\n"
"buffer_load_dwordx4 v37, s[40:43], s45 offen offset:1024 lds\n"
"v_mfma_f32_16x16x32_bf16 v[216:219], a[180:183], a[136:139], v[216:219]\n"
"v_mfma_f32_16x16x32_bf16 v[220:223], a[180:183], a[140:143], v[220:223]\n"
"ds_read_b64_tr_b16 a[248:249], v44 offset:40960\n"
"v_mfma_f32_16x16x32_bf16 v[224:227], a[184:187], a[128:131], v[224:227]\n"
"ds_read_b64_tr_b16 a[250:251], v44 offset:41984\n"
"v_mfma_f32_16x16x32_bf16 v[228:231], a[184:187], a[132:135], v[228:231]\n"
"buffer_load_dwordx4 v38, s[40:43], s45 offen offset:2048 lds\n"
"v_mfma_f32_16x16x32_bf16 v[232:235], a[184:187], a[136:139], v[232:235]\n"
"ds_read_b64_tr_b16 a[252:253], v45 offset:40960\n"
"v_mfma_f32_16x16x32_bf16 v[236:239], a[184:187], a[140:143], v[236:239]\n"
"ds_read_b64_tr_b16 a[254:255], v45 offset:41984\n"
"v_mfma_f32_16x16x32_bf16 v[240:243], a[188:191], a[128:131], v[240:243]\n"
"v_mfma_f32_16x16x32_bf16 v[244:247], a[188:191], a[132:135], v[244:247]\n"
"buffer_load_dwordx4 v39, s[40:43], s45 offen offset:3072 lds\n"
"v_mfma_f32_16x16x32_bf16 v[248:251], a[188:191], a[136:139], v[248:251]\n"
"s_add_u32 s45, s45, s48\n"
"s_add_u32 s50, s50, 49152\n"
"s_cmp_eq_u32 s50, 147456\n"
"s_cselect_b32 s50, 0, s50\n"
"s_add_u32 s51, s51, 49152\n"
"s_cmp_eq_u32 s51, 147456\n"
"s_cselect_b32 s51, 0, s51\n"
"v_mfma_f32_16x16x32_bf16 v[252:255], a[188:191], a[140:143], v[252:255]\n"
"s_waitcnt lgkmcnt(0) vmcnt(12)\n"
"s_barrier\n"
"v_mfma_f32_16x16x32_bf16 v[128:131], a[224:227], a[192:195], v[128:131]\n"
"v_add_u32 v40, s51, v46\n"
"v_add_u32 v41, s51, v47\n"
"s_add_u32 m0, s50, s60\n"
"v_mfma_f32_16x16x32_bf16 v[132:135], a[224:227], a[196:199], v[132:135]\n"
"v_add_u32 v42, s51, v48\n"
"v_add_u32 v43, s51, v49\n"
"buffer_load_dwordx4 v24, s[36:39], s44 offen offset:0 sc1 lds\n"
"v_mfma_f32_16x16x32_bf16 v[136:139], a[224:227], a[200:203], v[136:139]\n"
"v_add_u32 v44, s51, v50\n"
"v_add_u32 v45, s51, v51\n"
"v_mfma_f32_16x16x32_bf16 v[140:143], a[224:227], a[204:207], v[140:143]\n"
"ds_read_b64_tr_b16 a[160:161], v42 offset:16384\n"
"v_mfma_f32_16x16x32_bf16 v[144:147], a[228:231], a[192:195], v[144:147]\n"
"ds_read_b64_tr_b16 a[162:163], v42 offset:17408\n"
"v_mfma_f32_16x16x32_bf16 v[148:151], a[228:231], a[196:199], v[148:151]\n"
"ds_read_b128 a[128:131], v40 offset:0\n"
"v_mfma_f32_16x16x32_bf16 v[152:155], a[228:231], a[200:203], v[152:155]\n"
"v_mfma_f32_16x16x32_bf16 v[156:159], a[228:231], a[204:207], v[156:159]\n"
"ds_read_b128 a[132:135], v40 offset:2048\n"
"v_mfma_f32_16x16x32_bf16 v[160:163], a[232:235], a[192:195], v[160:163]\n"
"ds_read_b128 a[136:139], v40 offset:4096\n"
"v_mfma_f32_16x16x32_bf16 v[164:167], a[232:235], a[196:199], v[164:167]\n"
"ds_read_b128 a[140:143], v40 offset:6144\n"
"buffer_load_dwordx4 v25, s[36:39], s44 offen offset:1024 sc1 lds\n"
"v_mfma_f32_16x16x32_bf16 v[168:171], a[232:235], a[200:203], v[168:171]\n"
"v_mfma_f32_16x16x32_bf16 v[172:175], a[232:235], a[204:207], v[172:175]\n"
"ds_read_b64_tr_b16 a[164:165], v43 offset:16384\n"
"v_mfma_f32_16x16x32_bf16 v[176:179], a[236:239], a[192:195], v[176:179]\n"
"ds_read_b64_tr_b16 a[166:167], v43 offset:17408\n"
"v_mfma_f32_16x16x32_bf16 v[180:183], a[236:239], a[196:199], v[180:183]\n"
"ds_read_b64_tr_b16 a[168:169], v44 offset:16384\n"
"v_mfma_f32_16x16x32_bf16 v[184:187], a[236:239], a[200:203], v[184:187]\n"
"v_mfma_f32_16x16x32_bf16 v[188:191], a[236:239], a[204:207], v[188:191]\n"
"ds_read_b64_tr_b16 a[170:171], v44 offset:17408\n"
"v_mfma_f32_16x16x32_bf16 v[192:195], a[240:243], a[192:195], v[192:195]\n"
"ds_read_b64_tr_b16 a[172:173], v45 offset:16384\n"
"v_mfma_f32_16x16x32_bf16 v[196:199], a[240:243], a[196:199], v[196:199]\n"
"ds_read_b64_tr_b16 a[174:175], v45 offset:17408\n"
"buffer_load_dwordx4 v26, s[36:39], s44 offen offset:2048 sc1 lds\n"
"v_mfma_f32_16x16x32_bf16 v[200:203], a[240:243], a[200:203], v[200:203]\n"
"v_mfma_f32_16x16x32_bf16 v[204:207], a[240:243], a[204:207], v[204:207]\n"
"ds_read_b64_tr_b16 a[176:177], v42 offset:32768\n"
"v_mfma_f32_16x16x32_bf16 v[208:211], a[244:247], a[192:195], v[208:211]\n"
"ds_read_b64_tr_b16 a[178:179], v42 offset:33792\n"
"v_mfma_f32_16x16x32_bf16 v[212:215], a[244:247], a[196:199], v[212:215]\n"
"ds_read_b64_tr_b16 a[180:181], v43 offset:32768\n"
"v_mfma_f32_16x16x32_bf16 v[216:219], a[244:247], a[200:203], v[216:219]\n"
"v_mfma_f32_16x16x32_bf16 v[220:223], a[244:247], a[204:207], v[220:223]\n"
"ds_read_b64_tr_b16 a[182:183], v43 offset:33792\n"
"v_mfma_f32_16x16x32_bf16 v[224:227], a[248:251], a[192:195], v[224:227]\n"
"ds_read_b64_tr_b16 a[184:185], v44 offset:32768\n"
"v_mfma_f32_16x16x32_bf16 v[228:231], a[248:251], a[196:199], v[228:231]\n"
"ds_read_b64_tr_b16 a[186:187], v44 offset:33792\n"
"buffer_load_dwordx4 v27, s[36:39], s44 offen offset:3072 sc1 lds\n"
"v_mfma_f32_16x16x32_bf16 v[232:235], a[248:251], a[200:203], v[232:235]\n"
"v_mfma_f32_16x16x32_bf16 v[236:239], a[248:251], a[204:207], v[236:239]\n"
"ds_read_b64_tr_b16 a[188:189], v45 offset:32768\n"
"v_mfma_f32_16x16x32_bf16 v[240:243], a[252:255], a[192:195], v[240:243]\n"
"ds_read_b64_tr_b16 a[190:191], v45 offset:33792\n"
"v_mfma_f32_16x16x32_bf16 v[244:247], a[252:255], a[196:199], v[244:247]\n"
"v_mfma_f32_16x16x32_bf16 v[248:251], a[252:255], a[200:203], v[248:251]\n"
"s_add_u32 s44, s44, 2048\n"
"v_mfma_f32_16x16x32_bf16 v[252:255], a[252:255], a[204:207], v[252:255]\n"
"s_sub_u32 s46, s46, 1\n"
"s_cmp_lg_u32 s46, 0\n"
"s_waitcnt lgkmcnt(0)\n"
"s_cbranch_scc1 KL_s_%=\n"
"v_mfma_f32_16x16x32_bf16 v[128:131], a[160:163], a[128:131], v[128:131]\n"
"ds_read_b64_tr_b16 a[224:225], v42 offset:24576\n"
"s_add_u32 m0, s50, s62\n"
"v_mfma_f32_16x16x32_bf16 v[132:135], a[160:163], a[132:135], v[132:135]\n"
"ds_read_b64_tr_b16 a[226:227], v42 offset:25600\n"
"buffer_load_dwordx4 v32, s[40:43], s45 offen offset:0 lds\n"
"v_mfma_f32_16x16x32_bf16 v[136:139], a[160:163], a[136:139], v[136:139]\n"
"ds_read_b128 a[192:195], v41 offset:0\n"
"v_mfma_f32_16x16x32_bf16 v[140:143], a[160:163], a[140:143], v[140:143]\n"
"v_mfma_f32_16x16x32_bf16 v[144:147], a[164:167], a[128:131], v[144:147]\n"
"ds_read_b128 a[196:199], v41 offset:2048\n"
"v_mfma_f32_16x16x32_bf16 v[148:151], a[164:167], a[132:135], v[148:151]\n"
"ds_read_b128 a[200:203], v41 offset:4096\n"
"buffer_load_dwordx4 v33, s[40:43], s45 offen offset:1024 lds\n"
"v_mfma_f32_16x16x32_bf16 v[152:155], a[164:167], a[136:139], v[152:155]\n"
"v_mfma_f32_16x16x32_bf16 v[156:159], a[164:167], a[140:143], v[156:159]\n"
"ds_read_b128 a[204:207], v41 offset:6144\n"
"v_mfma_f32_16x16x32_bf16 v[160:163], a[168:171], a[128:131], v[160:163]\n"
"ds_read_b64_tr_b16 a[228:229], v43 offset:24576\n"
"v_mfma_f32_16x16x32_bf16 v[164:167], a[168:171], a[132:135], v[164:167]\n"
"buffer_load_dwordx4 v34, s[40:43], s45 offen offset:2048 lds\n"
"v_mfma_f32_16x16x32_bf16 v[168:171], a[168:171], a[136:139], v[168:171]\n"
"ds_read_b64_tr_b16 a[230:231], v43 offset:25600\n"
"v_mfma_f32_16x16x32_bf16 v[172:175], a[168:171], a[140:143], v[172:175]\n"
"ds_read_b64_tr_b16 a[232:233], v44 offset:24576\n"
"v_mfma_f32_16x16x32_bf16 v[176:179], a[172:175], a[128:131], v[176:179]\n"
"v_mfma_f32_16x16x32_bf16 v[180:183], a[172:175], a[132:135], v[180:183]\n"
"ds_read_b64_tr_b16 a[234:235], v44 offset:25600\n"
"buffer_load_dwordx4 v35, s[40:43], s45 offen offset:3072 lds\n"
"v_mfma_f32_16x16x32_bf16 v[184:187], a[172:175], a[136:139], v[184:187]\n"
"ds_read_b64_tr_b16 a[236:237], v45 offset:24576\n"
"v_mfma_f32_16x16x32_bf16 v[188:191], a[172:175], a[140:143], v[188:191]\n"
"ds_read_b64_tr_b16 a[238:239], v45 offset:25600\n"
"v_mfma_f32_16x16x32_bf16 v[192:195], a[176:179], a[128:131], v[192:195]\n"
"s_add_u32 m0, s50, s63\n"
"v_mfma_f32_16x16x32_bf16 v[196:199], a[176:179], a[132:135], v[196:199]\n"
"ds_read_b64_tr_b16 a[240:241], v42 offset:40960\n"
"buffer_load_dwordx4 v36, s[40:43], s45 offen offset:0 lds\n"
"v_mfma_f32_16x16x32_bf16 v[200:203], a[176:179], a[136:139], v[200:203]\n"
"ds_read_b64_tr_b16 a[242:243], v42 offset:41984\n"
"v_mfma_f32_16x16x32_bf16 v[204:207], a[176:179], a[140:143], v[204:207]\n"
"v_mfma_f32_16x16x32_bf16 v[208:211], a[180:183], a[128:131], v[208:211]\n"
"ds_read_b64_tr_b16 a[244:245], v43 offset:40960\n"
"v_mfma_f32_16x16x32_bf16 v[212:215], a[180:183], a[132:135], v[212:215]\n"
"ds_read_b64_tr_b16 a[246:247], v43 offset:41984\n"
"buffer_load_dwordx4 v37, s[40:43], s45 offen offset:1024 lds\n"
"v_mfma_f32_16x16x32_bf16 v[216:219], a[180:183], a[136:139], v[216:219]\n"
"v_mfma_f32_16x16x32_bf16 v[220:223], a[180:183], a[140:143], v[220:223]\n"
"ds_read_b64_tr_b16 a[248:249], v44 offset:40960\n"
"v_mfma_f32_16x16x32_bf16 v[224:227], a[184:187], a[128:131], v[224:227]\n"
"ds_read_b64_tr_b16 a[250:251], v44 offset:41984\n"
"v_mfma_f32_16x16x32_bf16 v[228:231], a[184:187], a[132:135], v[228:231]\n"
"buffer_load_dwordx4 v38, s[40:43], s45 offen offset:2048 lds\n"
"v_mfma_f32_16x16x32_bf16 v[232:235], a[184:187], a[136:139], v[232:235]\n"
"ds_read_b64_tr_b16 a[252:253], v45 offset:40960\n"
"v_mfma_f32_16x16x32_bf16 v[236:239], a[184:187], a[140:143], v[236:239]\n"
"ds_read_b64_tr_b16 a[254:255], v45 offset:41984\n"
"v_mfma_f32_16x16x32_bf16 v[240:243], a[188:191], a[128:131], v[240:243]\n"
"v_mfma_f32_16x16x32_bf16 v[244:247], a[188:191], a[132:135], v[244:247]\n"
"buffer_load_dwordx4 v39, s[40:43], s45 offen offset:3072 lds\n"
"v_mfma_f32_16x16x32_bf16 v[248:251], a[188:191], a[136:139], v[248:251]\n"
"s_add_u32 s45, s45, s48\n"
"s_add_u32 s50, s50, 49152\n"
"s_cmp_eq_u32 s50, 147456\n"
"s_cselect_b32 s50, 0, s50\n"
"s_add_u32 s51, s51, 49152\n"
"s_cmp_eq_u32 s51, 147456\n"
"s_cselect_b32 s51, 0, s51\n"
"v_mfma_f32_16x16x32_bf16 v[252:255], a[188:191], a[140:143], v[252:255]\n"
"s_waitcnt lgkmcnt(0) vmcnt(12)\n"
"s_barrier\n"
"v_mfma_f32_16x16x32_bf16 v[128:131], a[224:227], a[192:195], v[128:131]\n"
"v_add_u32 v40, s51, v46\n"
"v_add_u32 v41, s51, v47\n"
"v_mfma_f32_16x16x32_bf16 v[132:135], a[224:227], a[196:199], v[132:135]\n"
"v_add_u32 v42, s51, v48\n"
"v_add_u32 v43, s51, v49\n"
"v_mfma_f32_16x16x32_bf16 v[136:139], a[224:227], a[200:203], v[136:139]\n"
"v_add_u32 v44, s51, v50\n"
"v_add_u32 v45, s51, v51\n"
"v_mfma_f32_16x16x32_bf16 v[140:143], a[224:227], a[204:207], v[140:143]\n"
"ds_read_b64_tr_b16 a[160:161], v42 offset:16384\n"
"v_mfma_f32_16x16x32_bf16 v[144:147], a[228:231], a[192:195], v[144:147]\n"
"ds_read_b64_tr_b16 a[162:163], v42 offset:17408\n"
"v_mfma_f32_16x16x32_bf16 v[148:151], a[228:231], a[196:199], v[148:151]\n"
"ds_read_b128 a[128:131], v40 offset:0\n"
"v_mfma_f32_16x16x32_bf16 v[152:155], a[228:231], a[200:203], v[152:155]\n"
"v_mfma_f32_16x16x32_bf16 v[156:159], a[228:231], a[204:207], v[156:159]\n"
"ds_read_b128 a[132:135], v40 offset:2048\n"
"v_mfma_f32_16x16x32_bf16 v[160:163], a[232:235], a[192:195], v[160:163]\n"
"ds_read_b128 a[136:139], v40 offset:4096\n"
"v_mfma_f32_16x16x32_bf16 v[164:167], a[232:235], a[196:199], v[164:167]\n"
"ds_read_b128 a[140:143], v40 offset:6144\n"
"v_mfma_f32_16x16x32_bf16 v[168:171], a[232:235], a[200:203], v[168:171]\n"
"v_mfma_f32_16x16x32_bf16 v[172:175], a[232:235], a[204:207], v[172:175]\n"
"ds_read_b64_tr_b16 a[164:165], v43 offset:16384\n"
"v_mfma_f32_16x16x32_bf16 v[176:179], a[236:239], a[192:195], v[176:179]\n"
"ds_read_b64_tr_b16 a[166:167], v43 offset:17408\n"
"v_mfma_f32_16x16x32_bf16 v[180:183], a[236:239], a[196:199], v[180:183]\n"
"ds_read_b64_tr_b16 a[168:169], v44 offset:16384\n"
"v_mfma_f32_16x16x32_bf16 v[184:187], a[236:239], a[200:203], v[184:187]\n"
"v_mfma_f32_16x16x32_bf16 v[188:191], a[236:239], a[204:207], v[188:191]\n"
"ds_read_b64_tr_b16 a[170:171], v44 offset:17408\n"
"v_mfma_f32_16x16x32_bf16 v[192:195], a[240:243], a[192:195], v[192:195]\n"
"ds_read_b64_tr_b16 a[172:173], v45 offset:16384\n"
"v_mfma_f32_16x16x32_bf16 v[196:199], a[240:243], a[196:199], v[196:199]\n"
"ds_read_b64_tr_b16 a[174:175], v45 offset:17408\n"
"v_mfma_f32_16x16x32_bf16 v[200:203], a[240:243], a[200:203], v[200:203]\n"
"v_mfma_f32_16x16x32_bf16 v[204:207], a[240:243], a[204:207], v[204:207]\n"
"ds_read_b64_tr_b16 a[176:177], v42 offset:32768\n"
"v_mfma_f32_16x16x32_bf16 v[208:211], a[244:247], a[192:195], v[208:211]\n"
"ds_read_b64_tr_b16 a[178:179], v42 offset:33792\n"
"v_mfma_f32_16x16x32_bf16 v[212:215], a[244:247], a[196:199], v[212:215]\n"
"ds_read_b64_tr_b16 a[180:181], v43 offset:32768\n"
"v_mfma_f32_16x16x32_bf16 v[216:219], a[244:247], a[200:203], v[216:219]\n"
"v_mfma_f32_16x16x32_bf16 v[220:223], a[244:247], a[204:207], v[220:223]\n"
"ds_read_b64_tr_b16 a[182:183], v43 offset:33792\n"
"v_mfma_f32_16x16x32_bf16 v[224:227], a[248:251], a[192:195], v[224:227]\n"
"ds_read_b64_tr_b16 a[184:185], v44 offset:32768\n"
"v_mfma_f32_16x16x32_bf16 v[228:231], a[248:251], a[196:199], v[228:231]\n"
"ds_read_b64_tr_b16 a[186:187], v44 offset:33792\n"
"v_mfma_f32_16x16x32_bf16 v[232:235], a[248:251], a[200:203], v[232:235]\n"
"v_mfma_f32_16x16x32_bf16 v[236:239], a[248:251], a[204:207], v[236:239]\n"
"ds_read_b64_tr_b16 a[188:189], v45 offset:32768\n"
"v_mfma_f32_16x16x32_bf16 v[240:243], a[252:255], a[192:195], v[240:243]\n"
"ds_read_b64_tr_b16 a[190:191], v45 offset:33792\n"
"v_mfma_f32_16x16x32_bf16 v[244:247], a[252:255], a[196:199], v[244:247]\n"
"v_mfma_f32_16x16x32_bf16 v[248:251], a[252:255], a[200:203], v[248:251]\n"
"v_mfma_f32_16x16x32_bf16 v[252:255], a[252:255], a[204:207], v[252:255]\n"
"s_waitcnt lgkmcnt(0)\n"
"v_mfma_f32_16x16x32_bf16 v[128:131], a[160:163], a[128:131], v[128:131]\n"
"ds_read_b64_tr_b16 a[224:225], v42 offset:24576\n"
"v_mfma_f32_16x16x32_bf16 v[132:135], a[160:163], a[132:135], v[132:135]\n"
"ds_read_b64_tr_b16 a[226:227], v42 offset:25600\n"
"v_mfma_f32_16x16x32_bf16 v[136:139], a[160:163], a[136:139], v[136:139]\n"
"ds_read_b128 a[192:195], v41 offset:0\n"
"v_mfma_f32_16x16x32_bf16 v[140:143], a[160:163], a[140:143], v[140:143]\n"
"v_mfma_f32_16x16x32_bf16 v[144:147], a[164:167], a[128:131], v[144:147]\n"
"ds_read_b128 a[196:199], v41 offset:2048\n"
"v_mfma_f32_16x16x32_bf16 v[148:151], a[164:167], a[132:135], v[148:151]\n"
"ds_read_b128 a[200:203], v41 offset:4096\n"
"v_mfma_f32_16x16x32_bf16 v[152:155], a[164:167], a[136:139], v[152:155]\n"
"v_mfma_f32_16x16x32_bf16 v[156:159], a[164:167], a[140:143], v[156:159]\n"
"ds_read_b128 a[204:207], v41 offset:6144\n"
"v_mfma_f32_16x16x32_bf16 v[160:163], a[168:171], a[128:131], v[160:163]\n"
"ds_read_b64_tr_b16 a[228:229], v43 offset:24576\n"
"v_mfma_f32_16x16x32_bf16 v[164:167], a[168:171], a[132:135], v[164:167]\n"
"v_mfma_f32_16x16x32_bf16 v[168:171], a[168:171], a[136:139], v[168:171]\n"
"ds_read_b64_tr_b16 a[230:231], v43 offset:25600\n"
"v_mfma_f32_16x16x32_bf16 v[172:175], a[168:171], a[140:143], v[172:175]\n"
"ds_read_b64_tr_b16 a[232:233], v44 offset:24576\n"
"v_mfma_f32_16x16x32_bf16 v[176:179], a[172:175], a[128:131], v[176:179]\n"
"v_mfma_f32_16x16x32_bf16 v[180:183], a[172:175], a[132:135], v[180:183]\n"
"ds_read_b64_tr_b16 a[234:235], v44 offset:25600\n"
"v_mfma_f32_16x16x32_bf16 v[184:187], a[172:175], a[136:139], v[184:187]\n"
"ds_read_b64_tr_b16 a[236:237], v45 offset:24576\n"
"v_mfma_f32_16x16x32_bf16 v[188:191], a[172:175], a[140:143], v[188:191]\n"
"ds_read_b64_tr_b16 a[238:239], v45 offset:25600\n"
"v_mfma_f32_16x16x32_bf16 v[192:195], a[176:179], a[128:131], v[192:195]\n"
"v_mfma_f32_16x16x32_bf16 v[196:199], a[176:179], a[132:135], v[196:199]\n"
"ds_read_b64_tr_b16 a[240:241], v42 offset:40960\n"
"v_mfma_f32_16x16x32_bf16 v[200:203], a[176:179], a[136:139], v[200:203]\n"
"ds_read_b64_tr_b16 a[242:243], v42 offset:41984\n"
"v_mfma_f32_16x16x32_bf16 v[204:207], a[176:179], a[140:143], v[204:207]\n"
"v_mfma_f32_16x16x32_bf16 v[208:211], a[180:183], a[128:131], v[208:211]\n"
"ds_read_b64_tr_b16 a[244:245], v43 offset:40960\n"
"v_mfma_f32_16x16x32_bf16 v[212:215], a[180:183], a[132:135], v[212:215]\n"
"ds_read_b64_tr_b16 a[246:247], v43 offset:41984\n"
"v_mfma_f32_16x16x32_bf16 v[216:219], a[180:183], a[136:139], v[216:219]\n"
"v_mfma_f32_16x16x32_bf16 v[220:223], a[180:183], a[140:143], v[220:223]\n"
"ds_read_b64_tr_b16 a[248:249], v44 offset:40960\n"
"v_mfma_f32_16x16x32_bf16 v[224:227], a[184:187], a[128:131], v[224:227]\n"
"ds_read_b64_tr_b16 a[250:251], v44 offset:41984\n"
"v_mfma_f32_16x16x32_bf16 v[228:231], a[184:187], a[132:135], v[228:231]\n"
"v_mfma_f32_16x16x32_bf16 v[232:235], a[184:187], a[136:139], v[232:235]\n"
"ds_read_b64_tr_b16 a[252:253], v45 offset:40960\n"
"v_mfma_f32_16x16x32_bf16 v[236:239], a[184:187], a[140:143], v[236:239]\n"
"ds_read_b64_tr_b16 a[254:255], v45 offset:41984\n"
"v_mfma_f32_16x16x32_bf16 v[240:243], a[188:191], a[128:131], v[240:243]\n"
"v_mfma_f32_16x16x32_bf16 v[244:247], a[188:191], a[132:135], v[244:247]\n"
"v_mfma_f32_16x16x32_bf16 v[248:251], a[188:191], a[136:139], v[248:251]\n"
"s_add_u32 s51, s51, 49152\n"
"s_cmp_eq_u32 s51, 147456\n"
"s_cselect_b32 s51, 0, s51\n"
"v_mfma_f32_16x16x32_bf16 v[252:255], a[188:191], a[140:143], v[252:255]\n"
"s_waitcnt lgkmcnt(0) vmcnt(0)\n"
"s_barrier\n"
"v_mfma_f32_16x16x32_bf16 v[128:131], a[224:227], a[192:195], v[128:131]\n"
"v_add_u32 v40, s51, v46\n"
"v_add_u32 v41, s51, v47\n"
"v_mfma_f32_16x16x32_bf16 v[132:135], a[224:227], a[196:199], v[132:135]\n"
"v_add_u32 v42, s51, v48\n"
"v_add_u32 v43, s51, v49\n"
"v_mfma_f32_16x16x32_bf16 v[136:139], a[224:227], a[200:203], v[136:139]\n"
"v_add_u32 v44, s51, v50\n"
"v_add_u32 v45, s51, v51\n"
"v_mfma_f32_16x16x32_bf16 v[140:143], a[224:227], a[204:207], v[140:143]\n"
"ds_read_b64_tr_b16 a[160:161], v42 offset:16384\n"
"v_mfma_f32_16x16x32_bf16 v[144:147], a[228:231], a[192:195], v[144:147]\n"
"ds_read_b64_tr_b16 a[162:163], v42 offset:17408\n"
"v_mfma_f32_16x16x32_bf16 v[148:151], a[228:231], a[196:199], v[148:151]\n"
"ds_read_b128 a[128:131], v40 offset:0\n"
"v_mfma_f32_16x16x32_bf16 v[152:155], a[228:231], a[200:203], v[152:155]\n"
"v_mfma_f32_16x16x32_bf16 v[156:159], a[228:231], a[204:207], v[156:159]\n"
"ds_read_b128 a[132:135], v40 offset:2048\n"
"v_mfma_f32_16x16x32_bf16 v[160:163], a[232:235], a[192:195], v[160:163]\n"
"ds_read_b128 a[136:139], v40 offset:4096\n"
"v_mfma_f32_16x16x32_bf16 v[164:167], a[232:235], a[196:199], v[164:167]\n"
"ds_read_b128 a[140:143], v40 offset:6144\n"
"v_mfma_f32_16x16x32_bf16 v[168:171], a[232:235], a[200:203], v[168:171]\n"
"v_mfma_f32_16x16x32_bf16 v[172:175], a[232:235], a[204:207], v[172:175]\n"
"ds_read_b64_tr_b16 a[164:165], v43 offset:16384\n"
"v_mfma_f32_16x16x32_bf16 v[176:179], a[236:239], a[192:195], v[176:179]\n"
"ds_read_b64_tr_b16 a[166:167], v43 offset:17408\n"
"v_mfma_f32_16x16x32_bf16 v[180:183], a[236:239], a[196:199], v[180:183]\n"
"ds_read_b64_tr_b16 a[168:169], v44 offset:16384\n"
"v_mfma_f32_16x16x32_bf16 v[184:187], a[236:239], a[200:203], v[184:187]\n"
"v_mfma_f32_16x16x32_bf16 v[188:191], a[236:239], a[204:207], v[188:191]\n"
"ds_read_b64_tr_b16 a[170:171], v44 offset:17408\n"
"v_mfma_f32_16x16x32_bf16 v[192:195], a[240:243], a[192:195], v[192:195]\n"
"ds_read_b64_tr_b16 a[172:173], v45 offset:16384\n"
"v_mfma_f32_16x16x32_bf16 v[196:199], a[240:243], a[196:199], v[196:199]\n"
"ds_read_b64_tr_b16 a[174:175], v45 offset:17408\n"
"v_mfma_f32_16x16x32_bf16 v[200:203], a[240:243], a[200:203], v[200:203]\n"
"v_mfma_f32_16x16x32_bf16 v[204:207], a[240:243], a[204:207], v[204:207]\n"
"ds_read_b64_tr_b16 a[176:177], v42 offset:32768\n"
"v_mfma_f32_16x16x32_bf16 v[208:211], a[244:247], a[192:195], v[208:211]\n"
"ds_read_b64_tr_b16 a[178:179], v42 offset:33792\n"
"v_mfma_f32_16x16x32_bf16 v[212:215], a[244:247], a[196:199], v[212:215]\n"
"ds_read_b64_tr_b16 a[180:181], v43 offset:32768\n"
"v_mfma_f32_16x16x32_bf16 v[216:219], a[244:247], a[200:203], v[216:219]\n"
"v_mfma_f32_16x16x32_bf16 v[220:223], a[244:247], a[204:207], v[220:223]\n"
"ds_read_b64_tr_b16 a[182:183], v43 offset:33792\n"
"v_mfma_f32_16x16x32_bf16 v[224:227], a[248:251], a[192:195], v[224:227]\n"
"ds_read_b64_tr_b16 a[184:185], v44 offset:32768\n"
"v_mfma_f32_16x16x32_bf16 v[228:231], a[248:251], a[196:199], v[228:231]\n"
"ds_read_b64_tr_b16 a[186:187], v44 offset:33792\n"
"v_mfma_f32_16x16x32_bf16 v[232:235], a[248:251], a[200:203], v[232:235]\n"
"v_mfma_f32_16x16x32_bf16 v[236:239], a[248:251], a[204:207], v[236:239]\n"
"ds_read_b64_tr_b16 a[188:189], v45 offset:32768\n"
"v_mfma_f32_16x16x32_bf16 v[240:243], a[252:255], a[192:195], v[240:243]\n"
"ds_read_b64_tr_b16 a[190:191], v45 offset:33792\n"
"v_mfma_f32_16x16x32_bf16 v[244:247], a[252:255], a[196:199], v[244:247]\n"
"v_mfma_f32_16x16x32_bf16 v[248:251], a[252:255], a[200:203], v[248:251]\n"
"v_mfma_f32_16x16x32_bf16 v[252:255], a[252:255], a[204:207], v[252:255]\n"
"s_waitcnt lgkmcnt(0)\n"
"v_mfma_f32_16x16x32_bf16 v[128:131], a[160:163], a[128:131], v[128:131]\n"
"ds_read_b64_tr_b16 a[224:225], v42 offset:24576\n"
"v_mfma_f32_16x16x32_bf16 v[132:135], a[160:163], a[132:135], v[132:135]\n"
"ds_read_b64_tr_b16 a[226:227], v42 offset:25600\n"
"v_mfma_f32_16x16x32_bf16 v[136:139], a[160:163], a[136:139], v[136:139]\n"
"ds_read_b128 a[192:195], v41 offset:0\n"
"v_mfma_f32_16x16x32_bf16 v[140:143], a[160:163], a[140:143], v[140:143]\n"
"v_mfma_f32_16x16x32_bf16 v[144:147], a[164:167], a[128:131], v[144:147]\n"
"ds_read_b128 a[196:199], v41 offset:2048\n"
"v_mfma_f32_16x16x32_bf16 v[148:151], a[164:167], a[132:135], v[148:151]\n"
"ds_read_b128 a[200:203], v41 offset:4096\n"
"v_mfma_f32_16x16x32_bf16 v[152:155], a[164:167], a[136:139], v[152:155]\n"
"v_mfma_f32_16x16x32_bf16 v[156:159], a[164:167], a[140:143], v[156:159]\n"
"ds_read_b128 a[204:207], v41 offset:6144\n"
"v_mfma_f32_16x16x32_bf16 v[160:163], a[168:171], a[128:131], v[160:163]\n"
"ds_read_b64_tr_b16 a[228:229], v43 offset:24576\n"
"v_mfma_f32_16x16x32_bf16 v[164:167], a[168:171], a[132:135], v[164:167]\n"
"v_mfma_f32_16x16x32_bf16 v[168:171], a[168:171], a[136:139], v[168:171]\n"
"ds_read_b64_tr_b16 a[230:231], v43 offset:25600\n"
"v_mfma_f32_16x16x32_bf16 v[172:175], a[168:171], a[140:143], v[172:175]\n"
"ds_read_b64_tr_b16 a[232:233], v44 offset:24576\n"
"v_mfma_f32_16x16x32_bf16 v[176:179], a[172:175], a[128:131], v[176:179]\n"
"v_mfma_f32_16x16x32_bf16 v[180:183], a[172:175], a[132:135], v[180:183]\n"
"ds_read_b64_tr_b16 a[234:235], v44 offset:25600\n"
"v_mfma_f32_16x16x32_bf16 v[184:187], a[172:175], a[136:139], v[184:187]\n"
"ds_read_b64_tr_b16 a[236:237], v45 offset:24576\n"
"v_mfma_f32_16x16x32_bf16 v[188:191], a[172:175], a[140:143], v[188:191]\n"
"ds_read_b64_tr_b16 a[238:239], v45 offset:25600\n"
"v_mfma_f32_16x16x32_bf16 v[192:195], a[176:179], a[128:131], v[192:195]\n"
"v_mfma_f32_16x16x32_bf16 v[196:199], a[176:179], a[132:135], v[196:199]\n"
"ds_read_b64_tr_b16 a[240:241], v42 offset:40960\n"
"v_mfma_f32_16x16x32_bf16 v[200:203], a[176:179], a[136:139], v[200:203]\n"
"ds_read_b64_tr_b16 a[242:243], v42 offset:41984\n"
"v_mfma_f32_16x16x32_bf16 v[204:207], a[176:179], a[140:143], v[204:207]\n"
"v_mfma_f32_16x16x32_bf16 v[208:211], a[180:183], a[128:131], v[208:211]\n"
"ds_read_b64_tr_b16 a[244:245], v43 offset:40960\n"
"v_mfma_f32_16x16x32_bf16 v[212:215], a[180:183], a[132:135], v[212:215]\n"
"ds_read_b64_tr_b16 a[246:247], v43 offset:41984\n"
"v_mfma_f32_16x16x32_bf16 v[216:219], a[180:183], a[136:139], v[216:219]\n"
"v_mfma_f32_16x16x32_bf16 v[220:223], a[180:183], a[140:143], v[220:223]\n"
"ds_read_b64_tr_b16 a[248:249], v44 offset:40960\n"
"v_mfma_f32_16x16x32_bf16 v[224:227], a[184:187], a[128:131], v[224:227]\n"
"ds_read_b64_tr_b16 a[250:251], v44 offset:41984\n"
"v_mfma_f32_16x16x32_bf16 v[228:231], a[184:187], a[132:135], v[228:231]\n"
"v_mfma_f32_16x16x32_bf16 v[232:235], a[184:187], a[136:139], v[232:235]\n"
"ds_read_b64_tr_b16 a[252:253], v45 offset:40960\n"
"v_mfma_f32_16x16x32_bf16 v[236:239], a[184:187], a[140:143], v[236:239]\n"
"ds_read_b64_tr_b16 a[254:255], v45 offset:41984\n"
"v_mfma_f32_16x16x32_bf16 v[240:243], a[188:191], a[128:131], v[240:243]\n"
"v_mfma_f32_16x16x32_bf16 v[244:247], a[188:191], a[132:135], v[244:247]\n"
"v_mfma_f32_16x16x32_bf16 v[248:251], a[188:191], a[136:139], v[248:251]\n"
"s_add_u32 s51, s51, 49152\n"
"s_cmp_eq_u32 s51, 147456\n"
"s_cselect_b32 s51, 0, s51\n"
"v_mfma_f32_16x16x32_bf16 v[252:255], a[188:191], a[140:143], v[252:255]\n"
"s_waitcnt lgkmcnt(0)\n"
"v_mfma_f32_16x16x32_bf16 v[128:131], a[224:227], a[192:195], v[128:131]\n"
"v_mfma_f32_16x16x32_bf16 v[132:135], a[224:227], a[196:199], v[132:135]\n"
"v_mfma_f32_16x16x32_bf16 v[136:139], a[224:227], a[200:203], v[136:139]\n"
"v_mfma_f32_16x16x32_bf16 v[140:143], a[224:227], a[204:207], v[140:143]\n"
"v_mfma_f32_16x16x32_bf16 v[144:147], a[228:231], a[192:195], v[144:147]\n"
"v_mfma_f32_16x16x32_bf16 v[148:151], a[228:231], a[196:199], v[148:151]\n"
"v_mfma_f32_16x16x32_bf16 v[152:155], a[228:231], a[200:203], v[152:155]\n"
"v_mfma_f32_16x16x32_bf16 v[156:159], a[228:231], a[204:207], v[156:159]\n"
"v_mfma_f32_16x16x32_bf16 v[160:163], a[232:235], a[192:195], v[160:163]\n"
"v_mfma_f32_16x16x32_bf16 v[164:167], a[232:235], a[196:199], v[164:167]\n"
"v_mfma_f32_16x16x32_bf16 v[168:171], a[232:235], a[200:203], v[168:171]\n"
"v_mfma_f32_16x16x32_bf16 v[172:175], a[232:235], a[204:207], v[172:175]\n"
"v_mfma_f32_16x16x32_bf16 v[176:179], a[236:239], a[192:195], v[176:179]\n"
"v_mfma_f32_16x16x32_bf16 v[180:183], a[236:239], a[196:199], v[180:183]\n"
"v_mfma_f32_16x16x32_bf16 v[184:187], a[236:239], a[200:203], v[184:187]\n"
"v_mfma_f32_16x16x32_bf16 v[188:191], a[236:239], a[204:207], v[188:191]\n"
"v_mfma_f32_16x16x32_bf16 v[192:195], a[240:243], a[192:195], v[192:195]\n"
"v_mfma_f32_16x16x32_bf16 v[196:199], a[240:243], a[196:199], v[196:199]\n"
"v_mfma_f32_16x16x32_bf16 v[200:203], a[240:243], a[200:203], v[200:203]\n"
"v_mfma_f32_16x16x32_bf16 v[204:207], a[240:243], a[204:207], v[204:207]\n"
"v_mfma_f32_16x16x32_bf16 v[208:211], a[244:247], a[192:195], v[208:211]\n"
"v_mfma_f32_16x16x32_bf16 v[212:215], a[244:247], a[196:199], v[212:215]\n"
"v_mfma_f32_16x16x32_bf16 v[216:219], a[244:247], a[200:203], v[216:219]\n"
"v_mfma_f32_16x16x32_bf16 v[220:223], a[244:247], a[204:207], v[220:223]\n"
"v_mfma_f32_16x16x32_bf16 v[224:227], a[248:251], a[192:195], v[224:227]\n"
"v_mfma_f32_16x16x32_bf16 v[228:231], a[248:251], a[196:199], v[228:231]\n"
"v_mfma_f32_16x16x32_bf16 v[232:235], a[248:251], a[200:203], v[232:235]\n"
"v_mfma_f32_16x16x32_bf16 v[236:239], a[248:251], a[204:207], v[236:239]\n"
"v_mfma_f32_16x16x32_bf16 v[240:243], a[252:255], a[192:195], v[240:243]\n"
"v_mfma_f32_16x16x32_bf16 v[244:247], a[252:255], a[196:199], v[244:247]\n"
"v_mfma_f32_16x16x32_bf16 v[248:251], a[252:255], a[200:203], v[248:251]\n"
"v_mfma_f32_16x16x32_bf16 v[252:255], a[252:255], a[204:207], v[252:255]\n"
"s_barrier\n"
"s_cmp_lg_u32 %[kind], 0\n"
"s_cbranch_scc1 NX0_%=\n"
"s_add_u32 s54, %[o0], 0\n"
"v_cvt_pk_bf16_f32 v60, v128, v129\n"
"v_cvt_pk_bf16_f32 v61, v130, v131\n"
"v_cvt_pk_bf16_f32 v62, v144, v145\n"
"v_cvt_pk_bf16_f32 v63, v146, v147\n"
"s_nop 0\n"
"v_permlane16_swap_b32 v60, v62\n"
"v_permlane16_swap_b32 v61, v63\n"
"buffer_store_dwordx4 v[60:63], %[vst], s[56:59], s54 offen offset:0 sc1\n"
"v_cvt_pk_bf16_f32 v64, v160, v161\n"
"v_cvt_pk_bf16_f32 v65, v162, v163\n"
"v_cvt_pk_bf16_f32 v66, v176, v177\n"
"v_cvt_pk_bf16_f32 v67, v178, v179\n"
"s_nop 0\n"
"v_permlane16_swap_b32 v64, v66\n"
"v_permlane16_swap_b32 v65, v67\n"
"buffer_store_dwordx4 v[64:67], %[vst], s[56:59], s54 offen offset:1024 sc1\n"
"s_add_u32 s54, %[o0], 0\n"
"s_add_u32 s54, s54, %[o1]\n"
"v_cvt_pk_bf16_f32 v68, v192, v193\n"
"v_cvt_pk_bf16_f32 v69, v194, v195\n"
"v_cvt_pk_bf16_f32 v70, v208, v209\n"
"v_cvt_pk_bf16_f32 v71, v210, v211\n"
"s_nop 0\n"
"v_permlane16_swap_b32 v68, v70\n"
"v_permlane16_swap_b32 v69, v71\n"
"buffer_store_dwordx4 v[68:71], %[vst], s[56:59], s54 offen offset:0 sc1\n"
"v_cvt_pk_bf16_f32 v72, v224, v225\n"
"v_cvt_pk_bf16_f32 v73, v226, v227\n"
"v_cvt_pk_bf16_f32 v74, v240, v241\n"
"v_cvt_pk_bf16_f32 v75, v242, v243\n"
"s_nop 0\n"
"v_permlane16_swap_b32 v72, v74\n"
"v_permlane16_swap_b32 v73, v75\n"
"buffer_store_dwordx4 v[72:75], %[vst], s[56:59], s54 offen offset:1024 sc1\n"
"s_add_u32 s54, %[o0], 4096\n"
"v_cvt_pk_bf16_f32 v60, v132, v133\n"
"v_cvt_pk_bf16_f32 v61, v134, v135\n"
"v_cvt_pk_bf16_f32 v62, v148, v149\n"
"v_cvt_pk_bf16_f32 v63, v150, v151\n"
"s_nop 0\n"
"v_permlane16_swap_b32 v60, v62\n"
"v_permlane16_swap_b32 v61, v63\n"
"buffer_store_dwordx4 v[60:63], %[vst], s[56:59], s54 offen offset:0 sc1\n"
"v_cvt_pk_bf16_f32 v64, v164, v165\n"
"v_cvt_pk_bf16_f32 v65, v166, v167\n"
"v_cvt_pk_bf16_f32 v66, v180, v181\n"
"v_cvt_pk_bf16_f32 v67, v182, v183\n"
"s_nop 0\n"
"v_permlane16_swap_b32 v64, v66\n"
"v_permlane16_swap_b32 v65, v67\n"
"buffer_store_dwordx4 v[64:67], %[vst], s[56:59], s54 offen offset:1024 sc1\n"
"s_add_u32 s54, %[o0], 4096\n"
"s_add_u32 s54, s54, %[o1]\n"
"v_cvt_pk_bf16_f32 v68, v196, v197\n"
"v_cvt_pk_bf16_f32 v69, v198, v199\n"
"v_cvt_pk_bf16_f32 v70, v212, v213\n"
"v_cvt_pk_bf16_f32 v71, v214, v215\n"
"s_nop 0\n"
"v_permlane16_swap_b32 v68, v70\n"
"v_permlane16_swap_b32 v69, v71\n"
"buffer_store_dwordx4 v[68:71], %[vst], s[56:59], s54 offen offset:0 sc1\n"
"v_cvt_pk_bf16_f32 v72, v228, v229\n"
"v_cvt_pk_bf16_f32 v73, v230, v231\n"
"v_cvt_pk_bf16_f32 v74, v244, v245\n"
"v_cvt_pk_bf16_f32 v75, v246, v247\n"
"s_nop 0\n"
"v_permlane16_swap_b32 v72, v74\n"
"v_permlane16_swap_b32 v73, v75\n"
"buffer_store_dwordx4 v[72:75], %[vst], s[56:59], s54 offen offset:1024 sc1\n"
"s_add_u32 s54, %[o0], 262144\n"
"v_cvt_pk_bf16_f32 v60, v136, v137\n"
"v_cvt_pk_bf16_f32 v61, v138, v139\n"
"v_cvt_pk_bf16_f32 v62, v152, v153\n"
"v_cvt_pk_bf16_f32 v63, v154, v155\n"
"s_nop 0\n"
"v_permlane16_swap_b32 v60, v62\n"
"v_permlane16_swap_b32 v61, v63\n"
"buffer_store_dwordx4 v[60:63], %[vst], s[56:59], s54 offen offset:0 sc1\n"
"v_cvt_pk_bf16_f32 v64, v168, v169\n"
"v_cvt_pk_bf16_f32 v65, v170, v171\n"
"v_cvt_pk_bf16_f32 v66, v184, v185\n"
"v_cvt_pk_bf16_f32 v67, v186, v187\n"
"s_nop 0\n"
"v_permlane16_swap_b32 v64, v66\n"
"v_permlane16_swap_b32 v65, v67\n"
"buffer_store_dwordx4 v[64:67], %[vst], s[56:59], s54 offen offset:1024 sc1\n"
"s_add_u32 s54, %[o0], 262144\n"
"s_add_u32 s54, s54, %[o1]\n"
"v_cvt_pk_bf16_f32 v68, v200, v201\n"
"v_cvt_pk_bf16_f32 v69, v202, v203\n"
"v_cvt_pk_bf16_f32 v70, v216, v217\n"
"v_cvt_pk_bf16_f32 v71, v218, v219\n"
"s_nop 0\n"
"v_permlane16_swap_b32 v68, v70\n"
"v_permlane16_swap_b32 v69, v71\n"
"buffer_store_dwordx4 v[68:71], %[vst], s[56:59], s54 offen offset:0 sc1\n"
"v_cvt_pk_bf16_f32 v72, v232, v233\n"
"v_cvt_pk_bf16_f32 v73, v234, v235\n"
"v_cvt_pk_bf16_f32 v74, v248, v249\n"
"v_cvt_pk_bf16_f32 v75, v250, v251\n"
"s_nop 0\n"
"v_permlane16_swap_b32 v72, v74\n"
"v_permlane16_swap_b32 v73, v75\n"
"buffer_store_dwordx4 v[72:75], %[vst], s[56:59], s54 offen offset:1024 sc1\n"
"s_add_u32 s54, %[o0], 266240\n"
"v_cvt_pk_bf16_f32 v60, v140, v141\n"
"v_cvt_pk_bf16_f32 v61, v142, v143\n"
"v_cvt_pk_bf16_f32 v62, v156, v157\n"
"v_cvt_pk_bf16_f32 v63, v158, v159\n"
"s_nop 0\n"
"v_permlane16_swap_b32 v60, v62\n"
"v_permlane16_swap_b32 v61, v63\n"
"buffer_store_dwordx4 v[60:63], %[vst], s[56:59], s54 offen offset:0 sc1\n"
"v_cvt_pk_bf16_f32 v64, v172, v173\n"
"v_cvt_pk_bf16_f32 v65, v174, v175\n"
"v_cvt_pk_bf16_f32 v66, v188, v189\n"
"v_cvt_pk_bf16_f32 v67, v190, v191\n"
"s_nop 0\n"
"v_permlane16_swap_b32 v64, v66\n"
"v_permlane16_swap_b32 v65, v67\n"
"buffer_store_dwordx4 v[64:67], %[vst], s[56:59], s54 offen offset:1024 sc1\n"
"s_add_u32 s54, %[o0], 266240\n"
"s_add_u32 s54, s54, %[o1]\n"
"v_cvt_pk_bf16_f32 v68, v204, v205\n"
"v_cvt_pk_bf16_f32 v69, v206, v207\n"
"v_cvt_pk_bf16_f32 v70, v220, v221\n"
"v_cvt_pk_bf16_f32 v71, v222, v223\n"
"s_nop 0\n"
"v_permlane16_swap_b32 v68, v70\n"
"v_permlane16_swap_b32 v69, v71\n"
"buffer_store_dwordx4 v[68:71], %[vst], s[56:59], s54 offen offset:0 sc1\n"
"v_cvt_pk_bf16_f32 v72, v236, v237\n"
"v_cvt_pk_bf16_f32 v73, v238, v239\n"
"v_cvt_pk_bf16_f32 v74, v252, v253\n"
"v_cvt_pk_bf16_f32 v75, v254, v255\n"
"s_nop 0\n"
"v_permlane16_swap_b32 v72, v74\n"
"v_permlane16_swap_b32 v73, v75\n"
"buffer_store_dwordx4 v[72:75], %[vst], s[56:59], s54 offen offset:1024 sc1\n"
"s_branch END_%=\n"
"NX0_%=:\n"
"s_cmp_lg_u32 %[kind], 2\n"
"s_cbranch_scc1 NX2_%=\n"
"s_add_u32 s54, %[o0], 0\n"
"v_cvt_pk_bf16_f32 v60, v128, v129\n"
"v_cvt_pk_bf16_f32 v61, v130, v131\n"
"v_cvt_pk_bf16_f32 v62, v144, v145\n"
"v_cvt_pk_bf16_f32 v63, v146, v147\n"
"s_nop 0\n"
"v_permlane16_swap_b32 v60, v62\n"
"v_permlane16_swap_b32 v61, v63\n"
"buffer_store_dwordx4 v[60:63], %[vst], s[56:59], s54 offen offset:0 sc1\n"
"v_cvt_pk_bf16_f32 v64, v160, v161\n"
"v_cvt_pk_bf16_f32 v65, v162, v163\n"
"v_cvt_pk_bf16_f32 v66, v176, v177\n"
"v_cvt_pk_bf16_f32 v67, v178, v179\n"
"s_nop 0\n"
"v_permlane16_swap_b32 v64, v66\n"
"v_permlane16_swap_b32 v65, v67\n"
"buffer_store_dwordx4 v[64:67], %[vst], s[56:59], s54 offen offset:1024 sc1\n"
"s_add_u32 s54, %[o0], 0\n"
"s_add_u32 s54, s54, %[o1]\n"
"v_cvt_pk_bf16_f32 v68, v192, v193\n"
"v_cvt_pk_bf16_f32 v69, v194, v195\n"
"v_cvt_pk_bf16_f32 v70, v208, v209\n"
"v_cvt_pk_bf16_f32 v71, v210, v211\n"
"s_nop 0\n"
"v_permlane16_swap_b32 v68, v70\n"
"v_permlane16_swap_b32 v69, v71\n"
"buffer_store_dwordx4 v[68:71], %[vst], s[56:59], s54 offen offset:0 sc1\n"
"v_cvt_pk_bf16_f32 v72, v224, v225\n"
"v_cvt_pk_bf16_f32 v73, v226, v227\n"
"v_cvt_pk_bf16_f32 v74, v240, v241\n"
"v_cvt_pk_bf16_f32 v75, v242, v243\n"
"s_nop 0\n"
"v_permlane16_swap_b32 v72, v74\n"
"v_permlane16_swap_b32 v73, v75\n"
"buffer_store_dwordx4 v[72:75], %[vst], s[56:59], s54 offen offset:1024 sc1\n"
"s_add_u32 s54, %[o0], 32768\n"
"v_cvt_pk_bf16_f32 v60, v132, v133\n"
"v_cvt_pk_bf16_f32 v61, v134, v135\n"
"v_cvt_pk_bf16_f32 v62, v148, v149\n"
"v_cvt_pk_bf16_f32 v63, v150, v151\n"
"s_nop 0\n"
"v_permlane16_swap_b32 v60, v62\n"
"v_permlane16_swap_b32 v61, v63\n"
"buffer_store_dwordx4 v[60:63], %[vst], s[56:59], s54 offen offset:0 sc1\n"
"v_cvt_pk_bf16_f32 v64, v164, v165\n"
"v_cvt_pk_bf16_f32 v65, v166, v167\n"
"v_cvt_pk_bf16_f32 v66, v180, v181\n"
"v_cvt_pk_bf16_f32 v67, v182, v183\n"
"s_nop 0\n"
"v_permlane16_swap_b32 v64, v66\n"
"v_permlane16_swap_b32 v65, v67\n"
"buffer_store_dwordx4 v[64:67], %[vst], s[56:59], s54 offen offset:1024 sc1\n"
"s_add_u32 s54, %[o0], 32768\n"
"s_add_u32 s54, s54, %[o1]\n"
"v_cvt_pk_bf16_f32 v68, v196, v197\n"
"v_cvt_pk_bf16_f32 v69, v198, v199\n"
"v_cvt_pk_bf16_f32 v70, v212, v213\n"
"v_cvt_pk_bf16_f32 v71, v214, v215\n"
"s_nop 0\n"
"v_permlane16_swap_b32 v68, v70\n"
"v_permlane16_swap_b32 v69, v71\n"
"buffer_store_dwordx4 v[68:71], %[vst], s[56:59], s54 offen offset:0 sc1\n"
"v_cvt_pk_bf16_f32 v72, v228, v229\n"
"v_cvt_pk_bf16_f32 v73, v230, v231\n"
"v_cvt_pk_bf16_f32 v74, v244, v245\n"
"v_cvt_pk_bf16_f32 v75, v246, v247\n"
"s_nop 0\n"
"v_permlane16_swap_b32 v72, v74\n"
"v_permlane16_swap_b32 v73, v75\n"
"buffer_store_dwordx4 v[72:75], %[vst], s[56:59], s54 offen offset:1024 sc1\n"
"s_add_u32 s54, %[o0], 65536\n"
"v_cvt_pk_bf16_f32 v60, v136, v137\n"
"v_cvt_pk_bf16_f32 v61, v138, v139\n"
"v_cvt_pk_bf16_f32 v62, v152, v153\n"
"v_cvt_pk_bf16_f32 v63, v154, v155\n"
"s_nop 0\n"
"v_permlane16_swap_b32 v60, v62\n"
"v_permlane16_swap_b32 v61, v63\n"
"buffer_store_dwordx4 v[60:63], %[vst], s[56:59], s54 offen offset:0 sc1\n"
"v_cvt_pk_bf16_f32 v64, v168, v169\n"
"v_cvt_pk_bf16_f32 v65, v170, v171\n"
"v_cvt_pk_bf16_f32 v66, v184, v185\n"
"v_cvt_pk_bf16_f32 v67, v186, v187\n"
"s_nop 0\n"
"v_permlane16_swap_b32 v64, v66\n"
"v_permlane16_swap_b32 v65, v67\n"
"buffer_store_dwordx4 v[64:67], %[vst], s[56:59], s54 offen offset:1024 sc1\n"
"s_add_u32 s54, %[o0], 65536\n"
"s_add_u32 s54, s54, %[o1]\n"
"v_cvt_pk_bf16_f32 v68, v200, v201\n"
"v_cvt_pk_bf16_f32 v69, v202, v203\n"
"v_cvt_pk_bf16_f32 v70, v216, v217\n"
"v_cvt_pk_bf16_f32 v71, v218, v219\n"
"s_nop 0\n"
"v_permlane16_swap_b32 v68, v70\n"
"v_permlane16_swap_b32 v69, v71\n"
"buffer_store_dwordx4 v[68:71], %[vst], s[56:59], s54 offen offset:0 sc1\n"
"v_cvt_pk_bf16_f32 v72, v232, v233\n"
"v_cvt_pk_bf16_f32 v73, v234, v235\n"
"v_cvt_pk_bf16_f32 v74, v248, v249\n"
"v_cvt_pk_bf16_f32 v75, v250, v251\n"
"s_nop 0\n"
"v_permlane16_swap_b32 v72, v74\n"
"v_permlane16_swap_b32 v73, v75\n"
"buffer_store_dwordx4 v[72:75], %[vst], s[56:59], s54 offen offset:1024 sc1\n"
"s_add_u32 s54, %[o0], 98304\n"
"v_cvt_pk_bf16_f32 v60, v140, v141\n"
"v_cvt_pk_bf16_f32 v61, v142, v143\n"
"v_cvt_pk_bf16_f32 v62, v156, v157\n"
"v_cvt_pk_bf16_f32 v63, v158, v159\n"
"s_nop 0\n"
"v_permlane16_swap_b32 v60, v62\n"
"v_permlane16_swap_b32 v61, v63\n"
"buffer_store_dwordx4 v[60:63], %[vst], s[56:59], s54 offen offset:0 sc1\n"
"v_cvt_pk_bf16_f32 v64, v172, v173\n"
"v_cvt_pk_bf16_f32 v65, v174, v175\n"
"v_cvt_pk_bf16_f32 v66, v188, v189\n"
"v_cvt_pk_bf16_f32 v67, v190, v191\n"
"s_nop 0\n"
"v_permlane16_swap_b32 v64, v66\n"
"v_permlane16_swap_b32 v65, v67\n"
"buffer_store_dwordx4 v[64:67], %[vst], s[56:59], s54 offen offset:1024 sc1\n"
"s_add_u32 s54, %[o0], 98304\n"
"s_add_u32 s54, s54, %[o1]\n"
"v_cvt_pk_bf16_f32 v68, v204, v205\n"
"v_cvt_pk_bf16_f32 v69, v206, v207\n"
"v_cvt_pk_bf16_f32 v70, v220, v221\n"
"v_cvt_pk_bf16_f32 v71, v222, v223\n"
"s_nop 0\n"
"v_permlane16_swap_b32 v68, v70\n"
"v_permlane16_swap_b32 v69, v71\n"
"buffer_store_dwordx4 v[68:71], %[vst], s[56:59], s54 offen offset:0 sc1\n"
"v_cvt_pk_bf16_f32 v72, v236, v237\n"
"v_cvt_pk_bf16_f32 v73, v238, v239\n"
"v_cvt_pk_bf16_f32 v74, v252, v253\n"
"v_cvt_pk_bf16_f32 v75, v254, v255\n"
"s_nop 0\n"
"v_permlane16_swap_b32 v72, v74\n"
"v_permlane16_swap_b32 v73, v75\n"
"buffer_store_dwordx4 v[72:75], %[vst], s[56:59], s54 offen offset:1024 sc1\n"
"s_branch END_%=\n"
"NX2_%=:\n"
"s_mov_b32 s64, 0xbfb8aa3b\n"
"s_mov_b32 s65, s64\n"
"s_mov_b32 s66, 1.0\n"
"s_mov_b32 s67, 1.0\n"
"s_add_u32 s54, %[o0], 0\n"
"v_pk_mul_f32 v[52:53], v[128:129], s[64:65]\n"
"v_pk_mul_f32 v[54:55], v[130:131], s[64:65]\n"
"v_exp_f32 v52, v52\n"
"v_exp_f32 v53, v53\n"
"v_exp_f32 v54, v54\n"
"v_exp_f32 v55, v55\n"
"s_nop 0\n"
"v_pk_add_f32 v[52:53], v[52:53], s[66:67]\n"
"v_pk_add_f32 v[54:55], v[54:55], s[66:67]\n"
"v_rcp_f32 v52, v52\n"
"v_rcp_f32 v53, v53\n"
"v_rcp_f32 v54, v54\n"
"v_rcp_f32 v55, v55\n"
"v_pk_mul_f32 v[128:129], v[128:129], v[192:193]\n"
"v_pk_mul_f32 v[130:131], v[130:131], v[194:195]\n"
"v_pk_mul_f32 v[128:129], v[128:129], v[52:53]\n"
"v_pk_mul_f32 v[130:131], v[130:131], v[54:55]\n"
"v_cvt_pk_bf16_f32 v68, v128, v129\n"
"v_cvt_pk_bf16_f32 v69, v130, v131\n"
"v_pk_mul_f32 v[52:53], v[144:145], s[64:65]\n"
"v_pk_mul_f32 v[54:55], v[146:147], s[64:65]\n"
"v_exp_f32 v52, v52\n"
"v_exp_f32 v53, v53\n"
"v_exp_f32 v54, v54\n"
"v_exp_f32 v55, v55\n"
"s_nop 0\n"
"v_pk_add_f32 v[52:53], v[52:53], s[66:67]\n"
"v_pk_add_f32 v[54:55], v[54:55], s[66:67]\n"
"v_rcp_f32 v52, v52\n"
"v_rcp_f32 v53, v53\n"
"v_rcp_f32 v54, v54\n"
"v_rcp_f32 v55, v55\n"
"v_pk_mul_f32 v[144:145], v[144:145], v[208:209]\n"
"v_pk_mul_f32 v[146:147], v[146:147], v[210:211]\n"
"v_pk_mul_f32 v[144:145], v[144:145], v[52:53]\n"
"v_pk_mul_f32 v[146:147], v[146:147], v[54:55]\n"
"v_cvt_pk_bf16_f32 v70, v144, v145\n"
"v_cvt_pk_bf16_f32 v71, v146, v147\n"
"s_nop 0\n"
"v_permlane16_swap_b32 v68, v70\n"
"v_permlane16_swap_b32 v69, v71\n"
"buffer_store_dwordx4 v[68:71], %[vst], s[56:59], s54 offen offset:0 sc1\n"
"v_pk_mul_f32 v[52:53], v[160:161], s[64:65]\n"
"v_pk_mul_f32 v[54:55], v[162:163], s[64:65]\n"
"v_exp_f32 v52, v52\n"
"v_exp_f32 v53, v53\n"
"v_exp_f32 v54, v54\n"
"v_exp_f32 v55, v55\n"
"s_nop 0\n"
"v_pk_add_f32 v[52:53], v[52:53], s[66:67]\n"
"v_pk_add_f32 v[54:55], v[54:55], s[66:67]\n"
"v_rcp_f32 v52, v52\n"
"v_rcp_f32 v53, v53\n"
"v_rcp_f32 v54, v54\n"
"v_rcp_f32 v55, v55\n"
"v_pk_mul_f32 v[160:161], v[160:161], v[224:225]\n"
"v_pk_mul_f32 v[162:163], v[162:163], v[226:227]\n"
"v_pk_mul_f32 v[160:161], v[160:161], v[52:53]\n"
"v_pk_mul_f32 v[162:163], v[162:163], v[54:55]\n"
"v_cvt_pk_bf16_f32 v72, v160, v161\n"
"v_cvt_pk_bf16_f32 v73, v162, v163\n"
"v_pk_mul_f32 v[52:53], v[176:177], s[64:65]\n"
"v_pk_mul_f32 v[54:55], v[178:179], s[64:65]\n"
"v_exp_f32 v52, v52\n"
"v_exp_f32 v53, v53\n"
"v_exp_f32 v54, v54\n"
"v_exp_f32 v55, v55\n"
"s_nop 0\n"
"v_pk_add_f32 v[52:53], v[52:53], s[66:67]\n"
"v_pk_add_f32 v[54:55], v[54:55], s[66:67]\n"
"v_rcp_f32 v52, v52\n"
"v_rcp_f32 v53, v53\n"
"v_rcp_f32 v54, v54\n"
"v_rcp_f32 v55, v55\n"
"v_pk_mul_f32 v[176:177], v[176:177], v[240:241]\n"
"v_pk_mul_f32 v[178:179], v[178:179], v[242:243]\n"
"v_pk_mul_f32 v[176:177], v[176:177], v[52:53]\n"
"v_pk_mul_f32 v[178:179], v[178:179], v[54:55]\n"
"v_cvt_pk_bf16_f32 v74, v176, v177\n"
"v_cvt_pk_bf16_f32 v75, v178, v179\n"
"s_nop 0\n"
"v_permlane16_swap_b32 v72, v74\n"
"v_permlane16_swap_b32 v73, v75\n"
"buffer_store_dwordx4 v[72:75], %[vst], s[56:59], s54 offen offset:1024 sc1\n"
"s_add_u32 s54, %[o0], 114688\n"
"v_pk_mul_f32 v[52:53], v[132:133], s[64:65]\n"
"v_pk_mul_f32 v[54:55], v[134:135], s[64:65]\n"
"v_exp_f32 v52, v52\n"
"v_exp_f32 v53, v53\n"
"v_exp_f32 v54, v54\n"
"v_exp_f32 v55, v55\n"
"s_nop 0\n"
"v_pk_add_f32 v[52:53], v[52:53], s[66:67]\n"
"v_pk_add_f32 v[54:55], v[54:55], s[66:67]\n"
"v_rcp_f32 v52, v52\n"
"v_rcp_f32 v53, v53\n"
"v_rcp_f32 v54, v54\n"
"v_rcp_f32 v55, v55\n"
"v_pk_mul_f32 v[132:133], v[132:133], v[196:197]\n"
"v_pk_mul_f32 v[134:135], v[134:135], v[198:199]\n"
"v_pk_mul_f32 v[132:133], v[132:133], v[52:53]\n"
"v_pk_mul_f32 v[134:135], v[134:135], v[54:55]\n"
"v_cvt_pk_bf16_f32 v76, v132, v133\n"
"v_cvt_pk_bf16_f32 v77, v134, v135\n"
"v_pk_mul_f32 v[52:53], v[148:149], s[64:65]\n"
"v_pk_mul_f32 v[54:55], v[150:151], s[64:65]\n"
"v_exp_f32 v52, v52\n"
"v_exp_f32 v53, v53\n"
"v_exp_f32 v54, v54\n"
"v_exp_f32 v55, v55\n"
"s_nop 0\n"
"v_pk_add_f32 v[52:53], v[52:53], s[66:67]\n"
"v_pk_add_f32 v[54:55], v[54:55], s[66:67]\n"
"v_rcp_f32 v52, v52\n"
"v_rcp_f32 v53, v53\n"
"v_rcp_f32 v54, v54\n"
"v_rcp_f32 v55, v55\n"
"v_pk_mul_f32 v[148:149], v[148:149], v[212:213]\n"
"v_pk_mul_f32 v[150:151], v[150:151], v[214:215]\n"
"v_pk_mul_f32 v[148:149], v[148:149], v[52:53]\n"
"v_pk_mul_f32 v[150:151], v[150:151], v[54:55]\n"
"v_cvt_pk_bf16_f32 v78, v148, v149\n"
"v_cvt_pk_bf16_f32 v79, v150, v151\n"
"s_nop 0\n"
"v_permlane16_swap_b32 v76, v78\n"
"v_permlane16_swap_b32 v77, v79\n"
"buffer_store_dwordx4 v[76:79], %[vst], s[56:59], s54 offen offset:0 sc1\n"
"v_pk_mul_f32 v[52:53], v[164:165], s[64:65]\n"
"v_pk_mul_f32 v[54:55], v[166:167], s[64:65]\n"
"v_exp_f32 v52, v52\n"
"v_exp_f32 v53, v53\n"
"v_exp_f32 v54, v54\n"
"v_exp_f32 v55, v55\n"
"s_nop 0\n"
"v_pk_add_f32 v[52:53], v[52:53], s[66:67]\n"
"v_pk_add_f32 v[54:55], v[54:55], s[66:67]\n"
"v_rcp_f32 v52, v52\n"
"v_rcp_f32 v53, v53\n"
"v_rcp_f32 v54, v54\n"
"v_rcp_f32 v55, v55\n"
"v_pk_mul_f32 v[164:165], v[164:165], v[228:229]\n"
"v_pk_mul_f32 v[166:167], v[166:167], v[230:231]\n"
"v_pk_mul_f32 v[164:165], v[164:165], v[52:53]\n"
"v_pk_mul_f32 v[166:167], v[166:167], v[54:55]\n"
"v_cvt_pk_bf16_f32 v80, v164, v165\n"
"v_cvt_pk_bf16_f32 v81, v166, v167\n"
"v_pk_mul_f32 v[52:53], v[180:181], s[64:65]\n"
"v_pk_mul_f32 v[54:55], v[182:183], s[64:65]\n"
"v_exp_f32 v52, v52\n"
"v_exp_f32 v53, v53\n"
"v_exp_f32 v54, v54\n"
"v_exp_f32 v55, v55\n"
"s_nop 0\n"
"v_pk_add_f32 v[52:53], v[52:53], s[66:67]\n"
"v_pk_add_f32 v[54:55], v[54:55], s[66:67]\n"
"v_rcp_f32 v52, v52\n"
"v_rcp_f32 v53, v53\n"
"v_rcp_f32 v54, v54\n"
"v_rcp_f32 v55, v55\n"
"v_pk_mul_f32 v[180:181], v[180:181], v[244:245]\n"
"v_pk_mul_f32 v[182:183], v[182:183], v[246:247]\n"
"v_pk_mul_f32 v[180:181], v[180:181], v[52:53]\n"
"v_pk_mul_f32 v[182:183], v[182:183], v[54:55]\n"
"v_cvt_pk_bf16_f32 v82, v180, v181\n"
"v_cvt_pk_bf16_f32 v83, v182, v183\n"
"s_nop 0\n"
"v_permlane16_swap_b32 v80, v82\n"
"v_permlane16_swap_b32 v81, v83\n"
"buffer_store_dwordx4 v[80:83], %[vst], s[56:59], s54 offen offset:1024 sc1\n"
"s_add_u32 s54, %[o0], 229376\n"
"v_pk_mul_f32 v[52:53], v[136:137], s[64:65]\n"
"v_pk_mul_f32 v[54:55], v[138:139], s[64:65]\n"
"v_exp_f32 v52, v52\n"
"v_exp_f32 v53, v53\n"
"v_exp_f32 v54, v54\n"
"v_exp_f32 v55, v55\n"
"s_nop 0\n"
"v_pk_add_f32 v[52:53], v[52:53], s[66:67]\n"
"v_pk_add_f32 v[54:55], v[54:55], s[66:67]\n"
"v_rcp_f32 v52, v52\n"
"v_rcp_f32 v53, v53\n"
"v_rcp_f32 v54, v54\n"
"v_rcp_f32 v55, v55\n"
"v_pk_mul_f32 v[136:137], v[136:137], v[200:201]\n"
"v_pk_mul_f32 v[138:139], v[138:139], v[202:203]\n"
"v_pk_mul_f32 v[136:137], v[136:137], v[52:53]\n"
"v_pk_mul_f32 v[138:139], v[138:139], v[54:55]\n"
"v_cvt_pk_bf16_f32 v68, v136, v137\n"
"v_cvt_pk_bf16_f32 v69, v138, v139\n"
"v_pk_mul_f32 v[52:53], v[152:153], s[64:65]\n"
"v_pk_mul_f32 v[54:55], v[154:155], s[64:65]\n"
"v_exp_f32 v52, v52\n"
"v_exp_f32 v53, v53\n"
"v_exp_f32 v54, v54\n"
"v_exp_f32 v55, v55\n"
"s_nop 0\n"
"v_pk_add_f32 v[52:53], v[52:53], s[66:67]\n"
"v_pk_add_f32 v[54:55], v[54:55], s[66:67]\n"
"v_rcp_f32 v52, v52\n"
"v_rcp_f32 v53, v53\n"
"v_rcp_f32 v54, v54\n"
"v_rcp_f32 v55, v55\n"
"v_pk_mul_f32 v[152:153], v[152:153], v[216:217]\n"
"v_pk_mul_f32 v[154:155], v[154:155], v[218:219]\n"
"v_pk_mul_f32 v[152:153], v[152:153], v[52:53]\n"
"v_pk_mul_f32 v[154:155], v[154:155], v[54:55]\n"
"v_cvt_pk_bf16_f32 v70, v152, v153\n"
"v_cvt_pk_bf16_f32 v71, v154, v155\n"
"s_nop 0\n"
"v_permlane16_swap_b32 v68, v70\n"
"v_permlane16_swap_b32 v69, v71\n"
"buffer_store_dwordx4 v[68:71], %[vst], s[56:59], s54 offen offset:0 sc1\n"
"v_pk_mul_f32 v[52:53], v[168:169], s[64:65]\n"
"v_pk_mul_f32 v[54:55], v[170:171], s[64:65]\n"
"v_exp_f32 v52, v52\n"
"v_exp_f32 v53, v53\n"
"v_exp_f32 v54, v54\n"
"v_exp_f32 v55, v55\n"
"s_nop 0\n"
"v_pk_add_f32 v[52:53], v[52:53], s[66:67]\n"
"v_pk_add_f32 v[54:55], v[54:55], s[66:67]\n"
"v_rcp_f32 v52, v52\n"
"v_rcp_f32 v53, v53\n"
"v_rcp_f32 v54, v54\n"
"v_rcp_f32 v55, v55\n"
"v_pk_mul_f32 v[168:169], v[168:169], v[232:233]\n"
"v_pk_mul_f32 v[170:171], v[170:171], v[234:235]\n"
"v_pk_mul_f32 v[168:169], v[168:169], v[52:53]\n"
"v_pk_mul_f32 v[170:171], v[170:171], v[54:55]\n"
"v_cvt_pk_bf16_f32 v72, v168, v169\n"
"v_cvt_pk_bf16_f32 v73, v170, v171\n"
"v_pk_mul_f32 v[52:53], v[184:185], s[64:65]\n"
"v_pk_mul_f32 v[54:55], v[186:187], s[64:65]\n"
"v_exp_f32 v52, v52\n"
"v_exp_f32 v53, v53\n"
"v_exp_f32 v54, v54\n"
"v_exp_f32 v55, v55\n"
"s_nop 0\n"
"v_pk_add_f32 v[52:53], v[52:53], s[66:67]\n"
"v_pk_add_f32 v[54:55], v[54:55], s[66:67]\n"
"v_rcp_f32 v52, v52\n"
"v_rcp_f32 v53, v53\n"
"v_rcp_f32 v54, v54\n"
"v_rcp_f32 v55, v55\n"
"v_pk_mul_f32 v[184:185], v[184:185], v[248:249]\n"
"v_pk_mul_f32 v[186:187], v[186:187], v[250:251]\n"
"v_pk_mul_f32 v[184:185], v[184:185], v[52:53]\n"
"v_pk_mul_f32 v[186:187], v[186:187], v[54:55]\n"
"v_cvt_pk_bf16_f32 v74, v184, v185\n"
"v_cvt_pk_bf16_f32 v75, v186, v187\n"
"s_nop 0\n"
"v_permlane16_swap_b32 v72, v74\n"
"v_permlane16_swap_b32 v73, v75\n"
"buffer_store_dwordx4 v[72:75], %[vst], s[56:59], s54 offen offset:1024 sc1\n"
"s_add_u32 s54, %[o0], 344064\n"
"v_pk_mul_f32 v[52:53], v[140:141], s[64:65]\n"
"v_pk_mul_f32 v[54:55], v[142:143], s[64:65]\n"
"v_exp_f32 v52, v52\n"
"v_exp_f32 v53, v53\n"
"v_exp_f32 v54, v54\n"
"v_exp_f32 v55, v55\n"
"s_nop 0\n"
"v_pk_add_f32 v[52:53], v[52:53], s[66:67]\n"
"v_pk_add_f32 v[54:55], v[54:55], s[66:67]\n"
"v_rcp_f32 v52, v52\n"
"v_rcp_f32 v53, v53\n"
"v_rcp_f32 v54, v54\n"
"v_rcp_f32 v55, v55\n"
"v_pk_mul_f32 v[140:141], v[140:141], v[204:205]\n"
"v_pk_mul_f32 v[142:143], v[142:143], v[206:207]\n"
"v_pk_mul_f32 v[140:141], v[140:141], v[52:53]\n"
"v_pk_mul_f32 v[142:143], v[142:143], v[54:55]\n"
"v_cvt_pk_bf16_f32 v76, v140, v141\n"
"v_cvt_pk_bf16_f32 v77, v142, v143\n"
"v_pk_mul_f32 v[52:53], v[156:157], s[64:65]\n"
"v_pk_mul_f32 v[54:55], v[158:159], s[64:65]\n"
"v_exp_f32 v52, v52\n"
"v_exp_f32 v53, v53\n"
"v_exp_f32 v54, v54\n"
"v_exp_f32 v55, v55\n"
"s_nop 0\n"
"v_pk_add_f32 v[52:53], v[52:53], s[66:67]\n"
"v_pk_add_f32 v[54:55], v[54:55], s[66:67]\n"
"v_rcp_f32 v52, v52\n"
"v_rcp_f32 v53, v53\n"
"v_rcp_f32 v54, v54\n"
"v_rcp_f32 v55, v55\n"
"v_pk_mul_f32 v[156:157], v[156:157], v[220:221]\n"
"v_pk_mul_f32 v[158:159], v[158:159], v[222:223]\n"
"v_pk_mul_f32 v[156:157], v[156:157], v[52:53]\n"
"v_pk_mul_f32 v[158:159], v[158:159], v[54:55]\n"
"v_cvt_pk_bf16_f32 v78, v156, v157\n"
"v_cvt_pk_bf16_f32 v79, v158, v159\n"
"s_nop 0\n"
"v_permlane16_swap_b32 v76, v78\n"
"v_permlane16_swap_b32 v77, v79\n"
"buffer_store_dwordx4 v[76:79], %[vst], s[56:59], s54 offen offset:0 sc1\n"
"v_pk_mul_f32 v[52:53], v[172:173], s[64:65]\n"
"v_pk_mul_f32 v[54:55], v[174:175], s[64:65]\n"
"v_exp_f32 v52, v52\n"
"v_exp_f32 v53, v53\n"
"v_exp_f32 v54, v54\n"
"v_exp_f32 v55, v55\n"
"s_nop 0\n"
"v_pk_add_f32 v[52:53], v[52:53], s[66:67]\n"
"v_pk_add_f32 v[54:55], v[54:55], s[66:67]\n"
"v_rcp_f32 v52, v52\n"
"v_rcp_f32 v53, v53\n"
"v_rcp_f32 v54, v54\n"
"v_rcp_f32 v55, v55\n"
"v_pk_mul_f32 v[172:173], v[172:173], v[236:237]\n"
"v_pk_mul_f32 v[174:175], v[174:175], v[238:239]\n"
"v_pk_mul_f32 v[172:173], v[172:173], v[52:53]\n"
"v_pk_mul_f32 v[174:175], v[174:175], v[54:55]\n"
"v_cvt_pk_bf16_f32 v80, v172, v173\n"
"v_cvt_pk_bf16_f32 v81, v174, v175\n"
"v_pk_mul_f32 v[52:53], v[188:189], s[64:65]\n"
"v_pk_mul_f32 v[54:55], v[190:191], s[64:65]\n"
"v_exp_f32 v52, v52\n"
"v_exp_f32 v53, v53\n"
"v_exp_f32 v54, v54\n"
"v_exp_f32 v55, v55\n"
"s_nop 0\n"
"v_pk_add_f32 v[52:53], v[52:53], s[66:67]\n"
"v_pk_add_f32 v[54:55], v[54:55], s[66:67]\n"
"v_rcp_f32 v52, v52\n"
"v_rcp_f32 v53, v53\n"
"v_rcp_f32 v54, v54\n"
"v_rcp_f32 v55, v55\n"
"v_pk_mul_f32 v[188:189], v[188:189], v[252:253]\n"
"v_pk_mul_f32 v[190:191], v[190:191], v[254:255]\n"
"v_pk_mul_f32 v[188:189], v[188:189], v[52:53]\n"
"v_pk_mul_f32 v[190:191], v[190:191], v[54:55]\n"
"v_cvt_pk_bf16_f32 v82, v188, v189\n"
"v_cvt_pk_bf16_f32 v83, v190, v191\n"
"s_nop 0\n"
"v_permlane16_swap_b32 v80, v82\n"
"v_permlane16_swap_b32 v81, v83\n"
"buffer_store_dwordx4 v[80:83], %[vst], s[56:59], s54 offen offset:1024 sc1\n"
"END_%=:\n"
"s_waitcnt vmcnt(0)\n"
  :: [ws] "s"(ws), [kind] "s"(kind), [wave] "s"(wave), [aoff] "s"(aoff), [lda] "s"(lda), [bp] "s"(bp), [bn] "s"(bn), [ldb] "s"(ldb), [nk] "s"(nk), [o0] "s"(o0), [o1] "s"(o1),
     [va0] "v"(va0), [vb0] "v"(vb0), [vx0] "v"(vx0), [vw0] "v"(vw0), [vst] "v"(vst)
  : "v24","v25","v26","v27","v28","v29","v30","v31","v32","v33","v34","v35","v36","v37","v38","v39","v40","v41","v42","v43","v44","v45","v46","v47","v48","v49","v50","v51","v52","v53","v54","v55",
"v56","v57","v58","v59","v60","v61","v62","v63","v64","v65","v66","v67","v68","v69","v70","v71","v72","v73","v74","v75","v76","v77","v78","v79","v80","v81","v82","v83","v84","v85","v86","v87",
"v88","v89","v90","v91","v92","v93","v94","v95","v96","v97","v98","v99","v100","v101","v102","v103","v104","v105","v106","v107","v108","v109","v110","v111","v112","v113","v114","v115","v116","v117","v118","v119",
"v120","v121","v122","v123","v124","v125","v126","v127","v128","v129","v130","v131","v132","v133","v134","v135","v136","v137","v138","v139","v140","v141","v142","v143","v144","v145","v146","v147","v148","v149","v150","v151",
"v152","v153","v154","v155","v156","v157","v158","v159","v160","v161","v162","v163","v164","v165","v166","v167","v168","v169","v170","v171","v172","v173","v174","v175","v176","v177","v178","v179","v180","v181","v182","v183",
"v184","v185","v186","v187","v188","v189","v190","v191","v192","v193","v194","v195","v196","v197","v198","v199","v200","v201","v202","v203","v204","v205","v206","v207","v208","v209","v210","v211","v212","v213","v214","v215",
"v216","v217","v218","v219","v220","v221","v222","v223","v224","v225","v226","v227","v228","v229","v230","v231","v232","v233","v234","v235","v236","v237","v238","v239","v240","v241","v242","v243","v244","v245","v246","v247",
"v248","v249","v250","v251","v252","v253","v254","v255","a0","a1","a2","a3","a4","a5","a6","a7","a8","a9","a10","a11","a12","a13","a14","a15","a16","a17","a18","a19","a20","a21","a22","a23",
"a24","a25","a26","a27","a28","a29","a30","a31","a32","a33","a34","a35","a36","a37","a38","a39","a40","a41","a42","a43","a44","a45","a46","a47","a48","a49","a50","a51","a52","a53","a54","a55",
"a56","a57","a58","a59","a60","a61","a62","a63","a64","a65","a66","a67","a68","a69","a70","a71","a72","a73","a74","a75","a76","a77","a78","a79","a80","a81","a82","a83","a84","a85","a86","a87",
"a88","a89","a90","a91","a92","a93","a94","a95","a96","a97","a98","a99","a100","a101","a102","a103","a104","a105","a106","a107","a108","a109","a110","a111","a112","a113","a114","a115","a116","a117","a118","a119",
"a120","a121","a122","a123","a124","a125","a126","a127","a128","a129","a130","a131","a132","a133","a134","a135","a136","a137","a138","a139","a140","a141","a142","a143","a144","a145","a146","a147","a148","a149","a150","a151",
"a152","a153","a154","a155","a156","a157","a158","a159","a160","a161","a162","a163","a164","a165","a166","a167","a168","a169","a170","a171","a172","a173","a174","a175","a176","a177","a178","a179","a180","a181","a182","a183",
"a184","a185","a186","a187","a188","a189","a190","a191","a192","a193","a194","a195","a196","a197","a198","a199","a200","a201","a202","a203","a204","a205","a206","a207","a208","a209","a210","a211","a212","a213","a214","a215",
"a216","a217","a218","a219","a220","a221","a222","a223","a224","a225","a226","a227","a228","a229","a230","a231","a232","a233","a234","a235","a236","a237","a238","a239","a240","a241","a242","a243","a244","a245","a246","a247",
"a248","a249","a250","a251","a252","a253","a254","a255","s36","s37","s38","s39","s40","s41","s42","s43","s44","s45","s46","s47","s48","s49","s50","s51","s52","s53","s54","s55","s56","s57","s58","s59",
"s60","s61","s62","s63","s64","s65","s66","s67","vcc","scc","memory");
}

__device__ __forceinline__ GP item_qkv_L(const u16* W, int mat, int mt, int nt, int wave) {
  const unsigned wm = wave >> 1, wn = wave & 1, col = nt * 256 + wm * 128;
  const unsigned row = mt * 256 + wm * 128, h0 = (nt * 256) >> 7;
  const unsigned ob = (mat == 0 ? QB : mat == 1 ? KB : VB) + ((row >> 5) * 32 + h0) * 8192;
  const unsigned o0 = ob + (mat == 2 ? wn * 4096 : wn * 2048), o1 = 8192;
  return GP{mat == 2 ? 1u : 0u, XN + mt * 524288, 32768, 8388608 - col * 2, 8192, 16, o0, o1, (unsigned long long)W + col * 2};
}
__device__ __forceinline__ GP item_part_L(const u16* W, int which, int mt, int nt, int k0, int nk, int sp, int wave) {
  const unsigned wm = wave >> 1, wn = wave & 1, col = nt * 256 + wm * 128, lda = which ? 114688 : 131072;
  const unsigned bo = k0 * 2048 + col * 2, row = mt * 256 + wm * 128;
  const unsigned o0 = (which ? PART2 : PART) + sp * 4194304 + row * 2048 + (nt * 256 + wn * 64) * 32;
  return GP{2u, (which ? ACT : OATT) + mt * 16 * lda + k0 * 32, lda, (which ? 7340032 : 8388608) - bo, 2048, (unsigned)nk, o0, 4096, (unsigned long long)W + bo};
}
__device__ __forceinline__ GP item_gu_L(const u16* Wg, const u16* Wu, int mt, int nt, int wave) {
  const unsigned wm = wave >> 1, wn = wave & 1, f0 = nt * 128, row = mt * 256 + wm * 128;
  return GP{3u, HN + mt * 524288, 32768, 7340032 - f0 * 2, 7168, 16, ACT + row * 7168 + (f0 + wn * 64) * 32, 0, (unsigned long long)(wm ? Wu : Wg) + f0 * 2};
}

__device__ __forceinline__ GP item_qkv_S(const u16* W, int mat, int mt, int nt, int wave) {
  const unsigned wm = wave >> 1, wn = wave & 1, col = nt * 128 + wm * 64;
  const unsigned row = mt * 128 + wm * 64, h0 = (nt * 128) >> 7;
  const unsigned ob = (mat == 0 ? QB : mat == 1 ? KB : VB) + ((row >> 5) * 32 + h0) * 8192;
  const unsigned o0 = ob + (mat == 2 ? wn * 2048 : wn * 1024), o1 = mat == 2 ? 4096 : 2048;
  return GP{mat == 2 ? 1u : 0u, XN + mt * 262144, 32768, 8388608 - col * 2, 8192, 16, o0, o1, (unsigned long long)W + col * 2};
}
__device__ __forceinline__ GP item_part_S(const u16* W, int which, int mt, int nt, int k0, int nk, int sp, int wave) {
  const unsigned wm = wave >> 1, wn = wave & 1, col = nt * 128 + wm * 64, lda = which ? 114688 : 131072;
  const unsigned bo = k0 * 2048 + col * 2, row = mt * 128 + wm * 64;
  const unsigned o0 = (which ? PART2 : PART) + sp * 4194304 + row * 2048 + (nt * 128 + wn * 32) * 32;
  return GP{2u, (which ? ACT : OATT) + mt * 8 * lda + k0 * 32, lda, (which ? 7340032 : 8388608) - bo, 2048, (unsigned)nk, o0, 2048, (unsigned long long)W + bo};
}
__device__ __forceinline__ GP item_gu_S(const u16* Wg, const u16* Wu, int mt, int nt, int wave) {
  const unsigned wm = wave >> 1, wn = wave & 1, f0 = nt * 64, row = mt * 128 + wm * 64;
  return GP{3u, HN + mt * 262144, 32768, 7340032 - f0 * 2, 7168, 16, ACT + row * 7168 + (f0 + wn * 32) * 32, 0, (unsigned long long)(wm ? Wu : Wg) + f0 * 2};
}

__device__ __forceinline__ GP item_qkv_X(const u16* W, int mat, int mt, int nt, int wave) {
  const unsigned wm = wave >> 1, wn = wave & 1, col = nt * 128 + wm * 64;
  const unsigned row = mt * 64 + wm * 32, h0 = (nt * 128) >> 7;
  const unsigned ob = (mat == 0 ? QB : mat == 1 ? KB : VB) + ((row >> 5) * 32 + h0) * 8192;
  const unsigned o0 = ob + (mat == 2 ? wn * 2048 : wn * 1024), o1 = mat == 2 ? 4096 : 2048;
  return GP{mat == 2 ? 1u : 0u, XN + mt * 131072, 32768, 8388608 - col * 2, 8192, 16, o0, o1, (unsigned long long)W + col * 2};
}
__device__ __forceinline__ GP item_part_X(const u16* W, int which, int mt, int nt, int k0, int nk, int sp, int wave) {
  const unsigned wm = wave >> 1, wn = wave & 1, col = nt * 128 + wm * 64, lda = which ? 114688 : 131072;
  const unsigned bo = k0 * 2048 + col * 2, row = mt * 64 + wm * 32;
  const unsigned o0 = (which ? PART2 : PART) + sp * 4194304 + row * 2048 + (nt * 128 + wn * 32) * 32;
  return GP{2u, (which ? ACT : OATT) + mt * 4 * lda + k0 * 32, lda, (which ? 7340032 : 8388608) - bo, 2048, (unsigned)nk, o0, 2048, (unsigned long long)W + bo};
}
__device__ __forceinline__ GP item_gu_X(const u16* Wg, const u16* Wu, int mt, int nt, int wave) {
  const unsigned wm = wave >> 1, wn = wave & 1, f0 = nt * 64, row = mt * 64 + wm * 32;
  return GP{3u, HN + mt * 131072, 32768, 7340032 - f0 * 2, 7168, 16, ACT + row * 7168 + (f0 + wn * 32) * 32, 0, (unsigned long long)(wm ? Wu : Wg) + f0 * 2};
}

__device__ __forceinline__ GP item_qkv_M(const u16* W, int mat, int mt, int nt, int wave) {
  const unsigned wm = wave >> 1, wn = wave & 1, col = nt * 256 + wm * 128;
  const unsigned row = mt * 128 + wm * 64, h0 = (nt * 256) >> 7;
  const unsigned ob = (mat == 0 ? QB : mat == 1 ? KB : VB) + ((row >> 5) * 32 + h0) * 8192;
  const unsigned o0 = ob + (mat == 2 ? wn * 4096 : wn * 2048), o1 = 8192;
  return GP{mat == 2 ? 1u : 0u, XN + mt * 262144, 32768, 8388608 - col * 2, 8192, 16, o0, o1, (unsigned long long)W + col * 2};
}
__device__ __forceinline__ GP item_part_M(const u16* W, int which, int mt, int nt, int k0, int nk, int sp, int wave) {
  const unsigned wm = wave >> 1, wn = wave & 1, col = nt * 256 + wm * 128, lda = which ? 114688 : 131072;
  const unsigned bo = k0 * 2048 + col * 2, row = mt * 128 + wm * 64;
  const unsigned o0 = (which ? PART2 : PART) + sp * 4194304 + row * 2048 + (nt * 256 + wn * 64) * 32;
  return GP{2u, (which ? ACT : OATT) + mt * 8 * lda + k0 * 32, lda, (which ? 7340032 : 8388608) - bo, 2048, (unsigned)nk, o0, 4096, (unsigned long long)W + bo};
}
__device__ __forceinline__ GP item_gu_M(const u16* Wg, const u16* Wu, int mt, int nt, int wave) {
  const unsigned wm = wave >> 1, wn = wave & 1, f0 = nt * 128, row = mt * 128 + wm * 64;
  return GP{3u, HN + mt * 262144, 32768, 7340032 - f0 * 2, 7168, 16, ACT + row * 7168 + (f0 + wn * 64) * 32, 0, (unsigned long long)(wm ? Wu : Wg) + f0 * 2};
}

constexpr unsigned OOB = 0x7ff00000u;
__device__ __forceinline__ void qk_role(char* ws, const u16* Wq, const u16* Wk, int h, int wave, int w0, unsigned dbgo) {
  const unsigned l = lane_id(), w = wave;
  const unsigned i16 = l & 15, g = l >> 4, m = i16 & 7, hf = i16 >> 3, q = (l >> 2) & 3, p = l & 3;
  const unsigned vm = (g >> 1) * 512 + (m >> 2) * 256 + (2 * (g & 1) + hf) * 64 + (m & 3) * 16 + w * 5120;
  const unsigned va = 131072 + l * 16, vf = w * 2048 + l * 16;
  const unsigned vqw = 147456 + (32 * w + 16 * (l >> 5) + i16) * 16 + (g & 1) * 8;
  const unsigned vqr = 147456 + (8 * g + q) * 16 + (p & 1) * 8;
  const unsigned vvd = h * 8192 + w * 2048 + l * 16;
  const unsigned vvm = l < 32 ? h * 2048 + w * 512 + l * 8 : OOB;
  const unsigned vo = i16 < 8 ? (m >> 2) * 36864 + (h >> 3) * 9216 + (h & 7) * 1024 + w * 256 + g * 64 + (m & 3) * 16 : OOB;
  const unsigned vkm = i16 < 8 ? h * 2048 + w * 512 + g * 128 + i16 * 16 : OOB;
  const unsigned vidx = i16 < 8 ? 0x4444u : 0xEEEEu;
  const float vci = l < 32 ? 0.f : -1e30f;
  const unsigned vhr = (l % 1) * 128, vhs = (w == 0 && l == 0 && h % 32 == 0) ? h / 32 * 128 : OOB;
  unsigned vs[4];
#pragma unroll
  for (int i = 0; i < 4; ++i) vs[i] = (16 * w + 4 * i + g) * 8192 + h * 256 + ((i16 ^ (2 * (g & 1)) ^ (4 * i)) * 16);
  const unsigned f = (q & 1) | ((g & 1) << 1) | ((q >> 1) << 2), rowp = 16 * (g >> 1) + 8 * (q >> 1) + 4 * (g & 1) + (q & 1);
  const unsigned vt0 = rowp * 256 + (((2 * w) ^ f) * 32) + 8 * p, vt1 = rowp * 256 + (((2 * w + 1) ^ f) * 32) + 8 * p;
  asm volatile(
"s_mov_b64 s[96:97], %[ws]\n"
"s_mov_b32 s36, s96\n"
"s_and_b32 s37, s97, 0xffff\n"
"s_mov_b32 s38, 0xf000000\n"
"s_mov_b32 s39, 0x20000\n"
"s_mov_b64 s[96:97], %[wq]\n"
"s_mov_b32 s40, s96\n"
"s_and_b32 s41, s97, 0xffff\n"
"s_mov_b32 s42, 0x800000\n"
"s_mov_b32 s43, 0x20000\n"
"s_mov_b64 s[96:97], %[wk]\n"
"s_mov_b32 s44, s96\n"
"s_and_b32 s45, s97, 0xffff\n"
"s_mov_b32 s46, 0x800000\n"
"s_mov_b32 s47, 0x20000\n"
"s_mov_b32 s49, %[wave]\n"
"s_mov_b32 s62, %[w0]\n"
"s_mov_b32 s65, 0\n"
"s_mov_b32 s63, 0\n"
"s_mov_b32 s64, 0\n"
"s_lshl_b32 s66, s49, 12\n"
"s_mov_b32 s68, 0\n"
"s_add_u32 m0, s66, 131072\n"
"s_nop 0\n"
"buffer_load_dwordx4 %[vs0], s[40:43], s68 offen lds\n"
"s_add_u32 m0, s66, 132096\n"
"s_nop 0\n"
"buffer_load_dwordx4 %[vs1], s[40:43], s68 offen lds\n"
"s_add_u32 m0, s66, 133120\n"
"s_nop 0\n"
"buffer_load_dwordx4 %[vs2], s[40:43], s68 offen lds\n"
"s_add_u32 m0, s66, 134144\n"
"s_nop 0\n"
"buffer_load_dwordx4 %[vs3], s[40:43], s68 offen lds\n"
"s_add_u32 m0, s66, 147456\n"
"s_nop 0\n"
"buffer_load_dwordx4 %[vs0], s[44:47], s68 offen lds\n"
"s_add_u32 m0, s66, 148480\n"
"s_nop 0\n"
"buffer_load_dwordx4 %[vs1], s[44:47], s68 offen lds\n"
"s_add_u32 m0, s66, 149504\n"
"s_nop 0\n"
"buffer_load_dwordx4 %[vs2], s[44:47], s68 offen lds\n"
"s_add_u32 m0, s66, 150528\n"
"s_nop 0\n"
"buffer_load_dwordx4 %[vs3], s[44:47], s68 offen lds\n"
"s_mov_b32 s68, 2097152\n"
"s_add_u32 m0, s66, 98304\n"
"s_nop 0\n"
"buffer_load_dwordx4 %[vs0], s[40:43], s68 offen lds\n"
"s_add_u32 m0, s66, 99328\n"
"s_nop 0\n"
"buffer_load_dwordx4 %[vs1], s[40:43], s68 offen lds\n"
"s_add_u32 m0, s66, 100352\n"
"s_nop 0\n"
"buffer_load_dwordx4 %[vs2], s[40:43], s68 offen lds\n"
"s_add_u32 m0, s66, 101376\n"
"s_nop 0\n"
"buffer_load_dwordx4 %[vs3], s[40:43], s68 offen lds\n"
"s_add_u32 m0, s66, 114688\n"
"s_nop 0\n"
"buffer_load_dwordx4 %[vs0], s[44:47], s68 offen lds\n"
"s_add_u32 m0, s66, 115712\n"
"s_nop 0\n"
"buffer_load_dwordx4 %[vs1], s[44:47], s68 offen lds\n"
"s_add_u32 m0, s66, 116736\n"
"s_nop 0\n"
"buffer_load_dwordx4 %[vs2], s[44:47], s68 offen lds\n"
"s_add_u32 m0, s66, 117760\n"
"s_nop 0\n"
"buffer_load_dwordx4 %[vs3], s[44:47], s68 offen lds\n"
"s_mov_b32 s68, 4194304\n"
"s_add_u32 m0, s66, 65536\n"
"s_nop 0\n"
"buffer_load_dwordx4 %[vs0], s[40:43], s68 offen lds\n"
"s_add_u32 m0, s66, 66560\n"
"s_nop 0\n"
"buffer_load_dwordx4 %[vs1], s[40:43], s68 offen lds\n"
"s_add_u32 m0, s66, 67584\n"
"s_nop 0\n"
"buffer_load_dwordx4 %[vs2], s[40:43], s68 offen lds\n"
"s_add_u32 m0, s66, 68608\n"
"s_nop 0\n"
"buffer_load_dwordx4 %[vs3], s[40:43], s68 offen lds\n"
"s_add_u32 m0, s66, 81920\n"
"s_nop 0\n"
"buffer_load_dwordx4 %[vs0], s[44:47], s68 offen lds\n"
"s_add_u32 m0, s66, 82944\n"
"s_nop 0\n"
"buffer_load_dwordx4 %[vs1], s[44:47], s68 offen lds\n"
"s_add_u32 m0, s66, 83968\n"
"s_nop 0\n"
"buffer_load_dwordx4 %[vs2], s[44:47], s68 offen lds\n"
"s_add_u32 m0, s66, 84992\n"
"s_nop 0\n"
"buffer_load_dwordx4 %[vs3], s[44:47], s68 offen lds\n"
"s_waitcnt vmcnt(16)\n"
"s_barrier\n"
"v_add_u32 v96, 131072, %[vt0]\n"
"v_add_u32 v97, 131072, %[vt1]\n"
"ds_read_b64_tr_b16 a[0:1], v96 offset:0\n"
"ds_read_b64_tr_b16 a[2:3], v96 offset:512\n"
"ds_read_b64_tr_b16 a[4:5], v96 offset:8192\n"
"ds_read_b64_tr_b16 a[6:7], v96 offset:8704\n"
"ds_read_b64_tr_b16 a[8:9], v97 offset:0\n"
"ds_read_b64_tr_b16 a[10:11], v97 offset:512\n"
"ds_read_b64_tr_b16 a[12:13], v97 offset:8192\n"
"ds_read_b64_tr_b16 a[14:15], v97 offset:8704\n"
"ds_read_b64_tr_b16 a[16:17], v96 offset:16384\n"
"ds_read_b64_tr_b16 a[18:19], v96 offset:16896\n"
"ds_read_b64_tr_b16 a[20:21], v96 offset:24576\n"
"ds_read_b64_tr_b16 a[22:23], v96 offset:25088\n"
"ds_read_b64_tr_b16 v[88:89], v97 offset:16384\n"
"ds_read_b64_tr_b16 v[90:91], v97 offset:16896\n"
"ds_read_b64_tr_b16 v[92:93], v97 offset:24576\n"
"ds_read_b64_tr_b16 v[94:95], v97 offset:25088\n"
"s_waitcnt lgkmcnt(0)\n"
"ds_write_b128 %[vf], v[88:91] offset:0\n"
"ds_write_b128 %[vf], v[92:95] offset:1024\n"
"s_waitcnt lgkmcnt(0)\n"
"s_barrier\n"
"s_mov_b32 s68, 6291456\n"
"s_add_u32 m0, s66, 131072\n"
"s_nop 0\n"
"buffer_load_dwordx4 %[vs0], s[40:43], s68 offen lds\n"
"s_add_u32 m0, s66, 132096\n"
"s_nop 0\n"
"buffer_load_dwordx4 %[vs1], s[40:43], s68 offen lds\n"
"s_add_u32 m0, s66, 133120\n"
"s_nop 0\n"
"buffer_load_dwordx4 %[vs2], s[40:43], s68 offen lds\n"
"s_add_u32 m0, s66, 134144\n"
"s_nop 0\n"
"buffer_load_dwordx4 %[vs3], s[40:43], s68 offen lds\n"
"s_add_u32 m0, s66, 147456\n"
"s_nop 0\n"
"buffer_load_dwordx4 %[vs0], s[44:47], s68 offen lds\n"
"s_add_u32 m0, s66, 148480\n"
"s_nop 0\n"
"buffer_load_dwordx4 %[vs1], s[44:47], s68 offen lds\n"
"s_add_u32 m0, s66, 149504\n"
"s_nop 0\n"
"buffer_load_dwordx4 %[vs2], s[44:47], s68 offen lds\n"
"s_add_u32 m0, s66, 150528\n"
"s_nop 0\n"
"buffer_load_dwordx4 %[vs3], s[44:47], s68 offen lds\n"
"s_waitcnt vmcnt(16)\n"
"s_barrier\n"
"v_add_u32 v96, 98304, %[vt0]\n"
"v_add_u32 v97, 98304, %[vt1]\n"
"ds_read_b64_tr_b16 a[24:25], v96 offset:0\n"
"ds_read_b64_tr_b16 a[26:27], v96 offset:512\n"
"ds_read_b64_tr_b16 a[28:29], v96 offset:8192\n"
"ds_read_b64_tr_b16 a[30:31], v96 offset:8704\n"
"ds_read_b64_tr_b16 a[32:33], v97 offset:0\n"
"ds_read_b64_tr_b16 a[34:35], v97 offset:512\n"
"ds_read_b64_tr_b16 a[36:37], v97 offset:8192\n"
"ds_read_b64_tr_b16 a[38:39], v97 offset:8704\n"
"ds_read_b64_tr_b16 a[40:41], v96 offset:16384\n"
"ds_read_b64_tr_b16 a[42:43], v96 offset:16896\n"
"ds_read_b64_tr_b16 a[44:45], v96 offset:24576\n"
"ds_read_b64_tr_b16 a[46:47], v96 offset:25088\n"
"ds_read_b64_tr_b16 v[88:89], v97 offset:16384\n"
"ds_read_b64_tr_b16 v[90:91], v97 offset:16896\n"
"ds_read_b64_tr_b16 v[92:93], v97 offset:24576\n"
"ds_read_b64_tr_b16 v[94:95], v97 offset:25088\n"
"s_waitcnt lgkmcnt(0)\n"
"ds_write_b128 %[vf], v[88:91] offset:8192\n"
"ds_write_b128 %[vf], v[92:95] offset:9216\n"
"s_waitcnt lgkmcnt(0)\n"
"s_barrier\n"
"s_mov_b32 s68, 524288\n"
"s_add_u32 m0, s66, 98304\n"
"s_nop 0\n"
"buffer_load_dwordx4 %[vs0], s[40:43], s68 offen lds\n"
"s_add_u32 m0, s66, 99328\n"
"s_nop 0\n"
"buffer_load_dwordx4 %[vs1], s[40:43], s68 offen lds\n"
"s_add_u32 m0, s66, 100352\n"
"s_nop 0\n"
"buffer_load_dwordx4 %[vs2], s[40:43], s68 offen lds\n"
"s_add_u32 m0, s66, 101376\n"
"s_nop 0\n"
"buffer_load_dwordx4 %[vs3], s[40:43], s68 offen lds\n"
"s_add_u32 m0, s66, 114688\n"
"s_nop 0\n"
"buffer_load_dwordx4 %[vs0], s[44:47], s68 offen lds\n"
"s_add_u32 m0, s66, 115712\n"
"s_nop 0\n"
"buffer_load_dwordx4 %[vs1], s[44:47], s68 offen lds\n"
"s_add_u32 m0, s66, 116736\n"
"s_nop 0\n"
"buffer_load_dwordx4 %[vs2], s[44:47], s68 offen lds\n"
"s_add_u32 m0, s66, 117760\n"
"s_nop 0\n"
"buffer_load_dwordx4 %[vs3], s[44:47], s68 offen lds\n"
"s_waitcnt vmcnt(16)\n"
"s_barrier\n"
"v_add_u32 v96, 65536, %[vt0]\n"
"v_add_u32 v97, 65536, %[vt1]\n"
"ds_read_b64_tr_b16 a[48:49], v96 offset:0\n"
"ds_read_b64_tr_b16 a[50:51], v96 offset:512\n"
"ds_read_b64_tr_b16 a[52:53], v96 offset:8192\n"
"ds_read_b64_tr_b16 a[54:55], v96 offset:8704\n"
"ds_read_b64_tr_b16 a[56:57], v97 offset:0\n"
"ds_read_b64_tr_b16 a[58:59], v97 offset:512\n"
"ds_read_b64_tr_b16 a[60:61], v97 offset:8192\n"
"ds_read_b64_tr_b16 a[62:63], v97 offset:8704\n"
"ds_read_b64_tr_b16 a[64:65], v96 offset:16384\n"
"ds_read_b64_tr_b16 a[66:67], v96 offset:16896\n"
"ds_read_b64_tr_b16 a[68:69], v96 offset:24576\n"
"ds_read_b64_tr_b16 a[70:71], v96 offset:25088\n"
"ds_read_b64_tr_b16 v[88:89], v97 offset:16384\n"
"ds_read_b64_tr_b16 v[90:91], v97 offset:16896\n"
"ds_read_b64_tr_b16 v[92:93], v97 offset:24576\n"
"ds_read_b64_tr_b16 v[94:95], v97 offset:25088\n"
"s_waitcnt lgkmcnt(0)\n"
"ds_write_b128 %[vf], v[88:91] offset:16384\n"
"ds_write_b128 %[vf], v[92:95] offset:17408\n"
"s_waitcnt lgkmcnt(0)\n"
"s_barrier\n"
"s_mov_b32 s68, 2621440\n"
"s_add_u32 m0, s66, 65536\n"
"s_nop 0\n"
"buffer_load_dwordx4 %[vs0], s[40:43], s68 offen lds\n"
"s_add_u32 m0, s66, 66560\n"
"s_nop 0\n"
"buffer_load_dwordx4 %[vs1], s[40:43], s68 offen lds\n"
"s_add_u32 m0, s66, 67584\n"
"s_nop 0\n"
"buffer_load_dwordx4 %[vs2], s[40:43], s68 offen lds\n"
"s_add_u32 m0, s66, 68608\n"
"s_nop 0\n"
"buffer_load_dwordx4 %[vs3], s[40:43], s68 offen lds\n"
"s_add_u32 m0, s66, 81920\n"
"s_nop 0\n"
"buffer_load_dwordx4 %[vs0], s[44:47], s68 offen lds\n"
"s_add_u32 m0, s66, 82944\n"
"s_nop 0\n"
"buffer_load_dwordx4 %[vs1], s[44:47], s68 offen lds\n"
"s_add_u32 m0, s66, 83968\n"
"s_nop 0\n"
"buffer_load_dwordx4 %[vs2], s[44:47], s68 offen lds\n"
"s_add_u32 m0, s66, 84992\n"
"s_nop 0\n"
"buffer_load_dwordx4 %[vs3], s[44:47], s68 offen lds\n"
"s_waitcnt vmcnt(16)\n"
"s_barrier\n"
"v_add_u32 v96, 131072, %[vt0]\n"
"v_add_u32 v97, 131072, %[vt1]\n"
"ds_read_b64_tr_b16 a[72:73], v96 offset:0\n"
"ds_read_b64_tr_b16 a[74:75], v96 offset:512\n"
"ds_read_b64_tr_b16 a[76:77], v96 offset:8192\n"
"ds_read_b64_tr_b16 a[78:79], v96 offset:8704\n"
"ds_read_b64_tr_b16 a[80:81], v97 offset:0\n"
"ds_read_b64_tr_b16 a[82:83], v97 offset:512\n"
"ds_read_b64_tr_b16 a[84:85], v97 offset:8192\n"
"ds_read_b64_tr_b16 a[86:87], v97 offset:8704\n"
"ds_read_b64_tr_b16 a[88:89], v96 offset:16384\n"
"ds_read_b64_tr_b16 a[90:91], v96 offset:16896\n"
"ds_read_b64_tr_b16 a[92:93], v96 offset:24576\n"
"ds_read_b64_tr_b16 a[94:95], v96 offset:25088\n"
"ds_read_b64_tr_b16 v[88:89], v97 offset:16384\n"
"ds_read_b64_tr_b16 v[90:91], v97 offset:16896\n"
"ds_read_b64_tr_b16 v[92:93], v97 offset:24576\n"
"ds_read_b64_tr_b16 v[94:95], v97 offset:25088\n"
"s_waitcnt lgkmcnt(0)\n"
"ds_write_b128 %[vf], v[88:91] offset:24576\n"
"ds_write_b128 %[vf], v[92:95] offset:25600\n"
"s_waitcnt lgkmcnt(0)\n"
"s_barrier\n"
"s_mov_b32 s68, 4718592\n"
"s_add_u32 m0, s66, 131072\n"
"s_nop 0\n"
"buffer_load_dwordx4 %[vs0], s[40:43], s68 offen lds\n"
"s_add_u32 m0, s66, 132096\n"
"s_nop 0\n"
"buffer_load_dwordx4 %[vs1], s[40:43], s68 offen lds\n"
"s_add_u32 m0, s66, 133120\n"
"s_nop 0\n"
"buffer_load_dwordx4 %[vs2], s[40:43], s68 offen lds\n"
"s_add_u32 m0, s66, 134144\n"
"s_nop 0\n"
"buffer_load_dwordx4 %[vs3], s[40:43], s68 offen lds\n"
"s_add_u32 m0, s66, 147456\n"
"s_nop 0\n"
"buffer_load_dwordx4 %[vs0], s[44:47], s68 offen lds\n"
"s_add_u32 m0, s66, 148480\n"
"s_nop 0\n"
"buffer_load_dwordx4 %[vs1], s[44:47], s68 offen lds\n"
"s_add_u32 m0, s66, 149504\n"
"s_nop 0\n"
"buffer_load_dwordx4 %[vs2], s[44:47], s68 offen lds\n"
"s_add_u32 m0, s66, 150528\n"
"s_nop 0\n"
"buffer_load_dwordx4 %[vs3], s[44:47], s68 offen lds\n"
"s_waitcnt vmcnt(16)\n"
"s_barrier\n"
"v_add_u32 v96, 98304, %[vt0]\n"
"v_add_u32 v97, 98304, %[vt1]\n"
"ds_read_b64_tr_b16 a[96:97], v96 offset:0\n"
"ds_read_b64_tr_b16 a[98:99], v96 offset:512\n"
"ds_read_b64_tr_b16 a[100:101], v96 offset:8192\n"
"ds_read_b64_tr_b16 a[102:103], v96 offset:8704\n"
"ds_read_b64_tr_b16 a[104:105], v97 offset:0\n"
"ds_read_b64_tr_b16 a[106:107], v97 offset:512\n"
"ds_read_b64_tr_b16 a[108:109], v97 offset:8192\n"
"ds_read_b64_tr_b16 a[110:111], v97 offset:8704\n"
"ds_read_b64_tr_b16 a[112:113], v96 offset:16384\n"
"ds_read_b64_tr_b16 a[114:115], v96 offset:16896\n"
"ds_read_b64_tr_b16 a[116:117], v96 offset:24576\n"
"ds_read_b64_tr_b16 a[118:119], v96 offset:25088\n"
"ds_read_b64_tr_b16 v[88:89], v97 offset:16384\n"
"ds_read_b64_tr_b16 v[90:91], v97 offset:16896\n"
"ds_read_b64_tr_b16 v[92:93], v97 offset:24576\n"
"ds_read_b64_tr_b16 v[94:95], v97 offset:25088\n"
"s_waitcnt lgkmcnt(0)\n"
"ds_write_b128 %[vf], v[88:91] offset:32768\n"
"ds_write_b128 %[vf], v[92:95] offset:33792\n"
"s_waitcnt lgkmcnt(0)\n"
"s_barrier\n"
"s_mov_b32 s68, 6815744\n"
"s_add_u32 m0, s66, 98304\n"
"s_nop 0\n"
"buffer_load_dwordx4 %[vs0], s[40:43], s68 offen lds\n"
"s_add_u32 m0, s66, 99328\n"
"s_nop 0\n"
"buffer_load_dwordx4 %[vs1], s[40:43], s68 offen lds\n"
"s_add_u32 m0, s66, 100352\n"
"s_nop 0\n"
"buffer_load_dwordx4 %[vs2], s[40:43], s68 offen lds\n"
"s_add_u32 m0, s66, 101376\n"
"s_nop 0\n"
"buffer_load_dwordx4 %[vs3], s[40:43], s68 offen lds\n"
"s_add_u32 m0, s66, 114688\n"
"s_nop 0\n"
"buffer_load_dwordx4 %[vs0], s[44:47], s68 offen lds\n"
"s_add_u32 m0, s66, 115712\n"
"s_nop 0\n"
"buffer_load_dwordx4 %[vs1], s[44:47], s68 offen lds\n"
"s_add_u32 m0, s66, 116736\n"
"s_nop 0\n"
"buffer_load_dwordx4 %[vs2], s[44:47], s68 offen lds\n"
"s_add_u32 m0, s66, 117760\n"
"s_nop 0\n"
"buffer_load_dwordx4 %[vs3], s[44:47], s68 offen lds\n"
"s_waitcnt vmcnt(16)\n"
"s_barrier\n"
"v_add_u32 v96, 65536, %[vt0]\n"
"v_add_u32 v97, 65536, %[vt1]\n"
"ds_read_b64_tr_b16 a[120:121], v96 offset:0\n"
"ds_read_b64_tr_b16 a[122:123], v96 offset:512\n"
"ds_read_b64_tr_b16 a[124:125], v96 offset:8192\n"
"ds_read_b64_tr_b16 a[126:127], v96 offset:8704\n"
"ds_read_b64_tr_b16 a[128:129], v97 offset:0\n"
"ds_read_b64_tr_b16 a[130:131], v97 offset:512\n"
"ds_read_b64_tr_b16 a[132:133], v97 offset:8192\n"
"ds_read_b64_tr_b16 a[134:135], v97 offset:8704\n"
"ds_read_b64_tr_b16 a[136:137], v96 offset:16384\n"
"ds_read_b64_tr_b16 a[138:139], v96 offset:16896\n"
"ds_read_b64_tr_b16 a[140:141], v96 offset:24576\n"
"ds_read_b64_tr_b16 a[142:143], v96 offset:25088\n"
"ds_read_b64_tr_b16 v[88:89], v97 offset:16384\n"
"ds_read_b64_tr_b16 v[90:91], v97 offset:16896\n"
"ds_read_b64_tr_b16 v[92:93], v97 offset:24576\n"
"ds_read_b64_tr_b16 v[94:95], v97 offset:25088\n"
"s_waitcnt lgkmcnt(0)\n"
"ds_write_b128 %[vf], v[88:91] offset:40960\n"
"ds_write_b128 %[vf], v[92:95] offset:41984\n"
"s_waitcnt lgkmcnt(0)\n"
"s_barrier\n"
"s_mov_b32 s68, 1048576\n"
"s_add_u32 m0, s66, 65536\n"
"s_nop 0\n"
"buffer_load_dwordx4 %[vs0], s[40:43], s68 offen lds\n"
"s_add_u32 m0, s66, 66560\n"
"s_nop 0\n"
"buffer_load_dwordx4 %[vs1], s[40:43], s68 offen lds\n"
"s_add_u32 m0, s66, 67584\n"
"s_nop 0\n"
"buffer_load_dwordx4 %[vs2], s[40:43], s68 offen lds\n"
"s_add_u32 m0, s66, 68608\n"
"s_nop 0\n"
"buffer_load_dwordx4 %[vs3], s[40:43], s68 offen lds\n"
"s_add_u32 m0, s66, 81920\n"
"s_nop 0\n"
"buffer_load_dwordx4 %[vs0], s[44:47], s68 offen lds\n"
"s_add_u32 m0, s66, 82944\n"
"s_nop 0\n"
"buffer_load_dwordx4 %[vs1], s[44:47], s68 offen lds\n"
"s_add_u32 m0, s66, 83968\n"
"s_nop 0\n"
"buffer_load_dwordx4 %[vs2], s[44:47], s68 offen lds\n"
"s_add_u32 m0, s66, 84992\n"
"s_nop 0\n"
"buffer_load_dwordx4 %[vs3], s[44:47], s68 offen lds\n"
"s_waitcnt vmcnt(16)\n"
"s_barrier\n"
"v_add_u32 v96, 131072, %[vt0]\n"
"v_add_u32 v97, 131072, %[vt1]\n"
"ds_read_b64_tr_b16 a[144:145], v96 offset:0\n"
"ds_read_b64_tr_b16 a[146:147], v96 offset:512\n"
"ds_read_b64_tr_b16 a[148:149], v96 offset:8192\n"
"ds_read_b64_tr_b16 a[150:151], v96 offset:8704\n"
"ds_read_b64_tr_b16 a[152:153], v97 offset:0\n"
"ds_read_b64_tr_b16 a[154:155], v97 offset:512\n"
"ds_read_b64_tr_b16 a[156:157], v97 offset:8192\n"
"ds_read_b64_tr_b16 a[158:159], v97 offset:8704\n"
"ds_read_b64_tr_b16 a[160:161], v96 offset:16384\n"
"ds_read_b64_tr_b16 a[162:163], v96 offset:16896\n"
"ds_read_b64_tr_b16 a[164:165], v96 offset:24576\n"
"ds_read_b64_tr_b16 a[166:167], v96 offset:25088\n"
"ds_read_b64_tr_b16 v[88:89], v97 offset:16384\n"
"ds_read_b64_tr_b16 v[90:91], v97 offset:16896\n"
"ds_read_b64_tr_b16 v[92:93], v97 offset:24576\n"
"ds_read_b64_tr_b16 v[94:95], v97 offset:25088\n"
"s_waitcnt lgkmcnt(0)\n"
"ds_write_b128 %[vf], v[88:91] offset:49152\n"
"ds_write_b128 %[vf], v[92:95] offset:50176\n"
"s_waitcnt lgkmcnt(0)\n"
"s_barrier\n"
"s_mov_b32 s68, 3145728\n"
"s_add_u32 m0, s66, 131072\n"
"s_nop 0\n"
"buffer_load_dwordx4 %[vs0], s[40:43], s68 offen lds\n"
"s_add_u32 m0, s66, 132096\n"
"s_nop 0\n"
"buffer_load_dwordx4 %[vs1], s[40:43], s68 offen lds\n"
"s_add_u32 m0, s66, 133120\n"
"s_nop 0\n"
"buffer_load_dwordx4 %[vs2], s[40:43], s68 offen lds\n"
"s_add_u32 m0, s66, 134144\n"
"s_nop 0\n"
"buffer_load_dwordx4 %[vs3], s[40:43], s68 offen lds\n"
"s_add_u32 m0, s66, 147456\n"
"s_nop 0\n"
"buffer_load_dwordx4 %[vs0], s[44:47], s68 offen lds\n"
"s_add_u32 m0, s66, 148480\n"
"s_nop 0\n"
"buffer_load_dwordx4 %[vs1], s[44:47], s68 offen lds\n"
"s_add_u32 m0, s66, 149504\n"
"s_nop 0\n"
"buffer_load_dwordx4 %[vs2], s[44:47], s68 offen lds\n"
"s_add_u32 m0, s66, 150528\n"
"s_nop 0\n"
"buffer_load_dwordx4 %[vs3], s[44:47], s68 offen lds\n"
"s_waitcnt vmcnt(16)\n"
"s_barrier\n"
"v_add_u32 v96, 98304, %[vt0]\n"
"v_add_u32 v97, 98304, %[vt1]\n"
"ds_read_b64_tr_b16 a[168:169], v96 offset:0\n"
"ds_read_b64_tr_b16 a[170:171], v96 offset:512\n"
"ds_read_b64_tr_b16 a[172:173], v96 offset:8192\n"
"ds_read_b64_tr_b16 a[174:175], v96 offset:8704\n"
"ds_read_b64_tr_b16 a[176:177], v97 offset:0\n"
"ds_read_b64_tr_b16 a[178:179], v97 offset:512\n"
"ds_read_b64_tr_b16 a[180:181], v97 offset:8192\n"
"ds_read_b64_tr_b16 a[182:183], v97 offset:8704\n"
"ds_read_b64_tr_b16 a[184:185], v96 offset:16384\n"
"ds_read_b64_tr_b16 a[186:187], v96 offset:16896\n"
"ds_read_b64_tr_b16 a[188:189], v96 offset:24576\n"
"ds_read_b64_tr_b16 a[190:191], v96 offset:25088\n"
"ds_read_b64_tr_b16 v[88:89], v97 offset:16384\n"
"ds_read_b64_tr_b16 v[90:91], v97 offset:16896\n"
"ds_read_b64_tr_b16 v[92:93], v97 offset:24576\n"
"ds_read_b64_tr_b16 v[94:95], v97 offset:25088\n"
"s_waitcnt lgkmcnt(0)\n"
"ds_write_b128 %[vf], v[88:91] offset:57344\n"
"ds_write_b128 %[vf], v[92:95] offset:58368\n"
"s_waitcnt lgkmcnt(0)\n"
"s_barrier\n"
"s_mov_b32 s68, 5242880\n"
"s_add_u32 m0, s66, 98304\n"
"s_nop 0\n"
"buffer_load_dwordx4 %[vs0], s[40:43], s68 offen lds\n"
"s_add_u32 m0, s66, 99328\n"
"s_nop 0\n"
"buffer_load_dwordx4 %[vs1], s[40:43], s68 offen lds\n"
"s_add_u32 m0, s66, 100352\n"
"s_nop 0\n"
"buffer_load_dwordx4 %[vs2], s[40:43], s68 offen lds\n"
"s_add_u32 m0, s66, 101376\n"
"s_nop 0\n"
"buffer_load_dwordx4 %[vs3], s[40:43], s68 offen lds\n"
"s_add_u32 m0, s66, 114688\n"
"s_nop 0\n"
"buffer_load_dwordx4 %[vs0], s[44:47], s68 offen lds\n"
"s_add_u32 m0, s66, 115712\n"
"s_nop 0\n"
"buffer_load_dwordx4 %[vs1], s[44:47], s68 offen lds\n"
"s_add_u32 m0, s66, 116736\n"
"s_nop 0\n"
"buffer_load_dwordx4 %[vs2], s[44:47], s68 offen lds\n"
"s_add_u32 m0, s66, 117760\n"
"s_nop 0\n"
"buffer_load_dwordx4 %[vs3], s[44:47], s68 offen lds\n"
"s_waitcnt vmcnt(16)\n"
"s_barrier\n"
"v_add_u32 v96, 65536, %[vt0]\n"
"v_add_u32 v97, 65536, %[vt1]\n"
"ds_read_b64_tr_b16 a[192:193], v96 offset:0\n"
"ds_read_b64_tr_b16 a[194:195], v96 offset:512\n"
"ds_read_b64_tr_b16 a[196:197], v96 offset:8192\n"
"ds_read_b64_tr_b16 a[198:199], v96 offset:8704\n"
"ds_read_b64_tr_b16 a[200:201], v97 offset:0\n"
"ds_read_b64_tr_b16 a[202:203], v97 offset:512\n"
"ds_read_b64_tr_b16 a[204:205], v97 offset:8192\n"
"ds_read_b64_tr_b16 a[206:207], v97 offset:8704\n"
"ds_read_b64_tr_b16 a[208:209], v96 offset:16384\n"
"ds_read_b64_tr_b16 a[210:211], v96 offset:16896\n"
"ds_read_b64_tr_b16 a[212:213], v96 offset:24576\n"
"ds_read_b64_tr_b16 a[214:215], v96 offset:25088\n"
"ds_read_b64_tr_b16 v[24:25], v97 offset:16384\n"
"ds_read_b64_tr_b16 v[26:27], v97 offset:16896\n"
"ds_read_b64_tr_b16 v[28:29], v97 offset:24576\n"
"ds_read_b64_tr_b16 v[30:31], v97 offset:25088\n"
"s_waitcnt lgkmcnt(0)\n"
"s_barrier\n"
"s_mov_b32 s68, 7340032\n"
"s_add_u32 m0, s66, 65536\n"
"s_nop 0\n"
"buffer_load_dwordx4 %[vs0], s[40:43], s68 offen lds\n"
"s_add_u32 m0, s66, 66560\n"
"s_nop 0\n"
"buffer_load_dwordx4 %[vs1], s[40:43], s68 offen lds\n"
"s_add_u32 m0, s66, 67584\n"
"s_nop 0\n"
"buffer_load_dwordx4 %[vs2], s[40:43], s68 offen lds\n"
"s_add_u32 m0, s66, 68608\n"
"s_nop 0\n"
"buffer_load_dwordx4 %[vs3], s[40:43], s68 offen lds\n"
"s_add_u32 m0, s66, 81920\n"
"s_nop 0\n"
"buffer_load_dwordx4 %[vs0], s[44:47], s68 offen lds\n"
"s_add_u32 m0, s66, 82944\n"
"s_nop 0\n"
"buffer_load_dwordx4 %[vs1], s[44:47], s68 offen lds\n"
"s_add_u32 m0, s66, 83968\n"
"s_nop 0\n"
"buffer_load_dwordx4 %[vs2], s[44:47], s68 offen lds\n"
"s_add_u32 m0, s66, 84992\n"
"s_nop 0\n"
"buffer_load_dwordx4 %[vs3], s[44:47], s68 offen lds\n"
"s_waitcnt vmcnt(16)\n"
"s_barrier\n"
"v_add_u32 v96, 131072, %[vt0]\n"
"v_add_u32 v97, 131072, %[vt1]\n"
"ds_read_b64_tr_b16 a[216:217], v96 offset:0\n"
"ds_read_b64_tr_b16 a[218:219], v96 offset:512\n"
"ds_read_b64_tr_b16 a[220:221], v96 offset:8192\n"
"ds_read_b64_tr_b16 a[222:223], v96 offset:8704\n"
"ds_read_b64_tr_b16 a[224:225], v97 offset:0\n"
"ds_read_b64_tr_b16 a[226:227], v97 offset:512\n"
"ds_read_b64_tr_b16 a[228:229], v97 offset:8192\n"
"ds_read_b64_tr_b16 a[230:231], v97 offset:8704\n"
"ds_read_b64_tr_b16 a[232:233], v96 offset:16384\n"
"ds_read_b64_tr_b16 a[234:235], v96 offset:16896\n"
"ds_read_b64_tr_b16 a[236:237], v96 offset:24576\n"
"ds_read_b64_tr_b16 a[238:239], v96 offset:25088\n"
"ds_read_b64_tr_b16 v[32:33], v97 offset:16384\n"
"ds_read_b64_tr_b16 v[34:35], v97 offset:16896\n"
"ds_read_b64_tr_b16 v[36:37], v97 offset:24576\n"
"ds_read_b64_tr_b16 v[38:39], v97 offset:25088\n"
"s_waitcnt lgkmcnt(0)\n"
"s_barrier\n"
"s_mov_b32 s68, 1572864\n"
"s_add_u32 m0, s66, 131072\n"
"s_nop 0\n"
"buffer_load_dwordx4 %[vs0], s[40:43], s68 offen lds\n"
"s_add_u32 m0, s66, 132096\n"
"s_nop 0\n"
"buffer_load_dwordx4 %[vs1], s[40:43], s68 offen lds\n"
"s_add_u32 m0, s66, 133120\n"
"s_nop 0\n"
"buffer_load_dwordx4 %[vs2], s[40:43], s68 offen lds\n"
"s_add_u32 m0, s66, 134144\n"
"s_nop 0\n"
"buffer_load_dwordx4 %[vs3], s[40:43], s68 offen lds\n"
"s_add_u32 m0, s66, 147456\n"
"s_nop 0\n"
"buffer_load_dwordx4 %[vs0], s[44:47], s68 offen lds\n"
"s_add_u32 m0, s66, 148480\n"
"s_nop 0\n"
"buffer_load_dwordx4 %[vs1], s[44:47], s68 offen lds\n"
"s_add_u32 m0, s66, 149504\n"
"s_nop 0\n"
"buffer_load_dwordx4 %[vs2], s[44:47], s68 offen lds\n"
"s_add_u32 m0, s66, 150528\n"
"s_nop 0\n"
"buffer_load_dwordx4 %[vs3], s[44:47], s68 offen lds\n"
"s_waitcnt vmcnt(16)\n"
"s_barrier\n"
"v_add_u32 v96, 98304, %[vt0]\n"
"v_add_u32 v97, 98304, %[vt1]\n"
"ds_read_b64_tr_b16 a[240:241], v96 offset:0\n"
"ds_read_b64_tr_b16 a[242:243], v96 offset:512\n"
"ds_read_b64_tr_b16 a[244:245], v96 offset:8192\n"
"ds_read_b64_tr_b16 a[246:247], v96 offset:8704\n"
"ds_read_b64_tr_b16 a[248:249], v97 offset:0\n"
"ds_read_b64_tr_b16 a[250:251], v97 offset:512\n"
"ds_read_b64_tr_b16 a[252:253], v97 offset:8192\n"
"ds_read_b64_tr_b16 a[254:255], v97 offset:8704\n"
"ds_read_b64_tr_b16 v[128:129], v96 offset:16384\n"
"ds_read_b64_tr_b16 v[130:131], v96 offset:16896\n"
"ds_read_b64_tr_b16 v[132:133], v96 offset:24576\n"
"ds_read_b64_tr_b16 v[134:135], v96 offset:25088\n"
"ds_read_b64_tr_b16 v[40:41], v97 offset:16384\n"
"ds_read_b64_tr_b16 v[42:43], v97 offset:16896\n"
"ds_read_b64_tr_b16 v[44:45], v97 offset:24576\n"
"ds_read_b64_tr_b16 v[46:47], v97 offset:25088\n"
"s_waitcnt lgkmcnt(0)\n"
"s_barrier\n"
"s_mov_b32 s68, 3670016\n"
"s_add_u32 m0, s66, 98304\n"
"s_nop 0\n"
"buffer_load_dwordx4 %[vs0], s[40:43], s68 offen lds\n"
"s_add_u32 m0, s66, 99328\n"
"s_nop 0\n"
"buffer_load_dwordx4 %[vs1], s[40:43], s68 offen lds\n"
"s_add_u32 m0, s66, 100352\n"
"s_nop 0\n"
"buffer_load_dwordx4 %[vs2], s[40:43], s68 offen lds\n"
"s_add_u32 m0, s66, 101376\n"
"s_nop 0\n"
"buffer_load_dwordx4 %[vs3], s[40:43], s68 offen lds\n"
"s_add_u32 m0, s66, 114688\n"
"s_nop 0\n"
"buffer_load_dwordx4 %[vs0], s[44:47], s68 offen lds\n"
"s_add_u32 m0, s66, 115712\n"
"s_nop 0\n"
"buffer_load_dwordx4 %[vs1], s[44:47], s68 offen lds\n"
"s_add_u32 m0, s66, 116736\n"
"s_nop 0\n"
"buffer_load_dwordx4 %[vs2], s[44:47], s68 offen lds\n"
"s_add_u32 m0, s66, 117760\n"
"s_nop 0\n"
"buffer_load_dwordx4 %[vs3], s[44:47], s68 offen lds\n"
"s_waitcnt vmcnt(16)\n"
"s_barrier\n"
"v_add_u32 v96, 65536, %[vt0]\n"
"v_add_u32 v97, 65536, %[vt1]\n"
"ds_read_b64_tr_b16 v[136:137], v96 offset:0\n"
"ds_read_b64_tr_b16 v[138:139], v96 offset:512\n"
"ds_read_b64_tr_b16 v[140:141], v96 offset:8192\n"
"ds_read_b64_tr_b16 v[142:143], v96 offset:8704\n"
"ds_read_b64_tr_b16 v[144:145], v97 offset:0\n"
"ds_read_b64_tr_b16 v[146:147], v97 offset:512\n"
"ds_read_b64_tr_b16 v[148:149], v97 offset:8192\n"
"ds_read_b64_tr_b16 v[150:151], v97 offset:8704\n"
"ds_read_b64_tr_b16 v[152:153], v96 offset:16384\n"
"ds_read_b64_tr_b16 v[154:155], v96 offset:16896\n"
"ds_read_b64_tr_b16 v[156:157], v96 offset:24576\n"
"ds_read_b64_tr_b16 v[158:159], v96 offset:25088\n"
"ds_read_b64_tr_b16 v[48:49], v97 offset:16384\n"
"ds_read_b64_tr_b16 v[50:51], v97 offset:16896\n"
"ds_read_b64_tr_b16 v[52:53], v97 offset:24576\n"
"ds_read_b64_tr_b16 v[54:55], v97 offset:25088\n"
"s_waitcnt lgkmcnt(0)\n"
"s_barrier\n"
"s_mov_b32 s68, 5767168\n"
"s_add_u32 m0, s66, 65536\n"
"s_nop 0\n"
"buffer_load_dwordx4 %[vs0], s[40:43], s68 offen lds\n"
"s_add_u32 m0, s66, 66560\n"
"s_nop 0\n"
"buffer_load_dwordx4 %[vs1], s[40:43], s68 offen lds\n"
"s_add_u32 m0, s66, 67584\n"
"s_nop 0\n"
"buffer_load_dwordx4 %[vs2], s[40:43], s68 offen lds\n"
"s_add_u32 m0, s66, 68608\n"
"s_nop 0\n"
"buffer_load_dwordx4 %[vs3], s[40:43], s68 offen lds\n"
"s_add_u32 m0, s66, 81920\n"
"s_nop 0\n"
"buffer_load_dwordx4 %[vs0], s[44:47], s68 offen lds\n"
"s_add_u32 m0, s66, 82944\n"
"s_nop 0\n"
"buffer_load_dwordx4 %[vs1], s[44:47], s68 offen lds\n"
"s_add_u32 m0, s66, 83968\n"
"s_nop 0\n"
"buffer_load_dwordx4 %[vs2], s[44:47], s68 offen lds\n"
"s_add_u32 m0, s66, 84992\n"
"s_nop 0\n"
"buffer_load_dwordx4 %[vs3], s[44:47], s68 offen lds\n"
"s_waitcnt vmcnt(16)\n"
"s_barrier\n"
"v_add_u32 v96, 131072, %[vt0]\n"
"v_add_u32 v97, 131072, %[vt1]\n"
"ds_read_b64_tr_b16 v[160:161], v96 offset:0\n"
"ds_read_b64_tr_b16 v[162:163], v96 offset:512\n"
"ds_read_b64_tr_b16 v[164:165], v96 offset:8192\n"
"ds_read_b64_tr_b16 v[166:167], v96 offset:8704\n"
"ds_read_b64_tr_b16 v[168:169], v97 offset:0\n"
"ds_read_b64_tr_b16 v[170:171], v97 offset:512\n"
"ds_read_b64_tr_b16 v[172:173], v97 offset:8192\n"
"ds_read_b64_tr_b16 v[174:175], v97 offset:8704\n"
"ds_read_b64_tr_b16 v[176:177], v96 offset:16384\n"
"ds_read_b64_tr_b16 v[178:179], v96 offset:16896\n"
"ds_read_b64_tr_b16 v[180:181], v96 offset:24576\n"
"ds_read_b64_tr_b16 v[182:183], v96 offset:25088\n"
"ds_read_b64_tr_b16 v[56:57], v97 offset:16384\n"
"ds_read_b64_tr_b16 v[58:59], v97 offset:16896\n"
"ds_read_b64_tr_b16 v[60:61], v97 offset:24576\n"
"ds_read_b64_tr_b16 v[62:63], v97 offset:25088\n"
"s_waitcnt lgkmcnt(0)\n"
"s_barrier\n"
"s_mov_b32 s68, 7864320\n"
"s_add_u32 m0, s66, 131072\n"
"s_nop 0\n"
"buffer_load_dwordx4 %[vs0], s[40:43], s68 offen lds\n"
"s_add_u32 m0, s66, 132096\n"
"s_nop 0\n"
"buffer_load_dwordx4 %[vs1], s[40:43], s68 offen lds\n"
"s_add_u32 m0, s66, 133120\n"
"s_nop 0\n"
"buffer_load_dwordx4 %[vs2], s[40:43], s68 offen lds\n"
"s_add_u32 m0, s66, 134144\n"
"s_nop 0\n"
"buffer_load_dwordx4 %[vs3], s[40:43], s68 offen lds\n"
"s_add_u32 m0, s66, 147456\n"
"s_nop 0\n"
"buffer_load_dwordx4 %[vs0], s[44:47], s68 offen lds\n"
"s_add_u32 m0, s66, 148480\n"
"s_nop 0\n"
"buffer_load_dwordx4 %[vs1], s[44:47], s68 offen lds\n"
"s_add_u32 m0, s66, 149504\n"
"s_nop 0\n"
"buffer_load_dwordx4 %[vs2], s[44:47], s68 offen lds\n"
"s_add_u32 m0, s66, 150528\n"
"s_nop 0\n"
"buffer_load_dwordx4 %[vs3], s[44:47], s68 offen lds\n"
"s_waitcnt vmcnt(16)\n"
"s_barrier\n"
"v_add_u32 v96, 98304, %[vt0]\n"
"v_add_u32 v97, 98304, %[vt1]\n"
"ds_read_b64_tr_b16 v[184:185], v96 offset:0\n"
"ds_read_b64_tr_b16 v[186:187], v96 offset:512\n"
"ds_read_b64_tr_b16 v[188:189], v96 offset:8192\n"
"ds_read_b64_tr_b16 v[190:191], v96 offset:8704\n"
"ds_read_b64_tr_b16 v[192:193], v97 offset:0\n"
"ds_read_b64_tr_b16 v[194:195], v97 offset:512\n"
"ds_read_b64_tr_b16 v[196:197], v97 offset:8192\n"
"ds_read_b64_tr_b16 v[198:199], v97 offset:8704\n"
"ds_read_b64_tr_b16 v[200:201], v96 offset:16384\n"
"ds_read_b64_tr_b16 v[202:203], v96 offset:16896\n"
"ds_read_b64_tr_b16 v[204:205], v96 offset:24576\n"
"ds_read_b64_tr_b16 v[206:207], v96 offset:25088\n"
"ds_read_b64_tr_b16 v[64:65], v97 offset:16384\n"
"ds_read_b64_tr_b16 v[66:67], v97 offset:16896\n"
"ds_read_b64_tr_b16 v[68:69], v97 offset:24576\n"
"ds_read_b64_tr_b16 v[70:71], v97 offset:25088\n"
"s_waitcnt lgkmcnt(0)\n"
"s_barrier\n"
"s_waitcnt vmcnt(8)\n"
"s_barrier\n"
"v_add_u32 v96, 65536, %[vt0]\n"
"v_add_u32 v97, 65536, %[vt1]\n"
"ds_read_b64_tr_b16 v[208:209], v96 offset:0\n"
"ds_read_b64_tr_b16 v[210:211], v96 offset:512\n"
"ds_read_b64_tr_b16 v[212:213], v96 offset:8192\n"
"ds_read_b64_tr_b16 v[214:215], v96 offset:8704\n"
"ds_read_b64_tr_b16 v[216:217], v97 offset:0\n"
"ds_read_b64_tr_b16 v[218:219], v97 offset:512\n"
"ds_read_b64_tr_b16 v[220:221], v97 offset:8192\n"
"ds_read_b64_tr_b16 v[222:223], v97 offset:8704\n"
"ds_read_b64_tr_b16 v[224:225], v96 offset:16384\n"
"ds_read_b64_tr_b16 v[226:227], v96 offset:16896\n"
"ds_read_b64_tr_b16 v[228:229], v96 offset:24576\n"
"ds_read_b64_tr_b16 v[230:231], v96 offset:25088\n"
"ds_read_b64_tr_b16 v[72:73], v97 offset:16384\n"
"ds_read_b64_tr_b16 v[74:75], v97 offset:16896\n"
"ds_read_b64_tr_b16 v[76:77], v97 offset:24576\n"
"ds_read_b64_tr_b16 v[78:79], v97 offset:25088\n"
"s_waitcnt lgkmcnt(0)\n"
"s_barrier\n"
"s_waitcnt vmcnt(0)\n"
"s_barrier\n"
"v_add_u32 v96, 131072, %[vt0]\n"
"v_add_u32 v97, 131072, %[vt1]\n"
"ds_read_b64_tr_b16 v[232:233], v96 offset:0\n"
"ds_read_b64_tr_b16 v[234:235], v96 offset:512\n"
"ds_read_b64_tr_b16 v[236:237], v96 offset:8192\n"
"ds_read_b64_tr_b16 v[238:239], v96 offset:8704\n"
"ds_read_b64_tr_b16 v[240:241], v97 offset:0\n"
"ds_read_b64_tr_b16 v[242:243], v97 offset:512\n"
"ds_read_b64_tr_b16 v[244:245], v97 offset:8192\n"
"ds_read_b64_tr_b16 v[246:247], v97 offset:8704\n"
"ds_read_b64_tr_b16 v[248:249], v96 offset:16384\n"
"ds_read_b64_tr_b16 v[250:251], v96 offset:16896\n"
"ds_read_b64_tr_b16 v[252:253], v96 offset:24576\n"
"ds_read_b64_tr_b16 v[254:255], v96 offset:25088\n"
"ds_read_b64_tr_b16 v[80:81], v97 offset:16384\n"
"ds_read_b64_tr_b16 v[82:83], v97 offset:16896\n"
"ds_read_b64_tr_b16 v[84:85], v97 offset:24576\n"
"ds_read_b64_tr_b16 v[86:87], v97 offset:25088\n"
"s_waitcnt lgkmcnt(0)\n"
"s_barrier\n"
"v_add_u32 v124, 0x10000, %[vf]\n"
"ds_write_b128 v124, v[24:27] offset:0\n"
"ds_write_b128 v124, v[28:31] offset:1024\n"
"ds_write_b128 v124, v[32:35] offset:8192\n"
"ds_write_b128 v124, v[36:39] offset:9216\n"
"ds_write_b128 v124, v[40:43] offset:16384\n"
"ds_write_b128 v124, v[44:47] offset:17408\n"
"ds_write_b128 v124, v[48:51] offset:24576\n"
"ds_write_b128 v124, v[52:55] offset:25600\n"
"ds_write_b128 v124, v[56:59] offset:32768\n"
"ds_write_b128 v124, v[60:63] offset:33792\n"
"ds_write_b128 v124, v[64:67] offset:40960\n"
"ds_write_b128 v124, v[68:71] offset:41984\n"
"ds_write_b128 v124, v[72:75] offset:49152\n"
"ds_write_b128 v124, v[76:79] offset:50176\n"
"ds_write_b128 v124, v[80:83] offset:57344\n"
"ds_write_b128 v124, v[84:87] offset:58368\n"
"s_waitcnt lgkmcnt(0)\n"
"v_mov_b32 v116, %[vci]\n"
"v_mov_b32 v120, 0x3f803f80\n"
"v_mov_b32 v117, %[vci]\n"
"v_mov_b32 v121, 0x3f803f80\n"
"v_mov_b32 v118, %[vci]\n"
"v_mov_b32 v122, 0x3f803f80\n"
"v_mov_b32 v119, %[vci]\n"
"v_mov_b32 v123, 0x3f803f80\n"
"s_mov_b32 s70, 0x3e0293ee\n"
"s_mov_b32 s71, 0x3e0293ee\n"
"s_mov_b32 s48, 0\n"
"s_mov_b32 s51, 98566144\n"
"s_mov_b32 s52, 100663296\n"
"s_mov_b32 s53, 105906176\n"
"s_mov_b32 s54, 111149056\n"
"s_mov_b32 s55, 20971520\n"
"s_mov_b32 s56, 37748736\n"
"s_mov_b32 s57, 110362624\n"
"s_mov_b32 s67, 110100480\n"
"s_lshl_b32 s73, s49, 11\n"
"s_add_u32 s73, s73, 151552\n"
"s_lshl_b32 s74, s49, 12\n"
"s_add_u32 s74, s74, 131072\n"
"s_lshl_b32 s75, s49, 9\n"
"s_memtime s[58:59]\n"
"s_waitcnt lgkmcnt(0)\n"
"LOOP_%=:\n"
"s_and_b32 s96, s48, 3\n"
"s_cmp_lg_u32 s96, 0\n"
"s_cbranch_scc1 KVOK_%=\n"
"s_lshl_b32 s96, s48, 10\n"
"s_add_u32 s96, s96, %[kvc]\n"
"KVP_%=:\n"
"buffer_load_dword v126, off, s[36:39], s96 sc1\n"
"s_waitcnt vmcnt(0)\n"
"v_readfirstlane_b32 s97, v126\n"
"s_and_b32 s97, s97, 0xff\n"
"s_cmp_ge_u32 s97, 4\n"
"s_cbranch_scc1 KVOK_%=\n"
"s_sleep 4\n"
"s_add_u32 s63, s63, 1\n"
"s_cmp_lt_u32 s63, 196608\n"
"s_cbranch_scc1 KVP_%=\n"
"KVOK_%=:\n"
"s_mov_b32 m0, s73\n"
"s_nop 0\n"
"buffer_load_dwordx4 %[vvd], s[36:39], s55 offen offset:0 sc1 lds\n"
"buffer_load_dwordx4 %[vvd], s[36:39], s55 offen offset:1024 sc1 lds\n"
"buffer_load_dwordx4 v[104:107], %[vvd], s[36:39], s56 offen offset:0 sc1\n"
"buffer_load_dwordx4 v[108:111], %[vvd], s[36:39], s56 offen offset:1024 sc1\n"
"buffer_load_dword v126, off, s[36:39], s57 sc1\n"
"v_mov_b64 v[24:25], 0\n"
"v_mov_b64 v[26:27], 0\n"
"v_mov_b64 v[28:29], 0\n"
"v_mov_b64 v[30:31], 0\n"
"v_mov_b64 v[32:33], 0\n"
"v_mov_b64 v[34:35], 0\n"
"v_mov_b64 v[36:37], 0\n"
"v_mov_b64 v[38:39], 0\n"
"ds_read_b128 v[56:59], %[vf] offset:0\n"
"ds_read_b128 v[60:63], %[vf] offset:1024\n"
"ds_read_b128 v[64:67], %[vf] offset:8192\n"
"ds_read_b128 v[68:71], %[vf] offset:9216\n"
"v_mov_b32 v125, 0\n"
"s_mov_b32 m0, s74\n"
"s_waitcnt lgkmcnt(0)\n"
"s_cmp_eq_u32 s48, 0\n"
"s_cbranch_scc1 NOHINT_%=\n"
"s_cmp_lg_u32 s49, 3\n"
"s_cbranch_scc1 HB_q_%=\n"
"buffer_load_dword v126, %[vhr], s[36:39], s57 offen sc1\n"
"s_sleep 2\n"
"HL_q_%=:\n"
"buffer_load_dword v127, %[vhr], s[36:39], s57 offen sc1\n"
"s_add_u32 s63, s63, 1\n"
"s_waitcnt vmcnt(1)\n"
"v_cmp_eq_u32 vcc, -1, v126\n"
"s_cmp_ge_u32 s63, 196608\n"
"s_cbranch_scc1 HO_q_%=\n"
"s_cbranch_vccz HO_q_%=\n"
"buffer_load_dword v126, %[vhr], s[36:39], s57 offen sc1\n"
"s_waitcnt vmcnt(1)\n"
"v_cmp_eq_u32 vcc, -1, v127\n"
"s_cbranch_vccnz HL_q_%=\n"
"HO_q_%=:\n"
"s_mov_b32 s60, s62\n"
"HD_q_%=:\n"
"s_sub_u32 s60, s60, 1\n"
"s_cbranch_scc0 HD_q_%=\n"
"HB_q_%=:\n"
"NOHINT_%=:\n"
"s_waitcnt vmcnt(1)\n"
"s_barrier\n"
"buffer_load_dwordx4 %[vm], s[36:39], s51 offen offset:0 sc1 lds\n"
"s_nop 11\n"
"buffer_load_dwordx4 %[vm], s[36:39], s51 offen offset:1024 sc1 lds\n"
"s_nop 11\n"
"buffer_load_dwordx4 %[vm], s[36:39], s51 offen offset:2048 sc1 lds\n"
"s_nop 11\n"
"buffer_load_dwordx4 %[vm], s[36:39], s51 offen offset:3072 sc1 lds\n"
"ds_read_b128 v[72:75], %[va] offset:20480\n"
"ds_read_b128 v[76:79], %[va] offset:21504\n"
"ds_read_b128 v[80:83], %[va] offset:22528\n"
"ds_read_b128 v[84:87], %[va] offset:23552\n"
"ds_read_b128 v[88:91], %[va] offset:24576\n"
"ds_read_b128 v[92:95], %[va] offset:25600\n"
"ds_read_b128 v[96:99], %[va] offset:26624\n"
"ds_read_b128 v[100:103], %[va] offset:27648\n"
"ENTRY_%=:\n"
"s_waitcnt vmcnt(3) lgkmcnt(0)\n"
"s_barrier\n"
"ds_read_b128 v[40:43], %[va] offset:0\n"
"ds_read_b128 v[44:47], %[va] offset:4096\n"
"ds_read_b128 v[48:51], %[va] offset:8192\n"
"ds_read_b128 v[52:55], %[va] offset:12288\n"
"s_waitcnt lgkmcnt(3)\n"
"v_smfmac_f32_16x16x64_bf16 v[24:27], v[40:43], a[0:7], %[vidx]\n"
"v_max3_u32 v125, v125, v40, v41\n"
"v_smfmac_f32_16x16x64_bf16 v[28:31], v[40:43], a[8:15], %[vidx]\n"
"v_max3_u32 v125, v125, v42, v43\n"
"v_smfmac_f32_16x16x64_bf16 v[32:35], v[40:43], a[16:23], %[vidx]\n"
"s_waitcnt lgkmcnt(2)\n"
"v_smfmac_f32_16x16x64_bf16 v[36:39], v[40:43], v[56:63], %[vidx]\n"
"s_waitcnt vmcnt(2)\n"
"s_barrier\n"
"ds_read_b128 v[40:43], %[va] offset:1024\n"
"v_smfmac_f32_16x16x64_bf16 v[24:27], v[44:47], a[24:31], %[vidx]\n"
"ds_read_b128 v[56:59], %[vf] offset:16384\n"
"v_max3_u32 v125, v125, v44, v45\n"
"v_smfmac_f32_16x16x64_bf16 v[28:31], v[44:47], a[32:39], %[vidx]\n"
"ds_read_b128 v[60:63], %[vf] offset:17408\n"
"v_max3_u32 v125, v125, v46, v47\n"
"v_smfmac_f32_16x16x64_bf16 v[32:35], v[44:47], a[40:47], %[vidx]\n"
"s_waitcnt lgkmcnt(4)\n"
"v_smfmac_f32_16x16x64_bf16 v[36:39], v[44:47], v[64:71], %[vidx]\n"
"ds_read_b128 v[44:47], %[va] offset:5120\n"
"v_smfmac_f32_16x16x64_bf16 v[24:27], v[48:51], a[48:55], %[vidx]\n"
"ds_read_b128 v[64:67], %[vf] offset:24576\n"
"v_max3_u32 v125, v125, v48, v49\n"
"v_smfmac_f32_16x16x64_bf16 v[28:31], v[48:51], a[56:63], %[vidx]\n"
"ds_read_b128 v[68:71], %[vf] offset:25600\n"
"v_max3_u32 v125, v125, v50, v51\n"
"v_smfmac_f32_16x16x64_bf16 v[32:35], v[48:51], a[64:71], %[vidx]\n"
"s_waitcnt lgkmcnt(3)\n"
"v_smfmac_f32_16x16x64_bf16 v[36:39], v[48:51], v[56:63], %[vidx]\n"
"ds_read_b128 v[48:51], %[va] offset:9216\n"
"v_smfmac_f32_16x16x64_bf16 v[24:27], v[52:55], a[72:79], %[vidx]\n"
"ds_read_b128 v[56:59], %[vf] offset:32768\n"
"v_max3_u32 v125, v125, v52, v53\n"
"v_smfmac_f32_16x16x64_bf16 v[28:31], v[52:55], a[80:87], %[vidx]\n"
"ds_read_b128 v[60:63], %[vf] offset:33792\n"
"v_max3_u32 v125, v125, v54, v55\n"
"v_smfmac_f32_16x16x64_bf16 v[32:35], v[52:55], a[88:95], %[vidx]\n"
"v_cmp_eq_u32 vcc, -1, v125\n"
"s_waitcnt lgkmcnt(3)\n"
"v_smfmac_f32_16x16x64_bf16 v[36:39], v[52:55], v[64:71], %[vidx]\n"
"s_cbranch_vccnz RETRY_%=\n"
"ds_read_b128 v[52:55], %[va] offset:13312\n"
"v_smfmac_f32_16x16x64_bf16 v[24:27], v[40:43], a[96:103], %[vidx]\n"
"ds_read_b128 v[64:67], %[vf] offset:40960\n"
"v_max3_u32 v125, v125, v40, v41\n"
"v_smfmac_f32_16x16x64_bf16 v[28:31], v[40:43], a[104:111], %[vidx]\n"
"ds_read_b128 v[68:71], %[vf] offset:41984\n"
"v_max3_u32 v125, v125, v42, v43\n"
"v_smfmac_f32_16x16x64_bf16 v[32:35], v[40:43], a[112:119], %[vidx]\n"
"s_waitcnt lgkmcnt(3)\n"
"v_smfmac_f32_16x16x64_bf16 v[36:39], v[40:43], v[56:63], %[vidx]\n"
"s_waitcnt vmcnt(1)\n"
"s_barrier\n"
"ds_read_b128 v[40:43], %[va] offset:2048\n"
"v_smfmac_f32_16x16x64_bf16 v[24:27], v[44:47], a[120:127], %[vidx]\n"
"ds_read_b128 v[56:59], %[vf] offset:49152\n"
"v_max3_u32 v125, v125, v44, v45\n"
"v_smfmac_f32_16x16x64_bf16 v[28:31], v[44:47], a[128:135], %[vidx]\n"
"ds_read_b128 v[60:63], %[vf] offset:50176\n"
"v_max3_u32 v125, v125, v46, v47\n"
"v_smfmac_f32_16x16x64_bf16 v[32:35], v[44:47], a[136:143], %[vidx]\n"
"s_waitcnt lgkmcnt(3)\n"
"v_smfmac_f32_16x16x64_bf16 v[36:39], v[44:47], v[64:71], %[vidx]\n"
"ds_read_b128 v[44:47], %[va] offset:6144\n"
"v_smfmac_f32_16x16x64_bf16 v[24:27], v[48:51], a[144:151], %[vidx]\n"
"ds_read_b128 v[64:67], %[vf] offset:57344\n"
"v_max3_u32 v125, v125, v48, v49\n"
"v_smfmac_f32_16x16x64_bf16 v[28:31], v[48:51], a[152:159], %[vidx]\n"
"ds_read_b128 v[68:71], %[vf] offset:58368\n"
"v_max3_u32 v125, v125, v50, v51\n"
"v_smfmac_f32_16x16x64_bf16 v[32:35], v[48:51], a[160:167], %[vidx]\n"
"s_waitcnt lgkmcnt(3)\n"
"v_smfmac_f32_16x16x64_bf16 v[36:39], v[48:51], v[56:63], %[vidx]\n"
"ds_read_b128 v[48:51], %[va] offset:10240\n"
"v_smfmac_f32_16x16x64_bf16 v[24:27], v[52:55], a[168:175], %[vidx]\n"
"ds_read_b128 v[56:59], v124 offset:0\n"
"v_max3_u32 v125, v125, v52, v53\n"
"v_smfmac_f32_16x16x64_bf16 v[28:31], v[52:55], a[176:183], %[vidx]\n"
"ds_read_b128 v[60:63], v124 offset:1024\n"
"v_max3_u32 v125, v125, v54, v55\n"
"v_smfmac_f32_16x16x64_bf16 v[32:35], v[52:55], a[184:191], %[vidx]\n"
"v_cmp_eq_u32 vcc, -1, v125\n"
"s_waitcnt lgkmcnt(3)\n"
"v_smfmac_f32_16x16x64_bf16 v[36:39], v[52:55], v[64:71], %[vidx]\n"
"s_cbranch_vccnz RETRY_%=\n"
"ds_read_b128 v[52:55], %[va] offset:14336\n"
"v_smfmac_f32_16x16x64_bf16 v[24:27], v[40:43], a[192:199], %[vidx]\n"
"ds_read_b128 v[64:67], v124 offset:8192\n"
"v_max3_u32 v125, v125, v40, v41\n"
"v_smfmac_f32_16x16x64_bf16 v[28:31], v[40:43], a[200:207], %[vidx]\n"
"ds_read_b128 v[68:71], v124 offset:9216\n"
"v_max3_u32 v125, v125, v42, v43\n"
"v_smfmac_f32_16x16x64_bf16 v[32:35], v[40:43], a[208:215], %[vidx]\n"
"s_waitcnt lgkmcnt(3)\n"
"v_smfmac_f32_16x16x64_bf16 v[36:39], v[40:43], v[56:63], %[vidx]\n"
"s_waitcnt vmcnt(0)\n"
"s_barrier\n"
"ds_read_b128 v[40:43], %[va] offset:3072\n"
"v_smfmac_f32_16x16x64_bf16 v[24:27], v[44:47], a[216:223], %[vidx]\n"
"ds_read_b128 v[56:59], v124 offset:16384\n"
"v_max3_u32 v125, v125, v44, v45\n"
"v_smfmac_f32_16x16x64_bf16 v[28:31], v[44:47], a[224:231], %[vidx]\n"
"ds_read_b128 v[60:63], v124 offset:17408\n"
"v_max3_u32 v125, v125, v46, v47\n"
"v_smfmac_f32_16x16x64_bf16 v[32:35], v[44:47], a[232:239], %[vidx]\n"
"s_waitcnt lgkmcnt(3)\n"
"v_smfmac_f32_16x16x64_bf16 v[36:39], v[44:47], v[64:71], %[vidx]\n"
"ds_read_b128 v[44:47], %[va] offset:7168\n"
"v_smfmac_f32_16x16x64_bf16 v[24:27], v[48:51], a[240:247], %[vidx]\n"
"ds_read_b128 v[64:67], v124 offset:24576\n"
"v_max3_u32 v125, v125, v48, v49\n"
"v_smfmac_f32_16x16x64_bf16 v[28:31], v[48:51], a[248:255], %[vidx]\n"
"ds_read_b128 v[68:71], v124 offset:25600\n"
"v_max3_u32 v125, v125, v50, v51\n"
"v_smfmac_f32_16x16x64_bf16 v[32:35], v[48:51], v[128:135], %[vidx]\n"
"s_waitcnt lgkmcnt(3)\n"
"v_smfmac_f32_16x16x64_bf16 v[36:39], v[48:51], v[56:63], %[vidx]\n"
"ds_read_b128 v[48:51], %[va] offset:11264\n"
"v_smfmac_f32_16x16x64_bf16 v[24:27], v[52:55], v[136:143], %[vidx]\n"
"ds_read_b128 v[56:59], v124 offset:32768\n"
"v_max3_u32 v125, v125, v52, v53\n"
"v_smfmac_f32_16x16x64_bf16 v[28:31], v[52:55], v[144:151], %[vidx]\n"
"ds_read_b128 v[60:63], v124 offset:33792\n"
"v_max3_u32 v125, v125, v54, v55\n"
"v_smfmac_f32_16x16x64_bf16 v[32:35], v[52:55], v[152:159], %[vidx]\n"
"v_cmp_eq_u32 vcc, -1, v125\n"
"s_waitcnt lgkmcnt(3)\n"
"v_smfmac_f32_16x16x64_bf16 v[36:39], v[52:55], v[64:71], %[vidx]\n"
"s_cbranch_vccnz RETRY_%=\n"
"ds_read_b128 v[52:55], %[va] offset:15360\n"
"v_smfmac_f32_16x16x64_bf16 v[24:27], v[40:43], v[160:167], %[vidx]\n"
"ds_read_b128 v[64:67], v124 offset:40960\n"
"v_max3_u32 v125, v125, v40, v41\n"
"v_smfmac_f32_16x16x64_bf16 v[28:31], v[40:43], v[168:175], %[vidx]\n"
"ds_read_b128 v[68:71], v124 offset:41984\n"
"v_max3_u32 v125, v125, v42, v43\n"
"v_smfmac_f32_16x16x64_bf16 v[32:35], v[40:43], v[176:183], %[vidx]\n"
"buffer_store_dword %[vidx], %[vhs], s[36:39], s67 offen sc1\n"
"s_waitcnt lgkmcnt(3)\n"
"v_smfmac_f32_16x16x64_bf16 v[36:39], v[40:43], v[56:63], %[vidx]\n"
"ds_read_b128 v[56:59], v124 offset:49152\n"
"v_smfmac_f32_16x16x64_bf16 v[24:27], v[44:47], v[184:191], %[vidx]\n"
"ds_read_b128 v[60:63], v124 offset:50176\n"
"v_max3_u32 v125, v125, v44, v45\n"
"v_smfmac_f32_16x16x64_bf16 v[28:31], v[44:47], v[192:199], %[vidx]\n"
"v_max3_u32 v125, v125, v46, v47\n"
"v_smfmac_f32_16x16x64_bf16 v[32:35], v[44:47], v[200:207], %[vidx]\n"
"buffer_load_dwordx2 v[112:113], %[vvm], s[36:39], s53 offen sc1\n"
"buffer_load_dwordx2 v[114:115], %[vvm], s[36:39], s53 offen offset:256 sc1\n"
"s_waitcnt lgkmcnt(2)\n"
"v_smfmac_f32_16x16x64_bf16 v[36:39], v[44:47], v[64:71], %[vidx]\n"
"ds_read_b128 v[64:67], v124 offset:57344\n"
"v_smfmac_f32_16x16x64_bf16 v[24:27], v[48:51], v[208:215], %[vidx]\n"
"ds_read_b128 v[68:71], v124 offset:58368\n"
"v_max3_u32 v125, v125, v48, v49\n"
"v_smfmac_f32_16x16x64_bf16 v[28:31], v[48:51], v[216:223], %[vidx]\n"
"v_max3_u32 v125, v125, v50, v51\n"
"v_smfmac_f32_16x16x64_bf16 v[32:35], v[48:51], v[224:231], %[vidx]\n"
"s_waitcnt lgkmcnt(2)\n"
"v_smfmac_f32_16x16x64_bf16 v[36:39], v[48:51], v[56:63], %[vidx]\n"
"v_smfmac_f32_16x16x64_bf16 v[24:27], v[52:55], v[232:239], %[vidx]\n"
"v_max3_u32 v125, v125, v52, v53\n"
"v_smfmac_f32_16x16x64_bf16 v[28:31], v[52:55], v[240:247], %[vidx]\n"
"v_max3_u32 v125, v125, v54, v55\n"
"v_smfmac_f32_16x16x64_bf16 v[32:35], v[52:55], v[248:255], %[vidx]\n"
"v_cmp_eq_u32 vcc, -1, v125\n"
"s_waitcnt lgkmcnt(0)\n"
"v_smfmac_f32_16x16x64_bf16 v[36:39], v[52:55], v[64:71], %[vidx]\n"
"s_cbranch_vccnz RETRY_%=\n"
"v_permlane32_swap_b32 v24, v28\n"
"v_permlane32_swap_b32 v25, v29\n"
"v_permlane32_swap_b32 v26, v30\n"
"v_permlane32_swap_b32 v27, v31\n"
"v_pk_add_f32 v[24:25], v[24:25], v[28:29]\n"
"v_pk_add_f32 v[26:27], v[26:27], v[30:31]\n"
"v_cvt_pk_bf16_f32 v28, v24, v25\n"
"v_cvt_pk_bf16_f32 v29, v26, v27\n"
"ds_write_b64 %[vqw], v[28:29]\n"
"v_permlane32_swap_b32 v32, v36\n"
"v_permlane32_swap_b32 v33, v37\n"
"v_permlane32_swap_b32 v34, v38\n"
"v_permlane32_swap_b32 v35, v39\n"
"v_pk_add_f32 v[32:33], v[32:33], v[36:37]\n"
"v_pk_add_f32 v[34:35], v[34:35], v[38:39]\n"
"v_cvt_pk_bf16_f32 v36, v32, v33\n"
"v_cvt_pk_bf16_f32 v37, v34, v35\n"
"ds_write_b64 %[vqw], v[36:37] offset:2048\n"
"s_waitcnt lgkmcnt(0)\n"
"s_barrier\n"
"ds_read_b64_tr_b16 v[40:41], %[vqr] offset:0\n"
"ds_read_b64_tr_b16 v[42:43], %[vqr] offset:64\n"
"ds_read_b64_tr_b16 v[56:57], %[vqr] offset:2048\n"
"ds_read_b64_tr_b16 v[58:59], %[vqr] offset:2112\n"
"ds_read_b64_tr_b16 v[44:45], %[vqr] offset:512\n"
"ds_read_b64_tr_b16 v[46:47], %[vqr] offset:576\n"
"ds_read_b64_tr_b16 v[60:61], %[vqr] offset:2560\n"
"ds_read_b64_tr_b16 v[62:63], %[vqr] offset:2624\n"
"ds_read_b64_tr_b16 v[48:49], %[vqr] offset:1024\n"
"ds_read_b64_tr_b16 v[50:51], %[vqr] offset:1088\n"
"ds_read_b64_tr_b16 v[64:65], %[vqr] offset:3072\n"
"ds_read_b64_tr_b16 v[66:67], %[vqr] offset:3136\n"
"ds_read_b64_tr_b16 v[52:53], %[vqr] offset:1536\n"
"ds_read_b64_tr_b16 v[54:55], %[vqr] offset:1600\n"
"ds_read_b64_tr_b16 v[68:69], %[vqr] offset:3584\n"
"ds_read_b64_tr_b16 v[70:71], %[vqr] offset:3648\n"
"s_waitcnt lgkmcnt(14)\n"
"v_mfma_f32_16x16x32_bf16 v[24:27], v[72:75], v[40:43], 0\n"
"v_mfma_f32_16x16x32_bf16 v[28:31], v[88:91], v[40:43], 0\n"
"s_waitcnt lgkmcnt(12)\n"
"v_mfma_f32_16x16x32_bf16 v[32:35], v[56:59], v[40:43], v[116:119]\n"
"s_waitcnt lgkmcnt(10)\n"
"v_mfma_f32_16x16x32_bf16 v[24:27], v[76:79], v[44:47], v[24:27]\n"
"v_mfma_f32_16x16x32_bf16 v[28:31], v[92:95], v[44:47], v[28:31]\n"
"s_waitcnt lgkmcnt(8)\n"
"v_mfma_f32_16x16x32_bf16 v[32:35], v[60:63], v[44:47], v[32:35]\n"
"s_waitcnt lgkmcnt(6)\n"
"v_mfma_f32_16x16x32_bf16 v[24:27], v[80:83], v[48:51], v[24:27]\n"
"v_mfma_f32_16x16x32_bf16 v[28:31], v[96:99], v[48:51], v[28:31]\n"
"s_waitcnt lgkmcnt(4)\n"
"v_mfma_f32_16x16x32_bf16 v[32:35], v[64:67], v[48:51], v[32:35]\n"
"s_waitcnt lgkmcnt(2)\n"
"v_mfma_f32_16x16x32_bf16 v[24:27], v[84:87], v[52:55], v[24:27]\n"
"v_mfma_f32_16x16x32_bf16 v[28:31], v[100:103], v[52:55], v[28:31]\n"
"s_waitcnt lgkmcnt(0)\n"
"v_mfma_f32_16x16x32_bf16 v[32:35], v[68:71], v[52:55], v[32:35]\n"
"s_nop 3\n"
"v_max3_f32 v125, v24, v25, v26\n"
"v_max3_f32 v125, v125, v27, v28\n"
"v_max3_f32 v125, v125, v29, v30\n"
"s_nop 1\n"
"v_max3_f32 v125, v125, v31, v32\n"
"v_max3_f32 v125, v125, v33, v34\n"
"v_max_f32 v125, v125, v35\n"
"v_mov_b32 v58, v125\n"
"s_nop 1\n"
"v_permlane32_swap_b32 v58, v125\n"
"v_max_f32 v125, v125, v58\n"
"v_mov_b32 v58, v125\n"
"s_nop 1\n"
"v_permlane16_swap_b32 v58, v125\n"
"v_max_f32 v125, v125, v58\n"
"v_mul_f32 v56, s70, v125\n"
"v_pk_fma_f32 v[24:25], v[24:25], s[70:71], v[56:57] op_sel_hi:[1,1,0] neg_lo:[0,0,1] neg_hi:[0,0,1]\n"
"v_pk_fma_f32 v[26:27], v[26:27], s[70:71], v[56:57] op_sel_hi:[1,1,0] neg_lo:[0,0,1] neg_hi:[0,0,1]\n"
"v_pk_fma_f32 v[28:29], v[28:29], s[70:71], v[56:57] op_sel_hi:[1,1,0] neg_lo:[0,0,1] neg_hi:[0,0,1]\n"
"v_pk_fma_f32 v[30:31], v[30:31], s[70:71], v[56:57] op_sel_hi:[1,1,0] neg_lo:[0,0,1] neg_hi:[0,0,1]\n"
"v_exp_f32 v24, v24\n"
"v_exp_f32 v25, v25\n"
"v_exp_f32 v26, v26\n"
"v_exp_f32 v27, v27\n"
"v_exp_f32 v28, v28\n"
"v_exp_f32 v29, v29\n"
"v_exp_f32 v30, v30\n"
"v_exp_f32 v31, v31\n"
"v_pk_fma_f32 v[32:33], v[32:33], s[70:71], v[56:57] op_sel_hi:[1,1,0] neg_lo:[0,0,1] neg_hi:[0,0,1]\n"
"v_pk_fma_f32 v[34:35], v[34:35], s[70:71], v[56:57] op_sel_hi:[1,1,0] neg_lo:[0,0,1] neg_hi:[0,0,1]\n"
"s_waitcnt vmcnt(0)\n"
"v_max3_u32 v125, v112, v113, v114\n"
"v_max_u32 v125, v125, v115\n"
"v_cmp_eq_u32 vcc, -1, v125\n"
"v_cvt_pk_bf16_f32 v60, v24, v25\n"
"v_cvt_pk_bf16_f32 v61, v26, v27\n"
"v_cvt_pk_bf16_f32 v62, v28, v29\n"
"v_cvt_pk_bf16_f32 v63, v30, v31\n"
"v_exp_f32 v32, v32\n"
"v_exp_f32 v33, v33\n"
"v_mfma_f32_16x16x32_bf16 v[48:51], v[120:123], v[60:63], 0\n"
"v_exp_f32 v34, v34\n"
"v_mfma_f32_16x16x32_bf16 v[40:43], v[104:107], v[60:63], 0\n"
"v_exp_f32 v35, v35\n"
"v_mfma_f32_16x16x32_bf16 v[44:47], v[108:111], v[60:63], 0\n"
"v_cvt_pk_bf16_f32 v64, v32, v33\n"
"v_cvt_pk_bf16_f32 v65, v34, v35\n"
"s_cbranch_vccnz VMRETRY_%=\n"
"VMOK_%=:\n"
"s_nop 0\n"
"v_mfma_f32_16x16x16_bf16 v[48:51], v[120:121], v[64:65], v[48:51]\n"
"v_mfma_f32_16x16x16_bf16 v[40:43], v[112:113], v[64:65], v[40:43]\n"
"v_mfma_f32_16x16x16_bf16 v[44:47], v[114:115], v[64:65], v[44:47]\n"
"s_nop 5\n"
"v_rcp_f32 v56, v48\n"
"s_nop 0\n"
"v_pk_mul_f32 v[40:41], v[40:41], v[56:57] op_sel_hi:[1,0]\n"
"v_pk_mul_f32 v[42:43], v[42:43], v[56:57] op_sel_hi:[1,0]\n"
"v_pk_mul_f32 v[44:45], v[44:45], v[56:57] op_sel_hi:[1,0]\n"
"v_pk_mul_f32 v[46:47], v[46:47], v[56:57] op_sel_hi:[1,0]\n"
"v_cvt_pk_bf16_f32 v60, v40, v41\n"
"v_cvt_pk_bf16_f32 v61, v42, v43\n"
"v_cvt_pk_bf16_f32 v62, v44, v45\n"
"v_cvt_pk_bf16_f32 v63, v46, v47\n"
"buffer_store_dwordx4 v[60:63], %[vo], s[36:39], s52 offen sc1\n"
"s_sleep 4\n"
"v_add_u32 v126, s75, %[vqr]\n"
"ds_read_b64_tr_b16 v[24:25], v126 offset:2048\n"
"ds_read_b64_tr_b16 v[26:27], v126 offset:2112\n"
"s_waitcnt lgkmcnt(0)\n"
"buffer_store_dwordx4 v[24:27], %[vkm], s[36:39], s54 offen sc1\n"
"s_mov_b32 s65, 0\n"
"s_add_u32 s48, s48, 1\n"
"s_add_u32 s57, s57, 0x2000\n"
"s_add_u32 s67, s67, 0x1000\n"
"s_add_u32 s51, s51, 20480\n"
"s_add_u32 s52, s52, 73728\n"
"s_add_u32 s53, s53, 0x10000\n"
"s_add_u32 s54, s54, 0x10000\n"
"s_add_u32 s55, s55, 0x40000\n"
"s_add_u32 s56, s56, 0x40000\n"
"s_cmp_lt_u32 s48, 64\n"
"s_cbranch_scc1 LOOP_%=\n"
"s_waitcnt vmcnt(0)\n"
"s_branch END_%=\n"
"RETRY_%=:\n"
"s_waitcnt lgkmcnt(0) vmcnt(0)\n"
"s_barrier\n"
"s_add_u32 s65, s65, 1\n"
"RP_%=:\n"
"buffer_load_dwordx4 v[40:43], %[vm], s[36:39], s51 offen offset:0 sc1\n"
"buffer_load_dwordx4 v[44:47], %[vm], s[36:39], s51 offen offset:1024 sc1\n"
"buffer_load_dwordx4 v[48:51], %[vm], s[36:39], s51 offen offset:2048 sc1\n"
"buffer_load_dwordx4 v[52:55], %[vm], s[36:39], s51 offen offset:3072 sc1\n"
"v_mov_b32 v125, 0\n"
"s_add_u32 s64, s64, 1\n"
"s_waitcnt vmcnt(3)\n"
"v_max3_u32 v125, v125, v40, v41\n"
"v_max3_u32 v125, v125, v42, v43\n"
"s_waitcnt vmcnt(2)\n"
"v_max3_u32 v125, v125, v44, v45\n"
"v_max3_u32 v125, v125, v46, v47\n"
"s_waitcnt vmcnt(1)\n"
"v_max3_u32 v125, v125, v48, v49\n"
"v_max3_u32 v125, v125, v50, v51\n"
"s_waitcnt vmcnt(0)\n"
"v_max3_u32 v125, v125, v52, v53\n"
"v_max3_u32 v125, v125, v54, v55\n"
"v_cmp_eq_u32 vcc, -1, v125\n"
"s_cmp_ge_u32 s64, 196608\n"
"s_cbranch_scc1 RGIVE_%=\n"
"s_cbranch_vccnz RP_%=\n"
"s_branch RW_%=\n"
"RGIVE_%=:\n"
"v_mov_b64 v[40:41], 0\n"
"v_mov_b64 v[42:43], 0\n"
"v_mov_b64 v[44:45], 0\n"
"v_mov_b64 v[46:47], 0\n"
"v_mov_b64 v[48:49], 0\n"
"v_mov_b64 v[50:51], 0\n"
"v_mov_b64 v[52:53], 0\n"
"v_mov_b64 v[54:55], 0\n"
"RW_%=:\n"
"s_lshl_b32 s96, s49, 12\n"
"v_add_u32 v126, s96, %[va]\n"
"ds_write_b128 v126, v[40:43] offset:0\n"
"ds_write_b128 v126, v[44:47] offset:1024\n"
"ds_write_b128 v126, v[48:51] offset:2048\n"
"ds_write_b128 v126, v[52:55] offset:3072\n"
"v_mov_b64 v[24:25], 0\n"
"v_mov_b64 v[26:27], 0\n"
"v_mov_b64 v[28:29], 0\n"
"v_mov_b64 v[30:31], 0\n"
"v_mov_b64 v[32:33], 0\n"
"v_mov_b64 v[34:35], 0\n"
"v_mov_b64 v[36:37], 0\n"
"v_mov_b64 v[38:39], 0\n"
"ds_read_b128 v[56:59], %[vf] offset:0\n"
"ds_read_b128 v[60:63], %[vf] offset:1024\n"
"ds_read_b128 v[64:67], %[vf] offset:8192\n"
"ds_read_b128 v[68:71], %[vf] offset:9216\n"
"v_mov_b32 v125, 0\n"
"s_waitcnt lgkmcnt(0)\n"
"s_branch ENTRY_%=\n"
"VMRETRY_%=:\n"
"buffer_load_dwordx2 v[112:113], %[vvm], s[36:39], s53 offen sc1\n"
"buffer_load_dwordx2 v[114:115], %[vvm], s[36:39], s53 offen offset:256 sc1\n"
"s_add_u32 s63, s63, 1\n"
"s_add_u32 s65, s65, 0x10000\n"
"s_waitcnt vmcnt(0)\n"
"v_max3_u32 v125, v112, v113, v114\n"
"v_max_u32 v125, v125, v115\n"
"v_cmp_eq_u32 vcc, -1, v125\n"
"s_cmp_ge_u32 s63, 196608\n"
"s_cbranch_scc1 VMOK_%=\n"
"s_cbranch_vccnz VMRETRY_%=\n"
"s_branch VMOK_%=\n"
"END_%=:\n"
  :: [ws] "s"(ws), [wq] "s"(Wq), [wk] "s"(Wk), [wave] "s"(wave), [w0] "s"(w0), [dbgo] "s"(dbgo), [kvc] "s"(CNT + (8 + h) * 128),
     [vm] "v"(vm), [va] "v"(va), [vf] "v"(vf), [vqw] "v"(vqw), [vqr] "v"(vqr), [vvd] "v"(vvd), [vvm] "v"(vvm),
     [vo] "v"(vo), [vkm] "v"(vkm), [vidx] "v"(vidx), [vci] "v"(vci), [vs0] "v"(vs[0]), [vs1] "v"(vs[1]), [vs2] "v"(vs[2]), [vs3] "v"(vs[3]), [vt0] "v"(vt0), [vt1] "v"(vt1), [vhr] "v"(vhr), [vhs] "v"(vhs)
  : "v24","v25","v26","v27","v28","v29","v30","v31","v32","v33","v34","v35","v36","v37","v38","v39","v40","v41","v42","v43","v44","v45","v46","v47","v48","v49","v50","v51","v52","v53","v54","v55",
"v56","v57","v58","v59","v60","v61","v62","v63","v64","v65","v66","v67","v68","v69","v70","v71","v72","v73","v74","v75","v76","v77","v78","v79","v80","v81","v82","v83","v84","v85","v86","v87",
"v88","v89","v90","v91","v92","v93","v94","v95","v96","v97","v98","v99","v100","v101","v102","v103","v104","v105","v106","v107","v108","v109","v110","v111","v112","v113","v114","v115","v116","v117","v118","v119",
"v120","v121","v122","v123","v124","v125","v126","v127","v128","v129","v130","v131","v132","v133","v134","v135","v136","v137","v138","v139","v140","v141","v142","v143","v144","v145","v146","v147","v148","v149","v150","v151",
"v152","v153","v154","v155","v156","v157","v158","v159","v160","v161","v162","v163","v164","v165","v166","v167","v168","v169","v170","v171","v172","v173","v174","v175","v176","v177","v178","v179","v180","v181","v182","v183",
"v184","v185","v186","v187","v188","v189","v190","v191","v192","v193","v194","v195","v196","v197","v198","v199","v200","v201","v202","v203","v204","v205","v206","v207","v208","v209","v210","v211","v212","v213","v214","v215",
"v216","v217","v218","v219","v220","v221","v222","v223","v224","v225","v226","v227","v228","v229","v230","v231","v232","v233","v234","v235","v236","v237","v238","v239","v240","v241","v242","v243","v244","v245","v246","v247",
"v248","v249","v250","v251","v252","v253","v254","v255","a0","a1","a2","a3","a4","a5","a6","a7","a8","a9","a10","a11","a12","a13","a14","a15","a16","a17","a18","a19","a20","a21","a22","a23",
"a24","a25","a26","a27","a28","a29","a30","a31","a32","a33","a34","a35","a36","a37","a38","a39","a40","a41","a42","a43","a44","a45","a46","a47","a48","a49","a50","a51","a52","a53","a54","a55",
"a56","a57","a58","a59","a60","a61","a62","a63","a64","a65","a66","a67","a68","a69","a70","a71","a72","a73","a74","a75","a76","a77","a78","a79","a80","a81","a82","a83","a84","a85","a86","a87",
"a88","a89","a90","a91","a92","a93","a94","a95","a96","a97","a98","a99","a100","a101","a102","a103","a104","a105","a106","a107","a108","a109","a110","a111","a112","a113","a114","a115","a116","a117","a118","a119",
"a120","a121","a122","a123","a124","a125","a126","a127","a128","a129","a130","a131","a132","a133","a134","a135","a136","a137","a138","a139","a140","a141","a142","a143","a144","a145","a146","a147","a148","a149","a150","a151",
"a152","a153","a154","a155","a156","a157","a158","a159","a160","a161","a162","a163","a164","a165","a166","a167","a168","a169","a170","a171","a172","a173","a174","a175","a176","a177","a178","a179","a180","a181","a182","a183",
"a184","a185","a186","a187","a188","a189","a190","a191","a192","a193","a194","a195","a196","a197","a198","a199","a200","a201","a202","a203","a204","a205","a206","a207","a208","a209","a210","a211","a212","a213","a214","a215",
"a216","a217","a218","a219","a220","a221","a222","a223","a224","a225","a226","a227","a228","a229","a230","a231","a232","a233","a234","a235","a236","a237","a238","a239","a240","a241","a242","a243","a244","a245","a246","a247",
"a248","a249","a250","a251","a252","a253","a254","a255","s36","s37","s38","s39","s40","s41","s42","s43","s44","s45","s46","s47","s48","s49","s50","s51","s52","s53","s54","s55","s56","s57","s58","s59",
"s60","s61","s62","s63","s64","s65","s66","s67","s68","s69","s70","s71","s72","s73","s74","s75","s76","s77","s78","s79","s80","s81","s82","s83","s84","s85","s86","s87","s88","s89","s90","s91",
"s92","s93","s94","s95","s96","s97","s98","s99","vcc","scc","memory");
}
__device__ __forceinline__ void vo_role(char* ws, const u16* Wv, const u16* Wo, int c, int rh, int wave, int w0, int w1, unsigned dbgo) {
  const unsigned l = lane_id(), w = wave;
  const unsigned i16 = l & 15, g = l >> 4, m = i16 & 7, hf = i16 >> 3, q = (l >> 2) & 3, p = l & 3;
  const unsigned vm = (g >> 1) * 512 + (m >> 2) * 256 + (2 * (g & 1) + hf) * 64 + (m & 3) * 16 + w * 5120;
  const unsigned vidx = i16 < 8 ? 0x4444u : 0xEEEEu;
  const unsigned vso = (w * 1024 + (l >> 2)) * 2048 + c * 64 + ((p ^ (2 * (l >> 5))) * 16);
  const unsigned vsv0 = (256 * w + (l >> 3)) * 8192 + c * 256 + rh * 128 + (((l & 7) ^ (2 * (l >> 5))) * 16), vsv1 = vsv0 ^ 64;
  const unsigned ro = w * 32768 + (16 * g + 8 * (q >> 1) + (q & 1)) * 64 + 8 * p;
  const unsigned vto0 = ro + ((0 ^ (q >> 1)) * 32), vto1 = ro + ((1 ^ (q >> 1)) * 32);
  const unsigned rv = w * 32768 + (16 * (g >> 1) + 8 * (q >> 1) + 4 * (g & 1) + (q & 1)) * 128 + 8 * p, fv = (g & 1) | ((q >> 1) << 1);
  const unsigned vtv0 = rv + ((0 ^ fv) * 32), vtv1 = rv + ((1 ^ fv) * 32), vtv2 = rv + ((2 ^ fv) * 32), vtv3 = rv + ((3 ^ fv) * 32);
  const unsigned vhr = (l % 1) * 128, vhs = (w == 0 && l == 0 && (rh * 32 + c) % 64 == 0) ? (rh * 32 + c) / 64 * 128 : OOB;
  const unsigned vlw = w * 2048 + l * 16, vlr = (w & 1) * 1024 + l * 16;
  const unsigned vvs = c * 2048 + (4 * rh + 2 * (w & 1)) * 256 + l * 8;
  const unsigned vo0 = rh * 36864 + w * 9216 + l * 16, vo1 = vo0 + 4096;
  const unsigned vfr = 8192 + (g >> 1) * 512 + (8 * (g & 1) + 2 * p) * 16 + q * 4;
  const unsigned vms = (c >> 3) * 5120 + (c & 7) * 512 + rh * 256 + l * 4;
  asm volatile(
"s_mov_b64 s[96:97], %[ws]\n"
"s_mov_b32 s36, s96\n"
"s_and_b32 s37, s97, 0xffff\n"
"s_mov_b32 s38, 0xf000000\n"
"s_mov_b32 s39, 0x20000\n"
"s_mov_b64 s[96:97], %[wv]\n"
"s_mov_b32 s40, s96\n"
"s_and_b32 s41, s97, 0xffff\n"
"s_mov_b32 s42, 0x800000\n"
"s_mov_b32 s43, 0x20000\n"
"s_mov_b64 s[96:97], %[wo]\n"
"s_mov_b32 s44, s96\n"
"s_and_b32 s45, s97, 0xffff\n"
"s_mov_b32 s46, 0x800000\n"
"s_mov_b32 s47, 0x20000\n"
"s_mov_b32 s49, %[wave]\n"
"s_mov_b32 s62, %[w0]\n"
"s_mov_b32 s57, %[w1]\n"
"s_mov_b32 s65, 0\n"
"s_mov_b32 s64, 0\n"
"s_mov_b32 s63, 0\n"
"s_lshl_b32 s66, s49, 15\n"
"s_mov_b32 s68, 0\n"
"s_add_u32 m0, s66, 0\n"
"s_nop 0\n"
"buffer_load_dwordx4 %[vso], s[44:47], s68 offen lds\n"
"s_mov_b32 s68, 32768\n"
"s_add_u32 m0, s66, 1024\n"
"s_nop 0\n"
"buffer_load_dwordx4 %[vso], s[44:47], s68 offen lds\n"
"s_mov_b32 s68, 65536\n"
"s_add_u32 m0, s66, 2048\n"
"s_nop 0\n"
"buffer_load_dwordx4 %[vso], s[44:47], s68 offen lds\n"
"s_mov_b32 s68, 98304\n"
"s_add_u32 m0, s66, 3072\n"
"s_nop 0\n"
"buffer_load_dwordx4 %[vso], s[44:47], s68 offen lds\n"
"s_mov_b32 s68, 131072\n"
"s_add_u32 m0, s66, 4096\n"
"s_nop 0\n"
"buffer_load_dwordx4 %[vso], s[44:47], s68 offen lds\n"
"s_mov_b32 s68, 163840\n"
"s_add_u32 m0, s66, 5120\n"
"s_nop 0\n"
"buffer_load_dwordx4 %[vso], s[44:47], s68 offen lds\n"
"s_mov_b32 s68, 196608\n"
"s_add_u32 m0, s66, 6144\n"
"s_nop 0\n"
"buffer_load_dwordx4 %[vso], s[44:47], s68 offen lds\n"
"s_mov_b32 s68, 229376\n"
"s_add_u32 m0, s66, 7168\n"
"s_nop 0\n"
"buffer_load_dwordx4 %[vso], s[44:47], s68 offen lds\n"
"s_mov_b32 s68, 262144\n"
"s_add_u32 m0, s66, 8192\n"
"s_nop 0\n"
"buffer_load_dwordx4 %[vso], s[44:47], s68 offen lds\n"
"s_mov_b32 s68, 294912\n"
"s_add_u32 m0, s66, 9216\n"
"s_nop 0\n"
"buffer_load_dwordx4 %[vso], s[44:47], s68 offen lds\n"
"s_mov_b32 s68, 327680\n"
"s_add_u32 m0, s66, 10240\n"
"s_nop 0\n"
"buffer_load_dwordx4 %[vso], s[44:47], s68 offen lds\n"
"s_mov_b32 s68, 360448\n"
"s_add_u32 m0, s66, 11264\n"
"s_nop 0\n"
"buffer_load_dwordx4 %[vso], s[44:47], s68 offen lds\n"
"s_mov_b32 s68, 393216\n"
"s_add_u32 m0, s66, 12288\n"
"s_nop 0\n"
"buffer_load_dwordx4 %[vso], s[44:47], s68 offen lds\n"
"s_mov_b32 s68, 425984\n"
"s_add_u32 m0, s66, 13312\n"
"s_nop 0\n"
"buffer_load_dwordx4 %[vso], s[44:47], s68 offen lds\n"
"s_mov_b32 s68, 458752\n"
"s_add_u32 m0, s66, 14336\n"
"s_nop 0\n"
"buffer_load_dwordx4 %[vso], s[44:47], s68 offen lds\n"
"s_mov_b32 s68, 491520\n"
"s_add_u32 m0, s66, 15360\n"
"s_nop 0\n"
"buffer_load_dwordx4 %[vso], s[44:47], s68 offen lds\n"
"s_mov_b32 s68, 524288\n"
"s_add_u32 m0, s66, 16384\n"
"s_nop 0\n"
"buffer_load_dwordx4 %[vso], s[44:47], s68 offen lds\n"
"s_mov_b32 s68, 557056\n"
"s_add_u32 m0, s66, 17408\n"
"s_nop 0\n"
"buffer_load_dwordx4 %[vso], s[44:47], s68 offen lds\n"
"s_mov_b32 s68, 589824\n"
"s_add_u32 m0, s66, 18432\n"
"s_nop 0\n"
"buffer_load_dwordx4 %[vso], s[44:47], s68 offen lds\n"
"s_mov_b32 s68, 622592\n"
"s_add_u32 m0, s66, 19456\n"
"s_nop 0\n"
"buffer_load_dwordx4 %[vso], s[44:47], s68 offen lds\n"
"s_mov_b32 s68, 655360\n"
"s_add_u32 m0, s66, 20480\n"
"s_nop 0\n"
"buffer_load_dwordx4 %[vso], s[44:47], s68 offen lds\n"
"s_mov_b32 s68, 688128\n"
"s_add_u32 m0, s66, 21504\n"
"s_nop 0\n"
"buffer_load_dwordx4 %[vso], s[44:47], s68 offen lds\n"
"s_mov_b32 s68, 720896\n"
"s_add_u32 m0, s66, 22528\n"
"s_nop 0\n"
"buffer_load_dwordx4 %[vso], s[44:47], s68 offen lds\n"
"s_mov_b32 s68, 753664\n"
"s_add_u32 m0, s66, 23552\n"
"s_nop 0\n"
"buffer_load_dwordx4 %[vso], s[44:47], s68 offen lds\n"
"s_mov_b32 s68, 786432\n"
"s_add_u32 m0, s66, 24576\n"
"s_nop 0\n"
"buffer_load_dwordx4 %[vso], s[44:47], s68 offen lds\n"
"s_mov_b32 s68, 819200\n"
"s_add_u32 m0, s66, 25600\n"
"s_nop 0\n"
"buffer_load_dwordx4 %[vso], s[44:47], s68 offen lds\n"
"s_mov_b32 s68, 851968\n"
"s_add_u32 m0, s66, 26624\n"
"s_nop 0\n"
"buffer_load_dwordx4 %[vso], s[44:47], s68 offen lds\n"
"s_mov_b32 s68, 884736\n"
"s_add_u32 m0, s66, 27648\n"
"s_nop 0\n"
"buffer_load_dwordx4 %[vso], s[44:47], s68 offen lds\n"
"s_mov_b32 s68, 917504\n"
"s_add_u32 m0, s66, 28672\n"
"s_nop 0\n"
"buffer_load_dwordx4 %[vso], s[44:47], s68 offen lds\n"
"s_mov_b32 s68, 950272\n"
"s_add_u32 m0, s66, 29696\n"
"s_nop 0\n"
"buffer_load_dwordx4 %[vso], s[44:47], s68 offen lds\n"
"s_mov_b32 s68, 983040\n"
"s_add_u32 m0, s66, 30720\n"
"s_nop 0\n"
"buffer_load_dwordx4 %[vso], s[44:47], s68 offen lds\n"
"s_mov_b32 s68, 1015808\n"
"s_add_u32 m0, s66, 31744\n"
"s_nop 0\n"
"buffer_load_dwordx4 %[vso], s[44:47], s68 offen lds\n"
"s_waitcnt vmcnt(24)\n"
"ds_read_b64_tr_b16 a[0:1], %[vto0] offset:0\n"
"ds_read_b64_tr_b16 a[2:3], %[vto0] offset:128\n"
"ds_read_b64_tr_b16 a[4:5], %[vto0] offset:4096\n"
"ds_read_b64_tr_b16 a[6:7], %[vto0] offset:4224\n"
"ds_read_b64_tr_b16 a[8:9], %[vto1] offset:0\n"
"ds_read_b64_tr_b16 a[10:11], %[vto1] offset:128\n"
"ds_read_b64_tr_b16 a[12:13], %[vto1] offset:4096\n"
"ds_read_b64_tr_b16 a[14:15], %[vto1] offset:4224\n"
"ds_read_b64_tr_b16 a[16:17], %[vto0] offset:256\n"
"ds_read_b64_tr_b16 a[18:19], %[vto0] offset:384\n"
"ds_read_b64_tr_b16 a[20:21], %[vto0] offset:4352\n"
"ds_read_b64_tr_b16 a[22:23], %[vto0] offset:4480\n"
"ds_read_b64_tr_b16 a[24:25], %[vto1] offset:256\n"
"ds_read_b64_tr_b16 a[26:27], %[vto1] offset:384\n"
"ds_read_b64_tr_b16 a[28:29], %[vto1] offset:4352\n"
"ds_read_b64_tr_b16 a[30:31], %[vto1] offset:4480\n"
"s_waitcnt lgkmcnt(0)\n"
"s_mov_b32 s68, 1048576\n"
"s_add_u32 m0, s66, 0\n"
"s_nop 0\n"
"buffer_load_dwordx4 %[vso], s[44:47], s68 offen lds\n"
"s_mov_b32 s68, 1081344\n"
"s_add_u32 m0, s66, 1024\n"
"s_nop 0\n"
"buffer_load_dwordx4 %[vso], s[44:47], s68 offen lds\n"
"s_mov_b32 s68, 1114112\n"
"s_add_u32 m0, s66, 2048\n"
"s_nop 0\n"
"buffer_load_dwordx4 %[vso], s[44:47], s68 offen lds\n"
"s_mov_b32 s68, 1146880\n"
"s_add_u32 m0, s66, 3072\n"
"s_nop 0\n"
"buffer_load_dwordx4 %[vso], s[44:47], s68 offen lds\n"
"s_mov_b32 s68, 1179648\n"
"s_add_u32 m0, s66, 4096\n"
"s_nop 0\n"
"buffer_load_dwordx4 %[vso], s[44:47], s68 offen lds\n"
"s_mov_b32 s68, 1212416\n"
"s_add_u32 m0, s66, 5120\n"
"s_nop 0\n"
"buffer_load_dwordx4 %[vso], s[44:47], s68 offen lds\n"
"s_mov_b32 s68, 1245184\n"
"s_add_u32 m0, s66, 6144\n"
"s_nop 0\n"
"buffer_load_dwordx4 %[vso], s[44:47], s68 offen lds\n"
"s_mov_b32 s68, 1277952\n"
"s_add_u32 m0, s66, 7168\n"
"s_nop 0\n"
"buffer_load_dwordx4 %[vso], s[44:47], s68 offen lds\n"
"s_waitcnt vmcnt(24)\n"
"ds_read_b64_tr_b16 a[32:33], %[vto0] offset:8192\n"
"ds_read_b64_tr_b16 a[34:35], %[vto0] offset:8320\n"
"ds_read_b64_tr_b16 a[36:37], %[vto0] offset:12288\n"
"ds_read_b64_tr_b16 a[38:39], %[vto0] offset:12416\n"
"ds_read_b64_tr_b16 a[40:41], %[vto1] offset:8192\n"
"ds_read_b64_tr_b16 a[42:43], %[vto1] offset:8320\n"
"ds_read_b64_tr_b16 a[44:45], %[vto1] offset:12288\n"
"ds_read_b64_tr_b16 a[46:47], %[vto1] offset:12416\n"
"ds_read_b64_tr_b16 a[48:49], %[vto0] offset:8448\n"
"ds_read_b64_tr_b16 a[50:51], %[vto0] offset:8576\n"
"ds_read_b64_tr_b16 a[52:53], %[vto0] offset:12544\n"
"ds_read_b64_tr_b16 a[54:55], %[vto0] offset:12672\n"
"ds_read_b64_tr_b16 a[56:57], %[vto1] offset:8448\n"
"ds_read_b64_tr_b16 a[58:59], %[vto1] offset:8576\n"
"ds_read_b64_tr_b16 a[60:61], %[vto1] offset:12544\n"
"ds_read_b64_tr_b16 a[62:63], %[vto1] offset:12672\n"
"s_waitcnt lgkmcnt(0)\n"
"s_mov_b32 s68, 1310720\n"
"s_add_u32 m0, s66, 8192\n"
"s_nop 0\n"
"buffer_load_dwordx4 %[vso], s[44:47], s68 offen lds\n"
"s_mov_b32 s68, 1343488\n"
"s_add_u32 m0, s66, 9216\n"
"s_nop 0\n"
"buffer_load_dwordx4 %[vso], s[44:47], s68 offen lds\n"
"s_mov_b32 s68, 1376256\n"
"s_add_u32 m0, s66, 10240\n"
"s_nop 0\n"
"buffer_load_dwordx4 %[vso], s[44:47], s68 offen lds\n"
"s_mov_b32 s68, 1409024\n"
"s_add_u32 m0, s66, 11264\n"
"s_nop 0\n"
"buffer_load_dwordx4 %[vso], s[44:47], s68 offen lds\n"
"s_mov_b32 s68, 1441792\n"
"s_add_u32 m0, s66, 12288\n"
"s_nop 0\n"
"buffer_load_dwordx4 %[vso], s[44:47], s68 offen lds\n"
"s_mov_b32 s68, 1474560\n"
"s_add_u32 m0, s66, 13312\n"
"s_nop 0\n"
"buffer_load_dwordx4 %[vso], s[44:47], s68 offen lds\n"
"s_mov_b32 s68, 1507328\n"
"s_add_u32 m0, s66, 14336\n"
"s_nop 0\n"
"buffer_load_dwordx4 %[vso], s[44:47], s68 offen lds\n"
"s_mov_b32 s68, 1540096\n"
"s_add_u32 m0, s66, 15360\n"
"s_nop 0\n"
"buffer_load_dwordx4 %[vso], s[44:47], s68 offen lds\n"
"s_waitcnt vmcnt(24)\n"
"ds_read_b64_tr_b16 a[64:65], %[vto0] offset:16384\n"
"ds_read_b64_tr_b16 a[66:67], %[vto0] offset:16512\n"
"ds_read_b64_tr_b16 a[68:69], %[vto0] offset:20480\n"
"ds_read_b64_tr_b16 a[70:71], %[vto0] offset:20608\n"
"ds_read_b64_tr_b16 a[72:73], %[vto1] offset:16384\n"
"ds_read_b64_tr_b16 a[74:75], %[vto1] offset:16512\n"
"ds_read_b64_tr_b16 a[76:77], %[vto1] offset:20480\n"
"ds_read_b64_tr_b16 a[78:79], %[vto1] offset:20608\n"
"ds_read_b64_tr_b16 a[80:81], %[vto0] offset:16640\n"
"ds_read_b64_tr_b16 a[82:83], %[vto0] offset:16768\n"
"ds_read_b64_tr_b16 a[84:85], %[vto0] offset:20736\n"
"ds_read_b64_tr_b16 a[86:87], %[vto0] offset:20864\n"
"ds_read_b64_tr_b16 a[88:89], %[vto1] offset:16640\n"
"ds_read_b64_tr_b16 a[90:91], %[vto1] offset:16768\n"
"ds_read_b64_tr_b16 a[92:93], %[vto1] offset:20736\n"
"ds_read_b64_tr_b16 a[94:95], %[vto1] offset:20864\n"
"s_waitcnt lgkmcnt(0)\n"
"s_mov_b32 s68, 1572864\n"
"s_add_u32 m0, s66, 16384\n"
"s_nop 0\n"
"buffer_load_dwordx4 %[vso], s[44:47], s68 offen lds\n"
"s_mov_b32 s68, 1605632\n"
"s_add_u32 m0, s66, 17408\n"
"s_nop 0\n"
"buffer_load_dwordx4 %[vso], s[44:47], s68 offen lds\n"
"s_mov_b32 s68, 1638400\n"
"s_add_u32 m0, s66, 18432\n"
"s_nop 0\n"
"buffer_load_dwordx4 %[vso], s[44:47], s68 offen lds\n"
"s_mov_b32 s68, 1671168\n"
"s_add_u32 m0, s66, 19456\n"
"s_nop 0\n"
"buffer_load_dwordx4 %[vso], s[44:47], s68 offen lds\n"
"s_mov_b32 s68, 1703936\n"
"s_add_u32 m0, s66, 20480\n"
"s_nop 0\n"
"buffer_load_dwordx4 %[vso], s[44:47], s68 offen lds\n"
"s_mov_b32 s68, 1736704\n"
"s_add_u32 m0, s66, 21504\n"
"s_nop 0\n"
"buffer_load_dwordx4 %[vso], s[44:47], s68 offen lds\n"
"s_mov_b32 s68, 1769472\n"
"s_add_u32 m0, s66, 22528\n"
"s_nop 0\n"
"buffer_load_dwordx4 %[vso], s[44:47], s68 offen lds\n"
"s_mov_b32 s68, 1802240\n"
"s_add_u32 m0, s66, 23552\n"
"s_nop 0\n"
"buffer_load_dwordx4 %[vso], s[44:47], s68 offen lds\n"
"s_waitcnt vmcnt(24)\n"
"ds_read_b64_tr_b16 a[96:97], %[vto0] offset:24576\n"
"ds_read_b64_tr_b16 a[98:99], %[vto0] offset:24704\n"
"ds_read_b64_tr_b16 a[100:101], %[vto0] offset:28672\n"
"ds_read_b64_tr_b16 a[102:103], %[vto0] offset:28800\n"
"ds_read_b64_tr_b16 a[104:105], %[vto1] offset:24576\n"
"ds_read_b64_tr_b16 a[106:107], %[vto1] offset:24704\n"
"ds_read_b64_tr_b16 a[108:109], %[vto1] offset:28672\n"
"ds_read_b64_tr_b16 a[110:111], %[vto1] offset:28800\n"
"ds_read_b64_tr_b16 a[112:113], %[vto0] offset:24832\n"
"ds_read_b64_tr_b16 a[114:115], %[vto0] offset:24960\n"
"ds_read_b64_tr_b16 a[116:117], %[vto0] offset:28928\n"
"ds_read_b64_tr_b16 a[118:119], %[vto0] offset:29056\n"
"ds_read_b64_tr_b16 a[120:121], %[vto1] offset:24832\n"
"ds_read_b64_tr_b16 a[122:123], %[vto1] offset:24960\n"
"ds_read_b64_tr_b16 a[124:125], %[vto1] offset:28928\n"
"ds_read_b64_tr_b16 a[126:127], %[vto1] offset:29056\n"
"s_waitcnt lgkmcnt(0)\n"
"s_mov_b32 s68, 1835008\n"
"s_add_u32 m0, s66, 24576\n"
"s_nop 0\n"
"buffer_load_dwordx4 %[vso], s[44:47], s68 offen lds\n"
"s_mov_b32 s68, 1867776\n"
"s_add_u32 m0, s66, 25600\n"
"s_nop 0\n"
"buffer_load_dwordx4 %[vso], s[44:47], s68 offen lds\n"
"s_mov_b32 s68, 1900544\n"
"s_add_u32 m0, s66, 26624\n"
"s_nop 0\n"
"buffer_load_dwordx4 %[vso], s[44:47], s68 offen lds\n"
"s_mov_b32 s68, 1933312\n"
"s_add_u32 m0, s66, 27648\n"
"s_nop 0\n"
"buffer_load_dwordx4 %[vso], s[44:47], s68 offen lds\n"
"s_mov_b32 s68, 1966080\n"
"s_add_u32 m0, s66, 28672\n"
"s_nop 0\n"
"buffer_load_dwordx4 %[vso], s[44:47], s68 offen lds\n"
"s_mov_b32 s68, 1998848\n"
"s_add_u32 m0, s66, 29696\n"
"s_nop 0\n"
"buffer_load_dwordx4 %[vso], s[44:47], s68 offen lds\n"
"s_mov_b32 s68, 2031616\n"
"s_add_u32 m0, s66, 30720\n"
"s_nop 0\n"
"buffer_load_dwordx4 %[vso], s[44:47], s68 offen lds\n"
"s_mov_b32 s68, 2064384\n"
"s_add_u32 m0, s66, 31744\n"
"s_nop 0\n"
"buffer_load_dwordx4 %[vso], s[44:47], s68 offen lds\n"
"s_waitcnt vmcnt(24)\n"
"ds_read_b64_tr_b16 a[128:129], %[vto0] offset:0\n"
"ds_read_b64_tr_b16 a[130:131], %[vto0] offset:128\n"
"ds_read_b64_tr_b16 a[132:133], %[vto0] offset:4096\n"
"ds_read_b64_tr_b16 a[134:135], %[vto0] offset:4224\n"
"ds_read_b64_tr_b16 a[136:137], %[vto1] offset:0\n"
"ds_read_b64_tr_b16 a[138:139], %[vto1] offset:128\n"
"ds_read_b64_tr_b16 a[140:141], %[vto1] offset:4096\n"
"ds_read_b64_tr_b16 a[142:143], %[vto1] offset:4224\n"
"ds_read_b64_tr_b16 a[144:145], %[vto0] offset:256\n"
"ds_read_b64_tr_b16 a[146:147], %[vto0] offset:384\n"
"ds_read_b64_tr_b16 a[148:149], %[vto0] offset:4352\n"
"ds_read_b64_tr_b16 a[150:151], %[vto0] offset:4480\n"
"ds_read_b64_tr_b16 a[152:153], %[vto1] offset:256\n"
"ds_read_b64_tr_b16 a[154:155], %[vto1] offset:384\n"
"ds_read_b64_tr_b16 a[156:157], %[vto1] offset:4352\n"
"ds_read_b64_tr_b16 a[158:159], %[vto1] offset:4480\n"
"s_waitcnt lgkmcnt(0)\n"
"s_mov_b32 s68, 0\n"
"s_add_u32 m0, s66, 0\n"
"s_nop 0\n"
"buffer_load_dwordx4 %[vsv0], s[40:43], s68 offen lds\n"
"s_mov_b32 s68, 65536\n"
"s_add_u32 m0, s66, 1024\n"
"s_nop 0\n"
"buffer_load_dwordx4 %[vsv1], s[40:43], s68 offen lds\n"
"s_mov_b32 s68, 131072\n"
"s_add_u32 m0, s66, 2048\n"
"s_nop 0\n"
"buffer_load_dwordx4 %[vsv0], s[40:43], s68 offen lds\n"
"s_mov_b32 s68, 196608\n"
"s_add_u32 m0, s66, 3072\n"
"s_nop 0\n"
"buffer_load_dwordx4 %[vsv1], s[40:43], s68 offen lds\n"
"s_mov_b32 s68, 262144\n"
"s_add_u32 m0, s66, 4096\n"
"s_nop 0\n"
"buffer_load_dwordx4 %[vsv0], s[40:43], s68 offen lds\n"
"s_mov_b32 s68, 327680\n"
"s_add_u32 m0, s66, 5120\n"
"s_nop 0\n"
"buffer_load_dwordx4 %[vsv1], s[40:43], s68 offen lds\n"
"s_mov_b32 s68, 393216\n"
"s_add_u32 m0, s66, 6144\n"
"s_nop 0\n"
"buffer_load_dwordx4 %[vsv0], s[40:43], s68 offen lds\n"
"s_mov_b32 s68, 458752\n"
"s_add_u32 m0, s66, 7168\n"
"s_nop 0\n"
"buffer_load_dwordx4 %[vsv1], s[40:43], s68 offen lds\n"
"s_waitcnt vmcnt(24)\n"
"ds_read_b64_tr_b16 a[160:161], %[vto0] offset:8192\n"
"ds_read_b64_tr_b16 a[162:163], %[vto0] offset:8320\n"
"ds_read_b64_tr_b16 a[164:165], %[vto0] offset:12288\n"
"ds_read_b64_tr_b16 a[166:167], %[vto0] offset:12416\n"
"ds_read_b64_tr_b16 a[168:169], %[vto1] offset:8192\n"
"ds_read_b64_tr_b16 a[170:171], %[vto1] offset:8320\n"
"ds_read_b64_tr_b16 a[172:173], %[vto1] offset:12288\n"
"ds_read_b64_tr_b16 a[174:175], %[vto1] offset:12416\n"
"ds_read_b64_tr_b16 a[176:177], %[vto0] offset:8448\n"
"ds_read_b64_tr_b16 a[178:179], %[vto0] offset:8576\n"
"ds_read_b64_tr_b16 a[180:181], %[vto0] offset:12544\n"
"ds_read_b64_tr_b16 a[182:183], %[vto0] offset:12672\n"
"ds_read_b64_tr_b16 a[184:185], %[vto1] offset:8448\n"
"ds_read_b64_tr_b16 a[186:187], %[vto1] offset:8576\n"
"ds_read_b64_tr_b16 a[188:189], %[vto1] offset:12544\n"
"ds_read_b64_tr_b16 a[190:191], %[vto1] offset:12672\n"
"s_waitcnt lgkmcnt(0)\n"
"s_mov_b32 s68, 524288\n"
"s_add_u32 m0, s66, 8192\n"
"s_nop 0\n"
"buffer_load_dwordx4 %[vsv0], s[40:43], s68 offen lds\n"
"s_mov_b32 s68, 589824\n"
"s_add_u32 m0, s66, 9216\n"
"s_nop 0\n"
"buffer_load_dwordx4 %[vsv1], s[40:43], s68 offen lds\n"
"s_mov_b32 s68, 655360\n"
"s_add_u32 m0, s66, 10240\n"
"s_nop 0\n"
"buffer_load_dwordx4 %[vsv0], s[40:43], s68 offen lds\n"
"s_mov_b32 s68, 720896\n"
"s_add_u32 m0, s66, 11264\n"
"s_nop 0\n"
"buffer_load_dwordx4 %[vsv1], s[40:43], s68 offen lds\n"
"s_mov_b32 s68, 786432\n"
"s_add_u32 m0, s66, 12288\n"
"s_nop 0\n"
"buffer_load_dwordx4 %[vsv0], s[40:43], s68 offen lds\n"
"s_mov_b32 s68, 851968\n"
"s_add_u32 m0, s66, 13312\n"
"s_nop 0\n"
"buffer_load_dwordx4 %[vsv1], s[40:43], s68 offen lds\n"
"s_mov_b32 s68, 917504\n"
"s_add_u32 m0, s66, 14336\n"
"s_nop 0\n"
"buffer_load_dwordx4 %[vsv0], s[40:43], s68 offen lds\n"
"s_mov_b32 s68, 983040\n"
"s_add_u32 m0, s66, 15360\n"
"s_nop 0\n"
"buffer_load_dwordx4 %[vsv1], s[40:43], s68 offen lds\n"
"s_waitcnt vmcnt(24)\n"
"ds_read_b64_tr_b16 a[192:193], %[vto0] offset:16384\n"
"ds_read_b64_tr_b16 a[194:195], %[vto0] offset:16512\n"
"ds_read_b64_tr_b16 a[196:197], %[vto0] offset:20480\n"
"ds_read_b64_tr_b16 a[198:199], %[vto0] offset:20608\n"
"ds_read_b64_tr_b16 a[200:201], %[vto1] offset:16384\n"
"ds_read_b64_tr_b16 a[202:203], %[vto1] offset:16512\n"
"ds_read_b64_tr_b16 a[204:205], %[vto1] offset:20480\n"
"ds_read_b64_tr_b16 a[206:207], %[vto1] offset:20608\n"
"ds_read_b64_tr_b16 a[208:209], %[vto0] offset:16640\n"
"ds_read_b64_tr_b16 a[210:211], %[vto0] offset:16768\n"
"ds_read_b64_tr_b16 a[212:213], %[vto0] offset:20736\n"
"ds_read_b64_tr_b16 a[214:215], %[vto0] offset:20864\n"
"ds_read_b64_tr_b16 a[216:217], %[vto1] offset:16640\n"
"ds_read_b64_tr_b16 a[218:219], %[vto1] offset:16768\n"
"ds_read_b64_tr_b16 a[220:221], %[vto1] offset:20736\n"
"ds_read_b64_tr_b16 a[222:223], %[vto1] offset:20864\n"
"s_waitcnt lgkmcnt(0)\n"
"s_mov_b32 s68, 1048576\n"
"s_add_u32 m0, s66, 16384\n"
"s_nop 0\n"
"buffer_load_dwordx4 %[vsv0], s[40:43], s68 offen lds\n"
"s_mov_b32 s68, 1114112\n"
"s_add_u32 m0, s66, 17408\n"
"s_nop 0\n"
"buffer_load_dwordx4 %[vsv1], s[40:43], s68 offen lds\n"
"s_mov_b32 s68, 1179648\n"
"s_add_u32 m0, s66, 18432\n"
"s_nop 0\n"
"buffer_load_dwordx4 %[vsv0], s[40:43], s68 offen lds\n"
"s_mov_b32 s68, 1245184\n"
"s_add_u32 m0, s66, 19456\n"
"s_nop 0\n"
"buffer_load_dwordx4 %[vsv1], s[40:43], s68 offen lds\n"
"s_mov_b32 s68, 1310720\n"
"s_add_u32 m0, s66, 20480\n"
"s_nop 0\n"
"buffer_load_dwordx4 %[vsv0], s[40:43], s68 offen lds\n"
"s_mov_b32 s68, 1376256\n"
"s_add_u32 m0, s66, 21504\n"
"s_nop 0\n"
"buffer_load_dwordx4 %[vsv1], s[40:43], s68 offen lds\n"
"s_mov_b32 s68, 1441792\n"
"s_add_u32 m0, s66, 22528\n"
"s_nop 0\n"
"buffer_load_dwordx4 %[vsv0], s[40:43], s68 offen lds\n"
"s_mov_b32 s68, 1507328\n"
"s_add_u32 m0, s66, 23552\n"
"s_nop 0\n"
"buffer_load_dwordx4 %[vsv1], s[40:43], s68 offen lds\n"
"s_waitcnt vmcnt(24)\n"
"ds_read_b64_tr_b16 a[224:225], %[vto0] offset:24576\n"
"ds_read_b64_tr_b16 a[226:227], %[vto0] offset:24704\n"
"ds_read_b64_tr_b16 a[228:229], %[vto0] offset:28672\n"
"ds_read_b64_tr_b16 a[230:231], %[vto0] offset:28800\n"
"ds_read_b64_tr_b16 a[232:233], %[vto1] offset:24576\n"
"ds_read_b64_tr_b16 a[234:235], %[vto1] offset:24704\n"
"ds_read_b64_tr_b16 a[236:237], %[vto1] offset:28672\n"
"ds_read_b64_tr_b16 a[238:239], %[vto1] offset:28800\n"
"ds_read_b64_tr_b16 a[240:241], %[vto0] offset:24832\n"
"ds_read_b64_tr_b16 a[242:243], %[vto0] offset:24960\n"
"ds_read_b64_tr_b16 a[244:245], %[vto0] offset:28928\n"
"ds_read_b64_tr_b16 a[246:247], %[vto0] offset:29056\n"
"ds_read_b64_tr_b16 a[248:249], %[vto1] offset:24832\n"
"ds_read_b64_tr_b16 a[250:251], %[vto1] offset:24960\n"
"ds_read_b64_tr_b16 a[252:253], %[vto1] offset:28928\n"
"ds_read_b64_tr_b16 a[254:255], %[vto1] offset:29056\n"
"s_waitcnt lgkmcnt(0)\n"
"s_mov_b32 s68, 1572864\n"
"s_add_u32 m0, s66, 24576\n"
"s_nop 0\n"
"buffer_load_dwordx4 %[vsv0], s[40:43], s68 offen lds\n"
"s_mov_b32 s68, 1638400\n"
"s_add_u32 m0, s66, 25600\n"
"s_nop 0\n"
"buffer_load_dwordx4 %[vsv1], s[40:43], s68 offen lds\n"
"s_mov_b32 s68, 1703936\n"
"s_add_u32 m0, s66, 26624\n"
"s_nop 0\n"
"buffer_load_dwordx4 %[vsv0], s[40:43], s68 offen lds\n"
"s_mov_b32 s68, 1769472\n"
"s_add_u32 m0, s66, 27648\n"
"s_nop 0\n"
"buffer_load_dwordx4 %[vsv1], s[40:43], s68 offen lds\n"
"s_mov_b32 s68, 1835008\n"
"s_add_u32 m0, s66, 28672\n"
"s_nop 0\n"
"buffer_load_dwordx4 %[vsv0], s[40:43], s68 offen lds\n"
"s_mov_b32 s68, 1900544\n"
"s_add_u32 m0, s66, 29696\n"
"s_nop 0\n"
"buffer_load_dwordx4 %[vsv1], s[40:43], s68 offen lds\n"
"s_mov_b32 s68, 1966080\n"
"s_add_u32 m0, s66, 30720\n"
"s_nop 0\n"
"buffer_load_dwordx4 %[vsv0], s[40:43], s68 offen lds\n"
"s_mov_b32 s68, 2031616\n"
"s_add_u32 m0, s66, 31744\n"
"s_nop 0\n"
"buffer_load_dwordx4 %[vsv1], s[40:43], s68 offen lds\n"
"s_waitcnt vmcnt(24)\n"
"ds_read_b64_tr_b16 v[128:129], %[vtv0] offset:0\n"
"ds_read_b64_tr_b16 v[130:131], %[vtv0] offset:256\n"
"ds_read_b64_tr_b16 v[132:133], %[vtv0] offset:4096\n"
"ds_read_b64_tr_b16 v[134:135], %[vtv0] offset:4352\n"
"ds_read_b64_tr_b16 v[136:137], %[vtv1] offset:0\n"
"ds_read_b64_tr_b16 v[138:139], %[vtv1] offset:256\n"
"ds_read_b64_tr_b16 v[140:141], %[vtv1] offset:4096\n"
"ds_read_b64_tr_b16 v[142:143], %[vtv1] offset:4352\n"
"ds_read_b64_tr_b16 v[144:145], %[vtv2] offset:0\n"
"ds_read_b64_tr_b16 v[146:147], %[vtv2] offset:256\n"
"ds_read_b64_tr_b16 v[148:149], %[vtv2] offset:4096\n"
"ds_read_b64_tr_b16 v[150:151], %[vtv2] offset:4352\n"
"ds_read_b64_tr_b16 v[152:153], %[vtv3] offset:0\n"
"ds_read_b64_tr_b16 v[154:155], %[vtv3] offset:256\n"
"ds_read_b64_tr_b16 v[156:157], %[vtv3] offset:4096\n"
"ds_read_b64_tr_b16 v[158:159], %[vtv3] offset:4352\n"
"s_waitcnt lgkmcnt(0)\n"
"s_waitcnt vmcnt(16)\n"
"ds_read_b64_tr_b16 v[160:161], %[vtv0] offset:8192\n"
"ds_read_b64_tr_b16 v[162:163], %[vtv0] offset:8448\n"
"ds_read_b64_tr_b16 v[164:165], %[vtv0] offset:12288\n"
"ds_read_b64_tr_b16 v[166:167], %[vtv0] offset:12544\n"
"ds_read_b64_tr_b16 v[168:169], %[vtv1] offset:8192\n"
"ds_read_b64_tr_b16 v[170:171], %[vtv1] offset:8448\n"
"ds_read_b64_tr_b16 v[172:173], %[vtv1] offset:12288\n"
"ds_read_b64_tr_b16 v[174:175], %[vtv1] offset:12544\n"
"ds_read_b64_tr_b16 v[176:177], %[vtv2] offset:8192\n"
"ds_read_b64_tr_b16 v[178:179], %[vtv2] offset:8448\n"
"ds_read_b64_tr_b16 v[180:181], %[vtv2] offset:12288\n"
"ds_read_b64_tr_b16 v[182:183], %[vtv2] offset:12544\n"
"ds_read_b64_tr_b16 v[184:185], %[vtv3] offset:8192\n"
"ds_read_b64_tr_b16 v[186:187], %[vtv3] offset:8448\n"
"ds_read_b64_tr_b16 v[188:189], %[vtv3] offset:12288\n"
"ds_read_b64_tr_b16 v[190:191], %[vtv3] offset:12544\n"
"s_waitcnt lgkmcnt(0)\n"
"s_waitcnt vmcnt(8)\n"
"ds_read_b64_tr_b16 v[192:193], %[vtv0] offset:16384\n"
"ds_read_b64_tr_b16 v[194:195], %[vtv0] offset:16640\n"
"ds_read_b64_tr_b16 v[196:197], %[vtv0] offset:20480\n"
"ds_read_b64_tr_b16 v[198:199], %[vtv0] offset:20736\n"
"ds_read_b64_tr_b16 v[200:201], %[vtv1] offset:16384\n"
"ds_read_b64_tr_b16 v[202:203], %[vtv1] offset:16640\n"
"ds_read_b64_tr_b16 v[204:205], %[vtv1] offset:20480\n"
"ds_read_b64_tr_b16 v[206:207], %[vtv1] offset:20736\n"
"ds_read_b64_tr_b16 v[208:209], %[vtv2] offset:16384\n"
"ds_read_b64_tr_b16 v[210:211], %[vtv2] offset:16640\n"
"ds_read_b64_tr_b16 v[212:213], %[vtv2] offset:20480\n"
"ds_read_b64_tr_b16 v[214:215], %[vtv2] offset:20736\n"
"ds_read_b64_tr_b16 v[216:217], %[vtv3] offset:16384\n"
"ds_read_b64_tr_b16 v[218:219], %[vtv3] offset:16640\n"
"ds_read_b64_tr_b16 v[220:221], %[vtv3] offset:20480\n"
"ds_read_b64_tr_b16 v[222:223], %[vtv3] offset:20736\n"
"s_waitcnt lgkmcnt(0)\n"
"s_waitcnt vmcnt(0)\n"
"ds_read_b64_tr_b16 v[224:225], %[vtv0] offset:24576\n"
"ds_read_b64_tr_b16 v[226:227], %[vtv0] offset:24832\n"
"ds_read_b64_tr_b16 v[228:229], %[vtv0] offset:28672\n"
"ds_read_b64_tr_b16 v[230:231], %[vtv0] offset:28928\n"
"ds_read_b64_tr_b16 v[232:233], %[vtv1] offset:24576\n"
"ds_read_b64_tr_b16 v[234:235], %[vtv1] offset:24832\n"
"ds_read_b64_tr_b16 v[236:237], %[vtv1] offset:28672\n"
"ds_read_b64_tr_b16 v[238:239], %[vtv1] offset:28928\n"
"ds_read_b64_tr_b16 v[240:241], %[vtv2] offset:24576\n"
"ds_read_b64_tr_b16 v[242:243], %[vtv2] offset:24832\n"
"ds_read_b64_tr_b16 v[244:245], %[vtv2] offset:28672\n"
"ds_read_b64_tr_b16 v[246:247], %[vtv2] offset:28928\n"
"ds_read_b64_tr_b16 v[248:249], %[vtv3] offset:24576\n"
"ds_read_b64_tr_b16 v[250:251], %[vtv3] offset:24832\n"
"ds_read_b64_tr_b16 v[252:253], %[vtv3] offset:28672\n"
"ds_read_b64_tr_b16 v[254:255], %[vtv3] offset:28928\n"
"s_waitcnt lgkmcnt(0)\n"
"s_barrier\n"
"s_mov_b32 s48, 0\n"
"s_mov_b32 s51, 98566144\n"
"s_mov_b32 s52, 100663296\n"
"s_mov_b32 s53, 105906176\n"
"s_mov_b32 s68, 110100480\n"
"s_mov_b32 s69, 110370816\n"
"s_mov_b32 s66, 0xffff0000\n"
"s_mov_b32 s67, 0xffff0000\n"
"s_memtime s[58:59]\n"
"s_waitcnt lgkmcnt(0)\n"
"LOOP_%=:\n"
"v_mov_b64 v[24:25], 0\n"
"v_mov_b64 v[26:27], 0\n"
"v_mov_b64 v[28:29], 0\n"
"v_mov_b64 v[30:31], 0\n"
"v_mov_b64 v[32:33], 0\n"
"v_mov_b64 v[34:35], 0\n"
"v_mov_b64 v[36:37], 0\n"
"v_mov_b64 v[38:39], 0\n"
"TW_v_%=:\n"
"s_memtime s[60:61]\n"
"s_waitcnt lgkmcnt(0)\n"
"s_sub_u32 s60, s60, s58\n"
"s_cmp_lt_i32 s60, s62\n"
"s_cbranch_scc1 TW_v_%=\n"
"VENTRY_%=:\n"
"buffer_load_dwordx4 v[40:43], %[vm], s[36:39], s51 offen offset:0 sc1\n"
"s_nop 11\n"
"buffer_load_dwordx4 v[44:47], %[vm], s[36:39], s51 offen offset:1024 sc1\n"
"s_nop 11\n"
"buffer_load_dwordx4 v[48:51], %[vm], s[36:39], s51 offen offset:2048 sc1\n"
"s_nop 11\n"
"buffer_load_dwordx4 v[52:55], %[vm], s[36:39], s51 offen offset:3072 sc1\n"
"v_mov_b32 v124, 0\n"
"s_waitcnt vmcnt(3)\n"
"v_smfmac_f32_16x16x64_bf16 v[24:27], v[40:43], v[128:135], %[vidx]\n"
"v_max3_u32 v124, v124, v40, v41\n"
"v_smfmac_f32_16x16x64_bf16 v[28:31], v[40:43], v[136:143], %[vidx]\n"
"v_max3_u32 v124, v124, v42, v43\n"
"v_smfmac_f32_16x16x64_bf16 v[32:35], v[40:43], v[144:151], %[vidx]\n"
"v_cmp_eq_u32 vcc, -1, v124\n"
"v_smfmac_f32_16x16x64_bf16 v[36:39], v[40:43], v[152:159], %[vidx]\n"
"s_cbranch_vccnz VRETRY_%=\n"
"s_waitcnt vmcnt(2)\n"
"v_smfmac_f32_16x16x64_bf16 v[24:27], v[44:47], v[160:167], %[vidx]\n"
"v_max3_u32 v124, v124, v44, v45\n"
"v_smfmac_f32_16x16x64_bf16 v[28:31], v[44:47], v[168:175], %[vidx]\n"
"v_max3_u32 v124, v124, v46, v47\n"
"v_smfmac_f32_16x16x64_bf16 v[32:35], v[44:47], v[176:183], %[vidx]\n"
"v_smfmac_f32_16x16x64_bf16 v[36:39], v[44:47], v[184:191], %[vidx]\n"
"s_waitcnt vmcnt(1)\n"
"v_smfmac_f32_16x16x64_bf16 v[24:27], v[48:51], v[192:199], %[vidx]\n"
"v_max3_u32 v124, v124, v48, v49\n"
"v_smfmac_f32_16x16x64_bf16 v[28:31], v[48:51], v[200:207], %[vidx]\n"
"v_max3_u32 v124, v124, v50, v51\n"
"v_smfmac_f32_16x16x64_bf16 v[32:35], v[48:51], v[208:215], %[vidx]\n"
"v_smfmac_f32_16x16x64_bf16 v[36:39], v[48:51], v[216:223], %[vidx]\n"
"s_waitcnt vmcnt(0)\n"
"v_smfmac_f32_16x16x64_bf16 v[24:27], v[52:55], v[224:231], %[vidx]\n"
"v_max3_u32 v124, v124, v52, v53\n"
"v_smfmac_f32_16x16x64_bf16 v[28:31], v[52:55], v[232:239], %[vidx]\n"
"v_max3_u32 v124, v124, v54, v55\n"
"v_smfmac_f32_16x16x64_bf16 v[32:35], v[52:55], v[240:247], %[vidx]\n"
"v_cmp_eq_u32 vcc, -1, v124\n"
"v_smfmac_f32_16x16x64_bf16 v[36:39], v[52:55], v[248:255], %[vidx]\n"
"s_cbranch_vccnz VRETRY_%=\n"
"VGO_%=:\n"
"v_permlane32_swap_b32 v24, v28\n"
"v_permlane32_swap_b32 v25, v29\n"
"v_permlane32_swap_b32 v26, v30\n"
"v_permlane32_swap_b32 v27, v31\n"
"v_pk_add_f32 v[24:25], v[24:25], v[28:29]\n"
"v_pk_add_f32 v[26:27], v[26:27], v[30:31]\n"
"ds_write_b128 %[vlw], v[24:27]\n"
"v_permlane32_swap_b32 v32, v36\n"
"v_permlane32_swap_b32 v33, v37\n"
"v_permlane32_swap_b32 v34, v38\n"
"v_permlane32_swap_b32 v35, v39\n"
"v_pk_add_f32 v[32:33], v[32:33], v[36:37]\n"
"v_pk_add_f32 v[34:35], v[34:35], v[38:39]\n"
"ds_write_b128 %[vlw], v[32:35] offset:1024\n"
"s_waitcnt lgkmcnt(0)\n"
"s_barrier\n"
"s_cmp_ge_u32 s49, 2\n"
"s_cbranch_scc1 VDONE_%=\n"
"ds_read_b128 v[72:75], %[vlr] offset:0\n"
"ds_read_b128 v[76:79], %[vlr] offset:2048\n"
"ds_read_b128 v[80:83], %[vlr] offset:4096\n"
"ds_read_b128 v[84:87], %[vlr] offset:6144\n"
"s_waitcnt lgkmcnt(2)\n"
"v_pk_add_f32 v[72:73], v[72:73], v[76:77]\n"
"v_pk_add_f32 v[74:75], v[74:75], v[78:79]\n"
"s_waitcnt lgkmcnt(0)\n"
"v_pk_add_f32 v[80:81], v[80:81], v[84:85]\n"
"v_pk_add_f32 v[82:83], v[82:83], v[86:87]\n"
"v_pk_add_f32 v[72:73], v[72:73], v[80:81]\n"
"v_pk_add_f32 v[74:75], v[74:75], v[82:83]\n"
"v_cvt_pk_bf16_f32 v76, v72, v73\n"
"v_cvt_pk_bf16_f32 v77, v74, v75\n"
"buffer_store_dwordx2 v[76:77], %[vvs], s[36:39], s53 offen sc1\n"
"VDONE_%=:\n"
"s_cmp_eq_u32 s48, 63\n"
"s_cbranch_scc1 END_%=\n"
"v_mov_b64 v[24:25], 0\n"
"v_mov_b64 v[26:27], 0\n"
"v_mov_b64 v[28:29], 0\n"
"v_mov_b64 v[30:31], 0\n"
"v_mov_b64 v[32:33], 0\n"
"v_mov_b64 v[34:35], 0\n"
"v_mov_b64 v[36:37], 0\n"
"v_mov_b64 v[38:39], 0\n"
"s_cmp_lg_u32 s49, 3\n"
"s_cbranch_scc1 HB_o_%=\n"
"buffer_load_dword v125, %[vhr], s[36:39], s68 offen sc1\n"
"s_sleep 2\n"
"HL_o_%=:\n"
"buffer_load_dword v126, %[vhr], s[36:39], s68 offen sc1\n"
"s_add_u32 s63, s63, 1\n"
"s_waitcnt vmcnt(1)\n"
"v_cmp_eq_u32 vcc, -1, v125\n"
"s_cmp_ge_u32 s63, 196608\n"
"s_cbranch_scc1 HO_o_%=\n"
"s_cbranch_vccz HO_o_%=\n"
"buffer_load_dword v125, %[vhr], s[36:39], s68 offen sc1\n"
"s_waitcnt vmcnt(1)\n"
"v_cmp_eq_u32 vcc, -1, v126\n"
"s_cbranch_vccnz HL_o_%=\n"
"HO_o_%=:\n"
"s_mov_b32 s60, s57\n"
"HD_o_%=:\n"
"s_sub_u32 s60, s60, 1\n"
"s_cbranch_scc0 HD_o_%=\n"
"HB_o_%=:\n"
"s_barrier\n"
"buffer_store_dword %[vidx], %[vhs], s[36:39], s69 offen sc1\n"
"OENTRY_%=:\n"
"buffer_load_dwordx4 v[40:43], %[vo0], s[36:39], s52 offen offset:0 sc1\n"
"s_nop 11\n"
"buffer_load_dwordx4 v[44:47], %[vo0], s[36:39], s52 offen offset:1024 sc1\n"
"s_nop 11\n"
"buffer_load_dwordx4 v[48:51], %[vo0], s[36:39], s52 offen offset:2048 sc1\n"
"s_nop 11\n"
"buffer_load_dwordx4 v[52:55], %[vo0], s[36:39], s52 offen offset:3072 sc1\n"
"s_nop 11\n"
"buffer_load_dwordx4 v[56:59], %[vo1], s[36:39], s52 offen offset:0 sc1\n"
"s_nop 11\n"
"buffer_load_dwordx4 v[60:63], %[vo1], s[36:39], s52 offen offset:1024 sc1\n"
"s_nop 11\n"
"buffer_load_dwordx4 v[64:67], %[vo1], s[36:39], s52 offen offset:2048 sc1\n"
"s_nop 11\n"
"buffer_load_dwordx4 v[68:71], %[vo1], s[36:39], s52 offen offset:3072 sc1\n"
"v_mov_b32 v124, 0\n"
"s_waitcnt vmcnt(7)\n"
"v_smfmac_f32_16x16x64_bf16 v[24:27], v[40:43], a[0:7], %[vidx]\n"
"v_max3_u32 v124, v124, v40, v41\n"
"v_smfmac_f32_16x16x64_bf16 v[28:31], v[40:43], a[8:15], %[vidx]\n"
"v_max3_u32 v124, v124, v42, v43\n"
"v_smfmac_f32_16x16x64_bf16 v[32:35], v[40:43], a[16:23], %[vidx]\n"
"v_cmp_eq_u32 vcc, -1, v124\n"
"v_smfmac_f32_16x16x64_bf16 v[36:39], v[40:43], a[24:31], %[vidx]\n"
"s_cbranch_vccnz ORETRY_%=\n"
"s_waitcnt vmcnt(6)\n"
"v_smfmac_f32_16x16x64_bf16 v[24:27], v[44:47], a[32:39], %[vidx]\n"
"v_max3_u32 v124, v124, v44, v45\n"
"v_smfmac_f32_16x16x64_bf16 v[28:31], v[44:47], a[40:47], %[vidx]\n"
"v_max3_u32 v124, v124, v46, v47\n"
"v_smfmac_f32_16x16x64_bf16 v[32:35], v[44:47], a[48:55], %[vidx]\n"
"v_smfmac_f32_16x16x64_bf16 v[36:39], v[44:47], a[56:63], %[vidx]\n"
"s_waitcnt vmcnt(5)\n"
"v_smfmac_f32_16x16x64_bf16 v[24:27], v[48:51], a[64:71], %[vidx]\n"
"v_max3_u32 v124, v124, v48, v49\n"
"v_smfmac_f32_16x16x64_bf16 v[28:31], v[48:51], a[72:79], %[vidx]\n"
"v_max3_u32 v124, v124, v50, v51\n"
"v_smfmac_f32_16x16x64_bf16 v[32:35], v[48:51], a[80:87], %[vidx]\n"
"v_smfmac_f32_16x16x64_bf16 v[36:39], v[48:51], a[88:95], %[vidx]\n"
"s_waitcnt vmcnt(4)\n"
"v_smfmac_f32_16x16x64_bf16 v[24:27], v[52:55], a[96:103], %[vidx]\n"
"v_max3_u32 v124, v124, v52, v53\n"
"v_smfmac_f32_16x16x64_bf16 v[28:31], v[52:55], a[104:111], %[vidx]\n"
"v_max3_u32 v124, v124, v54, v55\n"
"v_smfmac_f32_16x16x64_bf16 v[32:35], v[52:55], a[112:119], %[vidx]\n"
"v_smfmac_f32_16x16x64_bf16 v[36:39], v[52:55], a[120:127], %[vidx]\n"
"s_waitcnt vmcnt(3)\n"
"v_smfmac_f32_16x16x64_bf16 v[24:27], v[56:59], a[128:135], %[vidx]\n"
"v_max3_u32 v124, v124, v56, v57\n"
"v_smfmac_f32_16x16x64_bf16 v[28:31], v[56:59], a[136:143], %[vidx]\n"
"v_max3_u32 v124, v124, v58, v59\n"
"v_smfmac_f32_16x16x64_bf16 v[32:35], v[56:59], a[144:151], %[vidx]\n"
"v_smfmac_f32_16x16x64_bf16 v[36:39], v[56:59], a[152:159], %[vidx]\n"
"s_waitcnt vmcnt(2)\n"
"v_smfmac_f32_16x16x64_bf16 v[24:27], v[60:63], a[160:167], %[vidx]\n"
"v_max3_u32 v124, v124, v60, v61\n"
"v_smfmac_f32_16x16x64_bf16 v[28:31], v[60:63], a[168:175], %[vidx]\n"
"v_max3_u32 v124, v124, v62, v63\n"
"v_smfmac_f32_16x16x64_bf16 v[32:35], v[60:63], a[176:183], %[vidx]\n"
"v_smfmac_f32_16x16x64_bf16 v[36:39], v[60:63], a[184:191], %[vidx]\n"
"s_waitcnt vmcnt(1)\n"
"v_smfmac_f32_16x16x64_bf16 v[24:27], v[64:67], a[192:199], %[vidx]\n"
"v_max3_u32 v124, v124, v64, v65\n"
"v_smfmac_f32_16x16x64_bf16 v[28:31], v[64:67], a[200:207], %[vidx]\n"
"v_max3_u32 v124, v124, v66, v67\n"
"v_smfmac_f32_16x16x64_bf16 v[32:35], v[64:67], a[208:215], %[vidx]\n"
"v_smfmac_f32_16x16x64_bf16 v[36:39], v[64:67], a[216:223], %[vidx]\n"
"s_waitcnt vmcnt(0)\n"
"v_smfmac_f32_16x16x64_bf16 v[24:27], v[68:71], a[224:231], %[vidx]\n"
"v_max3_u32 v124, v124, v68, v69\n"
"v_smfmac_f32_16x16x64_bf16 v[28:31], v[68:71], a[232:239], %[vidx]\n"
"v_max3_u32 v124, v124, v70, v71\n"
"v_smfmac_f32_16x16x64_bf16 v[32:35], v[68:71], a[240:247], %[vidx]\n"
"v_cmp_eq_u32 vcc, -1, v124\n"
"v_smfmac_f32_16x16x64_bf16 v[36:39], v[68:71], a[248:255], %[vidx]\n"
"s_cbranch_vccnz ORETRY_%=\n"
"OGO_%=:\n"
"s_nop 1\n"
"v_cndmask_b32 v24, v24, v32, s[66:67]\n"
"v_cndmask_b32 v25, v25, v33, s[66:67]\n"
"v_cndmask_b32 v26, v26, v34, s[66:67]\n"
"v_cndmask_b32 v27, v27, v35, s[66:67]\n"
"v_cndmask_b32 v28, v28, v36, s[66:67]\n"
"v_cndmask_b32 v29, v29, v37, s[66:67]\n"
"v_cndmask_b32 v30, v30, v38, s[66:67]\n"
"v_cndmask_b32 v31, v31, v39, s[66:67]\n"
"v_permlane32_swap_b32 v24, v28\n"
"v_permlane32_swap_b32 v25, v29\n"
"v_permlane32_swap_b32 v26, v30\n"
"v_permlane32_swap_b32 v27, v31\n"
"v_pk_add_f32 v[24:25], v[24:25], v[28:29]\n"
"v_pk_add_f32 v[26:27], v[26:27], v[30:31]\n"
"ds_write_b128 %[vlw], v[24:27] offset:8192\n"
"s_waitcnt lgkmcnt(0)\n"
"s_barrier\n"
"s_cmp_lg_u32 s49, 0\n"
"s_cbranch_scc1 ODONE_%=\n"
"ds_read2_b32 v[72:73], %[vfr] offset0:0 offset1:4\n"
"ds_read2_b32 v[74:75], %[vfr] offset0:64 offset1:68\n"
"v_add_u32 v125, 2048, %[vfr]\n"
"ds_read2_b32 v[76:77], v125 offset0:0 offset1:4\n"
"ds_read2_b32 v[78:79], v125 offset0:64 offset1:68\n"
"v_add_u32 v125, 4096, %[vfr]\n"
"ds_read2_b32 v[80:81], v125 offset0:0 offset1:4\n"
"ds_read2_b32 v[82:83], v125 offset0:64 offset1:68\n"
"v_add_u32 v125, 6144, %[vfr]\n"
"ds_read2_b32 v[84:85], v125 offset0:0 offset1:4\n"
"ds_read2_b32 v[86:87], v125 offset0:64 offset1:68\n"
"s_waitcnt lgkmcnt(6)\n"
"v_pk_add_f32 v[72:73], v[72:73], v[74:75]\n"
"s_waitcnt lgkmcnt(4)\n"
"v_pk_add_f32 v[76:77], v[76:77], v[78:79]\n"
"v_pk_add_f32 v[72:73], v[72:73], v[76:77]\n"
"s_waitcnt lgkmcnt(2)\n"
"v_pk_add_f32 v[80:81], v[80:81], v[82:83]\n"
"v_pk_add_f32 v[72:73], v[72:73], v[80:81]\n"
"s_waitcnt lgkmcnt(0)\n"
"v_pk_add_f32 v[84:85], v[84:85], v[86:87]\n"
"v_pk_add_f32 v[72:73], v[72:73], v[84:85]\n"
"s_add_u32 s96, s51, 20480\n"
"v_cvt_pk_bf16_f32 v76, v72, v73\n"
"buffer_store_dword v76, %[vms], s[36:39], s96 offen sc1\n"
"ODONE_%=:\n"
"s_memtime s[58:59]\n"
"s_waitcnt lgkmcnt(0)\n"
"s_mov_b32 s65, 0\n"
"s_mov_b32 s64, 0\n"
"s_add_u32 s68, s68, 0x1000\n"
"s_add_u32 s69, s69, 0x2000\n"
"s_add_u32 s48, s48, 1\n"
"s_add_u32 s51, s51, 20480\n"
"s_add_u32 s52, s52, 73728\n"
"s_add_u32 s53, s53, 0x10000\n"
"s_branch LOOP_%=\n"
"VRETRY_%=:\n"
"s_waitcnt vmcnt(0)\n"
"s_add_u32 s63, s63, 1\n"
"s_add_u32 s65, s65, 1\n"
"s_cmp_ge_u32 s63, 196608\n"
"s_cbranch_scc1 VGO_%=\n"
"v_mov_b64 v[24:25], 0\n"
"v_mov_b64 v[26:27], 0\n"
"v_mov_b64 v[28:29], 0\n"
"v_mov_b64 v[30:31], 0\n"
"v_mov_b64 v[32:33], 0\n"
"v_mov_b64 v[34:35], 0\n"
"v_mov_b64 v[36:37], 0\n"
"v_mov_b64 v[38:39], 0\n"
"s_branch VENTRY_%=\n"
"ORETRY_%=:\n"
"s_waitcnt vmcnt(0)\n"
"s_add_u32 s63, s63, 1\n"
"s_add_u32 s64, s64, 1\n"
"s_cmp_ge_u32 s63, 196608\n"
"s_cbranch_scc1 OGO_%=\n"
"v_mov_b64 v[24:25], 0\n"
"v_mov_b64 v[26:27], 0\n"
"v_mov_b64 v[28:29], 0\n"
"v_mov_b64 v[30:31], 0\n"
"v_mov_b64 v[32:33], 0\n"
"v_mov_b64 v[34:35], 0\n"
"v_mov_b64 v[36:37], 0\n"
"v_mov_b64 v[38:39], 0\n"
"s_branch OENTRY_%=\n"
"END_%=:\n"
"s_waitcnt vmcnt(0)\n"
  :: [ws] "s"(ws), [wv] "s"(Wv), [wo] "s"(Wo), [wave] "s"(wave), [w0] "s"(w0), [w1] "s"(w1), [dbgo] "s"(dbgo),
     [vm] "v"(vm), [vidx] "v"(vidx), [vso] "v"(vso), [vsv0] "v"(vsv0), [vsv1] "v"(vsv1), [vto0] "v"(vto0), [vto1] "v"(vto1),
     [vtv0] "v"(vtv0), [vtv1] "v"(vtv1), [vtv2] "v"(vtv2), [vtv3] "v"(vtv3), [vlw] "v"(vlw), [vlr] "v"(vlr), [vvs] "v"(vvs),
     [vo0] "v"(vo0), [vo1] "v"(vo1), [vfr] "v"(vfr), [vms] "v"(vms), [vhr] "v"(vhr), [vhs] "v"(vhs)
  : "v24","v25","v26","v27","v28","v29","v30","v31","v32","v33","v34","v35","v36","v37","v38","v39","v40","v41","v42","v43","v44","v45","v46","v47","v48","v49","v50","v51","v52","v53","v54","v55",
"v56","v57","v58","v59","v60","v61","v62","v63","v64","v65","v66","v67","v68","v69","v70","v71","v72","v73","v74","v75","v76","v77","v78","v79","v80","v81","v82","v83","v84","v85","v86","v87",
"v88","v89","v90","v91","v92","v93","v94","v95","v96","v97","v98","v99","v100","v101","v102","v103","v104","v105","v106","v107","v108","v109","v110","v111","v112","v113","v114","v115","v116","v117","v118","v119",
"v120","v121","v122","v123","v124","v125","v126","v127","v128","v129","v130","v131","v132","v133","v134","v135","v136","v137","v138","v139","v140","v141","v142","v143","v144","v145","v146","v147","v148","v149","v150","v151",
"v152","v153","v154","v155","v156","v157","v158","v159","v160","v161","v162","v163","v164","v165","v166","v167","v168","v169","v170","v171","v172","v173","v174","v175","v176","v177","v178","v179","v180","v181","v182","v183",
"v184","v185","v186","v187","v188","v189","v190","v191","v192","v193","v194","v195","v196","v197","v198","v199","v200","v201","v202","v203","v204","v205","v206","v207","v208","v209","v210","v211","v212","v213","v214","v215",
"v216","v217","v218","v219","v220","v221","v222","v223","v224","v225","v226","v227","v228","v229","v230","v231","v232","v233","v234","v235","v236","v237","v238","v239","v240","v241","v242","v243","v244","v245","v246","v247",
"v248","v249","v250","v251","v252","v253","v254","v255","a0","a1","a2","a3","a4","a5","a6","a7","a8","a9","a10","a11","a12","a13","a14","a15","a16","a17","a18","a19","a20","a21","a22","a23",
"a24","a25","a26","a27","a28","a29","a30","a31","a32","a33","a34","a35","a36","a37","a38","a39","a40","a41","a42","a43","a44","a45","a46","a47","a48","a49","a50","a51","a52","a53","a54","a55",
"a56","a57","a58","a59","a60","a61","a62","a63","a64","a65","a66","a67","a68","a69","a70","a71","a72","a73","a74","a75","a76","a77","a78","a79","a80","a81","a82","a83","a84","a85","a86","a87",
"a88","a89","a90","a91","a92","a93","a94","a95","a96","a97","a98","a99","a100","a101","a102","a103","a104","a105","a106","a107","a108","a109","a110","a111","a112","a113","a114","a115","a116","a117","a118","a119",
"a120","a121","a122","a123","a124","a125","a126","a127","a128","a129","a130","a131","a132","a133","a134","a135","a136","a137","a138","a139","a140","a141","a142","a143","a144","a145","a146","a147","a148","a149","a150","a151",
"a152","a153","a154","a155","a156","a157","a158","a159","a160","a161","a162","a163","a164","a165","a166","a167","a168","a169","a170","a171","a172","a173","a174","a175","a176","a177","a178","a179","a180","a181","a182","a183",
"a184","a185","a186","a187","a188","a189","a190","a191","a192","a193","a194","a195","a196","a197","a198","a199","a200","a201","a202","a203","a204","a205","a206","a207","a208","a209","a210","a211","a212","a213","a214","a215",
"a216","a217","a218","a219","a220","a221","a222","a223","a224","a225","a226","a227","a228","a229","a230","a231","a232","a233","a234","a235","a236","a237","a238","a239","a240","a241","a242","a243","a244","a245","a246","a247",
"a248","a249","a250","a251","a252","a253","a254","a255","s36","s37","s38","s39","s40","s41","s42","s43","s44","s45","s46","s47","s48","s49","s50","s51","s52","s53","s54","s55","s56","s57","s58","s59",
"s60","s61","s62","s63","s64","s65","s66","s67","s68","s69","s70","s71","s72","s73","s74","s75","s76","s77","s78","s79","s80","s81","s82","s83","s84","s85","s86","s87","s88","s89","s90","s91",
"s92","s93","s94","s95","s96","s97","s98","s99","vcc","scc","memory");
}

typedef unsigned u32x4 __attribute__((ext_vector_type(4)));
typedef unsigned u32x2 __attribute__((ext_vector_type(2)));
typedef float f32x4 __attribute__((ext_vector_type(4)));
typedef __bf16 bf16x8 __attribute__((ext_vector_type(8)));
typedef short s16x4 __attribute__((ext_vector_type(4)));
typedef __bf16 bf16x2 __attribute__((ext_vector_type(2)));
typedef float f32x2 __attribute__((ext_vector_type(2)));
#define GAS __attribute__((address_space(1)))
#define LAS __attribute__((address_space(3)))
constexpr int SC1 = 16;
constexpr unsigned SPIN = 1u << 17;
__device__ const unsigned k_items[2492] = {
278922194,243319765,281019346,245400533,283116498,247481301,2097160,2162720,2097312,196768,393308,4277065,344252,4310153,138416331,134267226,
49532,134267261,138417934,268452850,136321454,99284,234980309,2211848,2244672,2277568,136331290,138413131,136347677,376988,311516,213276,
138417483,2164457,49564,134218141,136321710,164852,237061077,2310152,4210720,2343072,49312,114780,136331389,134217885,140525789,2098729,
213372,136321771,134283722,98764,312308,239125493,2424840,4292672,4325568,138461210,138413259,138461245,98492,4195465,134250749,2115113,
344412,2180937,136348106,311756,443380,241222613,2523144,4390976,4423872,140558394,278620,4260681,138413454,138415883,134218042,138415310,
134250813,138527146,688556,136322059,276874226,136321422,541684,243287029,278922226,243319797,281019378,245400565,283116530,247481333,4194312,2179104,
2113696,114880,409692,138412110,138413198,138415243,4195593,138415374,136331581,140607914,836012,136322411,268485618,270540299,234963925,4309000,
2261056,2293952,136347674,138413451,136314941,393372,327900,229660,138417867,134218154,115116,134316461,270615506,115700,237061109,4407304,
4227104,4194464,213184,131164,136347773,134234269,140542173,2180649,229756,136321931,134300106,115148,328660,239141845,4521992,4309056,
4341952,140509210,138413643,140509245,114876,4277385,134267133,2197033,360796,136321643,136380874,328140,239158229,4620296,4407360,4440256,
4194761,138413323,140542013,196796,131292,138416971,140558682,442748,136321835,138445258,475596,558036,243303381,278938578,243336149,281035730,
245416917,283132882,247497685,2113544,2195488,2130080,131264,426076,138412430,138413518,138415563,4277513,138415758,136347965,140624298,852396,
2099177,268518354,270540555,234963957,2228232,2277440,64,136314938,138413835,136331325,409756,344284,246044,2098857,136321035,136331613,
138417678,270533586,136321326,197620,136322094,2326536,4243488,4210848,229568,147548,4227913,49340,4261001,138416459,136364410,138416590,
115100,136331677,136321742,263156,239141877,2441224,4325440,114752,138444858,138413963,136315034,4293577,138415435,140542266,138415950,2180713,
295292,136321803,2131849,138412445,136321934,410612,241222645,2539528,4423744,163904,4276681,138413707,140558397,213180,147676,4228489,
134218045,140509610,704940,136322219,276890578,270540427,243270645,278938610,243336181,281035762,245416949,283132914,247497717,4210696,2097216,2146464,
147648,442460,138412814,138413582,138415627,136364253,134250874,65916,2131785,138494410,688588,99316,234980341,4325384,2293824,16448,
136331322,4211337,136347709,426140,360668,262428,2180777,136321195,136347997,138417998,270533618,136321486,213972,136322254,4423688,2310208,
4227232,245952,163932,4309833,65724,377052,311580,2164361,134332842,328108,136322155,272679890,136321198,295892,136322126,4538376,
4341824,131136,138461242,4244105,136331418,134217946,138414478,4195657,138415566,136364442,2148137,475564,136322187,274777074,136321390,443348,
136322158,4636680,4440128,180288,138444890,4211401,138461309,138428573,134267194,138415694,134267197,140525994,721324,136322379,276890610,270540683,
243286997,278954962,243352533,281052114,245433301,283149266,247514069,2129928,2113600,2162848,114912,376956,138414155,138412282,136315162,4277705,
2180681,82268,2213705,138510794,704972,234882005,2244616,65536,32832,136347706,4293257,140525722,138413966,138416011,138428730,2213385,
2147977,2213577,16796,134234525,136321870,181204,136322414,2342920,2326592,4243616,213216,180316,138412238,82108,393436,327964,
138417227,136315306,344492,136322315,272679922,136321358,295924,136322286,2457608,163840,147520,140509242,196700,138412686,138413390,136347837,
134217949,140525853,138412458,491948,136322347,274793426,270540395,241206229,2555912,212992,196672,138461274,4293321,140509309,136364221,4261129,
49468,138461594,140542301,138477997,276923346,492500,243303413,278954994,243352565,281052146,245433333,283149298,247514101,4227080,2129984,2179232,
131296,393340,138414475,138428666,136331546,2115081,2213481,16764,136321547,138527178,721356,234882037,4341768,81920,49216,4227529,
98364,140542106,4260809,295132,196924,98652,136348029,134218157,270615538,132052,236985902,4440072,2342976,2310336,229600,98428,
138412622,138413326,134267069,140558557,138445085,136364458,360876,136322475,272696274,136321518,312276,136322446,4554760,180224,4260064,4260297,
294972,136347802,134234330,138414862,4277577,138415630,138412442,138445149,136429997,274809842,377812,136322318,4653064,229376,4358368,140509274,
140525597,229564,164060,4310409,134234429,2131721,140542333,138494381,276923378,492532,136322190,278971346,243368917,281068498,245449685,283165650,
247530453,2146312,2146368,2195616,147680,409724,138414859,4260873,138414670,138417035,134218106,82300,136321707,140509642,268436434,138478029,
2261000,98304,2212064,4309449,114748,140558490,138445018,4277449,4261257,2197065,115068,2148169,140575178,819660,214004,236986158,
2359304,4194368,2326720,245984,114812,138412942,138413710,4244553,4211913,278812,138417547,136331690,134218186,131532,328692,236985934,
2473992,196608,4276448,136364122,4195017,4244297,138413070,4260937,138414158,138416203,138445178,328060,136321963,2213769,138428829,136321070,
426964,136322478,2572296,245760,4374752,138444922,140541981,245948,180444,284,140509594,140558685,138510765,276939730,508884,136322350,
278971378,243368949,281068530,245449717,283165682,247530485,4243464,2162752,2097344,4194313,140525658,442492,4211593,4211785,138414990,4244873,
136315197,2148105,134267229,140542381,268534738,33780,136322062,4358152,2211936,2228448,134217818,16508,134267005,140525725,138445021,138412317,
134250922,131500,2164681,270566354,270540331,237044693,4456456,4210752,2343104,4227081,136347738,163964,138414283,140558586,4293833,295196,
138417931,136348074,134234570,147916,237077461,4571144,4259936,4292832,138412122,4276937,138412302,138413774,138415115,140558650,4244937,2098825,
2164425,229788,138417422,274744274,270540651,241206261,4669448,4358240,4391136,138461306,140558365,262332,98556,16668,2115209,2131689,
2164521,672172,136322539,276906962,136397261,278987730,243385301,281084882,245466069,283182034,247546837,2162696,2179136,2113728,4276233,140542042,
4227785,140558461,138444957,138428637,134267258,138416142,393628,140525981,136321998,66548,136322222,2277384,2228320,2244832,134234202,32892,
4211529,393404,278780,213308,115036,136321579,140591562,836044,234996693,2375688,4227136,4194496,4309001,136315002,180348,138414667,
4211721,138414414,138416843,138412410,138416654,131484,136348061,136321902,279508,236986190,2490376,4276320,4309216,138428506,138444829,131260,
220,4212105,2147913,278908,2131817,2164649,2197449,274760690,134332877,2588680,4374624,4407520,140509306,140525629,138413134,4211849,
136331517,2131497,442716,136321995,138461642,491980,558068,136322510,278987762,243385333,281084914,245466101,283182066,247546869,4259848,2195520,
2130112,4227145,140558426,4309705,4195145,360636,196828,131356,140542362,134218077,140558765,268534770,50132,136322382,4374536,2244704,
2261216,134250586,49276,4293449,409788,295164,229692,131420,136321739,140607946,852428,234996725,4472840,4243520,4210880,4259913,
136331386,4260553,136315005,134250653,140525821,138461469,136380842,134250954,164300,237077493,4587528,4292704,4325600,136364154,138461213,147644,
16604,4294025,2098793,311676,2213737,136364445,136321230,426996,236985966,4685832,4391008,4423904,4211209,278652,138412046,138413838,
4293769,136347901,2213417,377212,2148201,2099113,2131913,276857842,136413645,279004114,245367765,281101266,245482453,283198418,247563221,2179080,
0,2146496,4309065,140525690,134217757,311452,213212,147740,140558746,134234461,140575149,268551122,50164,136322542,2293768,2261088,
2277600,134266970,65660,138412174,426172,311548,246076,147804,136321899,140624330,868812,236979157,2392072,114688,4227264,4194441,
136347770,136364061,49308,409820,344348,2115241,136321387,138428797,136315309,272712690,230356,2506760,4309088,4341984,138412154,140509213,
164028,32988,377116,138417291,2197225,246172,138417806,274744306,136315341,2605064,4407392,4440288,4293129,295036,138412366,4228041,
98524,33052,2197129,2213609,140525917,138527149,276939762,508916,236985998,279004146,245367797,281101298,245482485,283198450,247563253,4276232,
16384,2162880,4243593,140542074,134234141,327836,229596,164124,2131593,2148073,2180905,803244,2181097,268518386,138494413,4390920,
2277472,2293984,134217850,82044,138412494,442556,327932,262460,164188,2099049,2099081,2131881,134283693,270631890,132084,4489224,
131072,4243648,4276361,4260361,138413899,136364093,65692,426204,360732,2197161,98716,138417358,272630770,270540363,239109109,4603912,
4325472,114784,138428538,138444861,180412,49372,393500,138417611,136397226,508332,136322507,274793458,136331725,4702216,4423776,163936,
138412235,311420,138412750,4309961,114908,49436,138417355,138477994,136397258,508364,241238997,279020498,245384149,281117650,247464917,283214802,
247579605,2195464,32768,2179264,4194505,140558458,134250525,344220,245980,180508,2213513,140542378,868780,557516,234898389,2211880,
2293856,96,134234234,4244169,134250621,140542109,138461405,138428701,134267306,147884,136322123,270566386,270540587,237044725,2408456,147456,
65600,4227273,138412107,4227721,134234266,138414030,138415371,140509498,4228553,136315290,136364381,136331693,272729042,230388,2424872,4341856,
131168,4194825,229468,138413006,4211657,138415819,4244745,138415246,138428826,138461533,138412461,274826194,377844,236986222,2523176,4440160,
180320,138412619,327804,138414091,4244489,138414222,4212041,65852,138417739,138494378,136413642,524748,241239029,279020530,245384181,281117682,
247464949,283214834,247579637,4292616,49152,2195648,4276425,4227593,138413387,134266909,360604,262364,98620,138417419,140558762,885164,
573900,234898421,4309032,2211968,16480,134250618,136314909,442524,344316,138415118,2147945,147836,2180969,2181001,2213801,134300077,
270631922,148436,4505608,2310240,81984,4309193,138412427,4309641,134250650,4195273,138415755,4228361,138415182,2131529,246140,2115433,
2115465,2148265,2181065,272663506,270540619,239125461,4522024,4259968,147552,4276745,245852,138414347,134234362,4228297,138416523,138461562,
344444,377260,2148329,274809810,136348109,4620328,4358272,196704,138412939,344188,138414411,136364189,136315101,134218013,2213641,140558717,
140509613,136321646,476116,236986254,279036882,236986030,281134034,236986062,283231186,236986094,2097192,2097248,213056,4210953,4309513,138413771,
134217789,278716,196860,115004,138417803,140575146,901548,590284,234914773,2228264,2228352,32864,134267002,136331293,377020,360700,
138415438,134218138,65948,134250909,136322030,181236,2310184,2326624,98368,4243721,138412811,98396,138414987,4293641,138414798,4195721,
138461501,2098953,136364413,136348077,272729074,246740,2441256,4276352,65664,138412171,262236,138414731,134250746,4310217,138416907,140509562,
360828,393644,197068,239158261,2539560,4374656,114816,4244041,360572,138414795,138412189,136331485,134234397,136321131,2115401,138412490,
541132,241255381,279036914,236986286,281134066,236986318,283231218,236986350,4194344,2113632,229440,4292873,138412299,4194953,134250557,295100,
213244,131388,138418123,140591530,138478026,268436466,138510797,4325416,2244736,49248,4243977,16476,138412558,138413262,4244617,4244809,
2131465,138417163,134234538,164268,136322283,270582738,140542413,4407336,2343008,2310368,4194633,4211273,131196,4244361,134250685,140542205,
140509469,2180873,138412413,136364461,272745426,246772,4538408,4292736,82048,138412491,196732,138415051,134267130,140525850,2164233,2180745,
2115305,262556,138418126,274760658,136364493,4636712,4391040,131200,376860,138428570,136315098,138414926,4293961,82236,138418059,138510762,
136430026,276825042,136430029,270540459,270540491,270540523,2113576,2130016,245824,4243785,138412683,4276873,134266941,311484,229628,147772,
2164393,377244,140542365,136321134,82900,236985870,2244648,2261120,213120,138412043,32860,138412878,138413646,278748,138415822,134234522,
82332,134267293,136321166,197588,2326568,4194400,2326752,4276553,4293193,147580,156,442588,278844,196956,2197353,2197385,
136315293,136321038,279540,2457640,4309120,98432,138412555,213116,4260745,136331453,134234333,140542237,138428842,524716,213452,239174613,
2555944,4407424,147584,393244,136364218,136331482,4244681,138416651,140525946,138416398,295324,138445213,136321966,525268,270540715,270540747,
270540779,4210728,2146400,2097376,4260233,138413003,376924,4293513,4293705,4261065,114972,348,136321867,140526026,268452818,138527181,
4341800,2277504,229504,138412363,49244,138414219,138461434,136364314,136315226,164220,428,136322443,270582770,140558797,4423720,4210784,
2343136,4211081,196636,134267034,4277193,138416075,4310281,138415502,2213449,262524,197036,2131945,272696306,134218189,4554792,4325504,
65696,138412875,229500,98460,65756,409884,138417675,136413610,541100,229836,239174645,4653096,4423808,114848,409628,138412218,
136347866,134218010,138416014,2164297,393596,557484,2164713,276906994,138412493,574420,689108,803796,2129960,2162784,2113760,134217754,
81948,138444954,138413902,138415947,138412253,2147881,65884,136322027,2164617,2197417,136322091,268501970,140509645,2261032,2293888,245888,
138412747,65628,138414539,140509434,138412314,136331610,180604,16812,2115561,270599122,140575181,2342952,4227168,4194528,4293001,213020,
134217914,140525786,138445082,4310473,136331674,138412381,136380845,272745458,263124,2474024,4341888,82080,4227657,245884,114844,82140,
426268,138417995,136429994,134332874,344524,239190997,2572328,4440192,131232,426012,138428602,136315130,134234394,4261321,2115177,409980,
573868,377292,241255413,574452,689140,803828,4227112,2179168,2130144,134234138,60,138461338,4244425,4228233,138416715,134234490,
138416462,410012,140558749,136321294,82932,236986126,4358184,65568,213152,4194889,82012,138414603,4195337,138414350,138416395,136348026,
138416206,136321355,136315261,134234541,270648274,148468,4440104,4243552,4210912,136364058,229436,134234298,140542170,138461466,2147849,136348058,
138428765,2099145,272663538,134234573,4571176,163872,98464,4309577,262268,131228,252,442652,2131625,136321099,138461565,138428845,
274826226,394196,4669480,213024,147616,442396,4227849,245916,114940,65820,2148009,278940,138461597,136321102,525300,590804,
705492,820180,2146344,2195552,2146528,134250522,16444,140509338,136364250,136347930,2197001,140525978,134250845,140591533,268551154,66516,
2277416,81952,229536,4276809,124,138414923,4277257,138414734,138416779,2164265,98684,33196,2197481,270599154,140591565,2359336,
2310272,4227296,138412058,245820,134250682,140558554,140509466,136364378,138416974,147868,138417742,272647122,134250957,2490408,180256,192,
278556,136315066,134250714,140542234,138445146,138416334,136321419,140509565,138445229,274842578,394228,2588712,229408,65728,376892,4309769,
262300,131324,82204,377180,590252,393676,241271765,590836,705524,820212,4243496,2097280,2162912,134266906,32828,138444986,
138412250,136315194,134218074,138416846,426396,138417230,268469202,140526029,4374568,98336,245920,98332,140525754,138461402,138428698,136347994,
138416526,136321515,136331645,134250925,270648306,164820,4456488,2326656,4243680,138428442,262204,134267066,140525818,138445114,138412378,2148041,
180636,138418062,272647154,134267341,4587560,196640,16576,294940,136331450,134267098,140558618,138461530,138416718,197020,136397229,274842610,
410580,4685864,245792,82112,393276,138444893,138412221,136347869,134250781,136321291,2197321,138428874,276825074,138428877,607188,721876,
836564,2162728,2113664,2179296,134217786,49212,138461370,138428634,136331578,134234458,2115273,442780,138417550,268469234,980,2293800,
2212000,164032,114716,140542138,140509402,136364346,136315258,138416910,412,138417294,270549970,140607949,2375720,2343040,65632,136364090,
138413195,138412093,82076,377084,295228,213340,213420,2213865,272712658,134283725,2506792,4260000,32960,311324,136347834,134217978,
140525882,140509530,138417038,213404,136413613,136321614,361428,2605096,4358304,98496,409660,138461277,138428605,136315133,134267165,136321451,
136321675,2148233,2181033,2213833,276874194,138445261,607220,721908,836596,4259880,2130048,2195680,134234170,65596,140509370,136364282,
136347962,134250842,2197193,2098985,819628,606668,234914805,4390952,2228384,180416,131100,140558522,138445050,138412346,136331642,2131657,
33180,138417614,270550002,140624333,4472872,4194432,82016,138412090,138413515,138428477,188,393468,311612,229724,229804,460,
237093845,4603944,4276384,49344,327708,4211465,163996,16636,377148,2213545,136321259,2099017,136364490,360908,239191029,4702248,
4374688,65760,426044,140509277,4195401,138414542,138416267,140542330,138416782,311708,140509597,136321262,541652,623572,738260,852948,
2179112,2146432,213088,134250554,81980,4244233,295068,246012,164156,16732,2164585,2115497,136322251,268502002,1012,2211872,
2244768,196800,131132,4260617,4309897,138415307,4211977,4212169,134250906,2115369,82348,737740,236979189,2392104,4210816,98400,
138428474,138413579,134217882,140542202,138461498,138428762,2098921,2131753,295340,16844,237093877,4259872,4292768,224,311356,4293385,
180380,33020,393532,278876,410028,246220,241173461,4358176,4391072,82144,138414027,136364186,136331514,134250778,2098697,2197097,
426364,606636,410060,241271797,623604,738292,852980,4276264,2162816,229472,134266938,138413067,134234173,327868,262396,180540,
33116,737708,623052,234931157,2228256,2261152,164064,147516,134217821,140558493,140509405,136364349,134283690,180652,754124,236995541,
4489256,4227200,128,4243913,229404,4195081,16540,409852,327996,246108,246188,33228,239076309,4276256,4309152,16608,
327740,136364125,4228105,65788,409916,295260,426412,262604,241173493,4374560,4407456,98528,4260489,138412186,136347898,134267162,
2180617,138445210,140525949,140525997,136321806,476148,639956,754644,869332,2195496,2179200,245856,4211145,4260425,426108,278684,
138414286,98588,49500,754092,639436,234931189,2244640,2277536,180448,163900,134234205,140525757,138445053,138412349,134300074,140542410,
885196,236995573,2408488,4243584,16512,136314970,138412061,16572,426236,344380,262492,262572,49612,239076341,4292640,4325536,
32992,344124,138412125,4310025,82172,426300,311644,442796,278988,241189845,4390944,4423840,4259849,4194569,140541978,295004,
4195209,138415179,134234426,140525914,138417102,328092,138417166,276841426,138461645,639988,754676,869364,4292648,2195584,163968,4293065,
28,140525661,138461341,136364285,136315165,136321163,134218109,140607917,136321678,17396,2261024,2293920,196832,180284,134250589,140542141,
138461437,138428733,134316458,140558794,901580,237011925,4505640,114720,32896,136331354,138428445,32956,442620,360764,196988,278956,
65996,239092693,4309024,4341920,49376,360508,138428509,136315037,134250717,140558621,138445226,136315338,274727890,136380877,4407328,4440224,
4210761,4276489,140558362,311388,4277129,138415499,134250810,140542298,2098889,344476,138417486,276841458,459732,656340,771028,885716,
2097184,32,180352,16412,140542045,140509341,138412285,136331549,136321323,134234493,140624301,136321838,33748,2277408,2212032,4210697,
4210889,4260169,147484,134266973,140558525,140509437,2115145,131452,49580,770508,237011957,2310176,131104,49280,245788,4277001,
32924,138414094,4277641,140509501,136321067,2164553,134267338,180684,239092725,4325408,4260032,4243465,4292745,4292937,344092,136364157,
136331421,134267101,140525885,138461610,136331722,274727922,345044,4423712,4358336,4292681,4227401,140525626,327772,196764,147708,316,
393564,623020,426444,241288149,656372,771060,885748,2113568,16416,196736,32796,140558429,138444989,138428669,136347933,136321483,
134250877,2148297,268485586,17364,2293792,2228416,4292617,4292809,4194697,163868,134217853,4228169,138415054,138417099,136364317,2164489,
136315229,134267309,136321550,115668,2326560,147488,160,262172,136314973,134267037,140558589,138445117,136321227,136321611,134316490,272630738,
134300109,4341792,4276416,4194377,4243657,4227465,360476,138412157,136347805,134217981,140542269,2115337,140509533,138461613,136321774,361460,
4440096,4374720,4227209,4309321,140542010,344156,213148,164092,16700,409948,639404,442828,241288181,672724,787412,902100,
2129952,32800,164000,49180,140525693,138461373,4228425,138416078,2098761,380,770476,655820,234947541,2211904,2244800,4243529,
4227337,4276617,180252,134234237,4310089,4195529,196892,180572,65964,786892,237028309,2342944,2310304,16544,196668,136331357,
134217917,4261193,138415886,2164329,138416270,164252,134332845,136321582,134316493,4259904,4292800,4276297,4260105,4309385,278588,138428541,
136315069,134234365,140558653,2197257,138445181,2115529,274777042,345076,4358208,4391104,4309129,4243849,442428,138444925,4277321,138414606,
138416587,140558714,2180809,360860,138417870,276857810,459764,672756,787444,902132,2146336,49184,180384,65564,140542077,140509373,
4310345,4195785,2131561,33148,786860,672204,234947573,2228288,2261184,4259977,4309257,136314906,92,4227977,138415691,4293897,
4294089,134267290,2197289,98732,803276,237028341,4194336,2326688,32928,213052,136347741,134234301,138416139,138428794,2180841,2213673,
311724,82380,239109077,4276288,4309184,4210825,4211017,138444826,213084,147612,49404,442684,328028,459180,295372,241189877,
4374592,4407488,4260041,140525594,360540,229532,180476,33084,426332,655788,459212,243270613};
__device__ const unsigned k_qoff[257] = {0,2,4,6,23,39,54,70,88,90,92,94,111,127,142,157,173,175,177,179,196,213,229,247,263,265,267,269,285,302,319,337,353,355,357,359,374,391,408,424,439,441,443,445,460,475,492,508,523,525,527,529,544,560,576,594,608,610,612,614,631,646,661,678,693,695,697,699,715,729,746,761,776,778,780,782,798,812,826,841,857,859,861,863,878,892,907,921,937,939,941,943,959,975,991,1005,1019,1021,1023,1025,1039,1054,1070,1086,1101,1103,1105,1107,1122,1138,1158,1173,1188,1190,1192,1194,1209,1222,1238,1252,1266,1268,1270,1272,1287,1303,1318,1333,1347,1348,1349,1350,1366,1380,1395,1408,1422,1423,1424,1425,1440,1454,1470,1483,1497,1498,1499,1500,1517,1531,1545,1558,1571,1572,1573,1574,1590,1606,1620,1634,1647,1648,1649,1650,1664,1679,1693,1707,1719,1720,1721,1722,1736,1750,1764,1777,1790,1791,1792,1793,1807,1820,1834,1847,1862,1863,1864,1865,1879,1892,1905,1919,1933,1934,1935,1936,1951,1965,1979,1991,2004,2005,2006,2007,2020,2032,2045,2057,2070,2071,2072,2073,2086,2098,2110,2122,2137,2138,2139,2140,2154,2166,2178,2190,2205,2206,2207,2208,2221,2235,2249,2263,2276,2277,2278,2279,2292,2308,2321,2336,2349,2350,2351,2352,2365,2379,2393,2408,2423,2424,2425,2426,2439,2454,2467,2480,2492};
__device__ __forceinline__ float bflo(unsigned x) { return __uint_as_float(x << 16); }
__device__ __forceinline__ float bfhi(unsigned x) { return __uint_as_float(x & 0xffff0000u); }
__device__ __forceinline__ unsigned pk(float a, float b) { f32x2 v = {a, b}; return __builtin_bit_cast(unsigned, __builtin_convertvector(v, bf16x2)); }
__device__ __forceinline__ float rsum(float v) {
  v += __builtin_bit_cast(float, __builtin_amdgcn_update_dpp(0, __builtin_bit_cast(int, v), 0xb1, 0xf, 0xf, true));
  v += __builtin_bit_cast(float, __builtin_amdgcn_update_dpp(0, __builtin_bit_cast(int, v), 0x4e, 0xf, 0xf, true));
  v += __builtin_bit_cast(float, __builtin_amdgcn_update_dpp(0, __builtin_bit_cast(int, v), 0x128, 0xf, 0xf, true));
  auto a = __builtin_amdgcn_permlane16_swap(__float_as_uint(v), __float_as_uint(v), false, false); v = __uint_as_float(a[0]) + __uint_as_float(a[1]);
  auto b = __builtin_amdgcn_permlane32_swap(__float_as_uint(v), __float_as_uint(v), false, false); return __uint_as_float(b[0]) + __uint_as_float(b[1]); }
__device__ __forceinline__ void ln32(float (&v)[32], u32x4 (&o)[4]) {
  float sum = 0.f, q = 0.f;
#pragma unroll
  for (int i = 0; i < 32; i += 4) sum += (v[i] + v[i + 1]) + (v[i + 2] + v[i + 3]);
  const float mean = rsum(sum) * (1.0f / 1024);
#pragma unroll
  for (int i = 0; i < 32; ++i) { v[i] -= mean; q += v[i] * v[i]; }
  const float rstd = 1.0f / sqrtf(rsum(q) * (1.0f / 1024) + 1e-5f);
#pragma unroll
  for (int k = 0; k < 4; ++k)
#pragma unroll
    for (int d = 0; d < 4; ++d) o[k][d] = pk(v[8 * k + 2 * d] * rstd, v[8 * k + 2 * d + 1] * rstd);
}
__device__ __forceinline__ bf16x8 B8(u32x4 v) { return __builtin_bit_cast(bf16x8, v); }
__device__ __forceinline__ s16x4 B4(u32x2 v) { return __builtin_bit_cast(s16x4, v); }
__device__ __forceinline__ float xmax(float v) {
  auto a = __builtin_amdgcn_permlane32_swap(__float_as_uint(v), __float_as_uint(v), false, false); v = fmaxf(__uint_as_float(a[0]), __uint_as_float(a[1]));
  auto b = __builtin_amdgcn_permlane16_swap(__float_as_uint(v), __float_as_uint(v), false, false); return fmaxf(__uint_as_float(b[0]), __uint_as_float(b[1])); }

__device__ __forceinline__ void wait_lines(char* ws, int wave, unsigned lane, unsigned line, unsigned n, unsigned target, unsigned& spins) {
  if (wave == 0) {
    GAS unsigned* p = (GAS unsigned*)(ws + CNT) + (line + (lane < n ? lane : 0)) * 32;
    while (!__all(__hip_atomic_load(p, __ATOMIC_RELAXED, __HIP_MEMORY_SCOPE_AGENT) >= target) && ++spins < SPIN) __builtin_amdgcn_s_sleep(2);
  }
  asm volatile("s_waitcnt vmcnt(0) lgkmcnt(0)\n s_barrier" ::: "memory");
}
__device__ __forceinline__ void signal(char* ws, int wave, unsigned lane, unsigned line, unsigned n, unsigned stride, unsigned hi, unsigned add) {
  asm volatile("s_waitcnt vmcnt(0) lgkmcnt(0)\n s_barrier" ::: "memory");
  if (wave == 0 && lane < n) __hip_atomic_fetch_add((GAS unsigned*)(ws + CNT) + (line + (lane & 1) * stride + (lane >> 1) * hi) * 32, add, __ATOMIC_RELAXED, __HIP_MEMORY_SCOPE_AGENT);
}

__device__ __forceinline__ void att_one(__amdgpu_buffer_rsrc_t rs, int s, int h, unsigned l, unsigned& spins) {
  const unsigned i16 = l & 15, g = l >> 4, blk = (s * 32 + h) * 8192 + l * 16;
  u32x4 qf[8], kf[8], vf[8];
#pragma unroll
  for (int f = 0; f < 8; ++f) { qf[f] = __builtin_amdgcn_raw_buffer_load_b128(rs, blk + f * 1024, QB, SC1); kf[f] = __builtin_amdgcn_raw_buffer_load_b128(rs, blk + f * 1024, KB, SC1); }
#pragma unroll
  for (int f = 0; f < 8; ++f) vf[f] = __builtin_amdgcn_raw_buffer_load_b128(rs, blk + f * 1024, VB, SC1);
  const unsigned mb = (s * 32 + h) * 2048, ko = mb + g * 128 + (i16 & 7) * 16, vo = l < 32 ? mb + l * 8 : OOB;
  u32x4 km[4]; u32x2 vm[8];
#pragma unroll
  for (int ks = 0; ks < 4; ++ks) km[ks] = __builtin_amdgcn_raw_buffer_load_b128(rs, ko + ks * 512, KMB, SC1);
#pragma unroll
  for (int t = 0; t < 8; ++t) vm[t] = __builtin_amdgcn_raw_buffer_load_b64(rs, vo + t * 256, VMB, SC1);
  __builtin_amdgcn_sched_barrier(0);
  const f32x4 z = {0.f, 0.f, 0.f, 0.f};
  f32x4 s00 = z, s01 = z, s11 = z;
#pragma unroll
  for (int ks = 0; ks < 4; ++ks) {
    s00 = __builtin_amdgcn_mfma_f32_16x16x32_bf16(B8(kf[ks]), B8(qf[ks]), s00, 0, 0, 0);
    s01 = __builtin_amdgcn_mfma_f32_16x16x32_bf16(B8(kf[ks]), B8(qf[4 + ks]), s01, 0, 0, 0);
    s11 = __builtin_amdgcn_mfma_f32_16x16x32_bf16(B8(kf[4 + ks]), B8(qf[4 + ks]), s11, 0, 0, 0);
  }
  for (;;) {
    unsigned mx = 0;
#pragma unroll
    for (int ks = 0; ks < 4; ++ks) mx = max(max(mx, max(km[ks].x, km[ks].y)), max(km[ks].z, km[ks].w));
#pragma unroll
    for (int t = 0; t < 8; ++t) mx = max(mx, max(vm[t].x, vm[t].y));
    if (!__any(mx == 0xffffffffu) || ++spins >= SPIN) break;
    for (;;) { asm volatile("" ::: "memory");
      if (RFL(__builtin_amdgcn_raw_buffer_load_b32(rs, mb + 2044, KMB, SC1)) != 0xffffffffu || ++spins >= SPIN) break;
      __builtin_amdgcn_s_sleep(2); }
    asm volatile("" ::: "memory");
#pragma unroll
    for (int ks = 0; ks < 4; ++ks) km[ks] = __builtin_amdgcn_raw_buffer_load_b128(rs, ko + ks * 512, KMB, SC1);
#pragma unroll
    for (int t = 0; t < 8; ++t) vm[t] = __builtin_amdgcn_raw_buffer_load_b64(rs, vo + t * 256, VMB, SC1);
    asm volatile("" ::: "memory");
  }
  f32x4 sm0 = z, sm1 = z;
#pragma unroll
  for (int ks = 0; ks < 4; ++ks) {
    sm0 = __builtin_amdgcn_mfma_f32_16x16x32_bf16(B8(km[ks]), B8(qf[ks]), sm0, 0, 0, 0);
    sm1 = __builtin_amdgcn_mfma_f32_16x16x32_bf16(B8(km[ks]), B8(qf[4 + ks]), sm1, 0, 0, 0);
  }
  const float sc = 0.08838834764831845f, L2E = 1.4426950408889634f;
  float m0 = -3e38f, m1 = -3e38f;
#pragma unroll
  for (int r = 0; r < 4; ++r) {
    const bool vis = 4 * g + r <= i16;
    s00[r] = vis ? s00[r] * sc : -1e9f; s01[r] *= sc; s11[r] = vis ? s11[r] * sc : -1e9f;
    sm0[r] *= sc; sm1[r] *= sc;
    m0 = fmaxf(m0, fmaxf(s00[r], sm0[r])); m1 = fmaxf(m1, fmaxf(fmaxf(s01[r], s11[r]), sm1[r]));
  }
  m0 = fmaxf(xmax(m0), -1e9f); m1 = xmax(m1);
  const float pmask = __builtin_amdgcn_exp2f((-1e9f - m0) * L2E);
#pragma unroll
  for (int r = 0; r < 4; ++r) {
    s00[r] = __builtin_amdgcn_exp2f((s00[r] - m0) * L2E); s01[r] = __builtin_amdgcn_exp2f((s01[r] - m1) * L2E); s11[r] = __builtin_amdgcn_exp2f((s11[r] - m1) * L2E);
    sm0[r] = g < 2 ? __builtin_amdgcn_exp2f((sm0[r] - m0) * L2E) : 0.f; sm1[r] = g < 2 ? __builtin_amdgcn_exp2f((sm1[r] - m1) * L2E) : 0.f;
  }
  const unsigned pm = pk(pmask, pmask);
  const u32x4 p0 = {pk(s00[0], s00[1]), pk(s00[2], s00[3]), pm, pm}, p1 = {pk(s01[0], s01[1]), pk(s01[2], s01[3]), pk(s11[0], s11[1]), pk(s11[2], s11[3])};
  const u32x2 q0 = {pk(sm0[0], sm0[1]), pk(sm0[2], sm0[3])}, q1 = {pk(sm1[0], sm1[1]), pk(sm1[2], sm1[3])};
  const u32x4 one = {0x3f803f80u, 0x3f803f80u, 0x3f803f80u, 0x3f803f80u}; const u32x2 one2 = {0x3f803f80u, 0x3f803f80u};
  f32x4 l0 = __builtin_amdgcn_mfma_f32_16x16x32_bf16(B8(one), B8(p0), z, 0, 0, 0), l1 = __builtin_amdgcn_mfma_f32_16x16x32_bf16(B8(one), B8(p1), z, 0, 0, 0);
  l0 = __builtin_amdgcn_mfma_f32_16x16x16bf16_1k(B4(one2), B4(q0), l0, 0, 0, 0); l1 = __builtin_amdgcn_mfma_f32_16x16x16bf16_1k(B4(one2), B4(q1), l1, 0, 0, 0);
  const float r0 = 1.0f / l0[0], r1 = 1.0f / l1[0];
  const unsigned gp = ((g & 1) << 1) | (g >> 1), ob = s * 262144 + h * 4096 + i16 * 64 + gp * 16;
#pragma unroll
  for (int pr = 0; pr < 4; ++pr) {
    u32x4 o0, o1;
#pragma unroll
    for (int hh = 0; hh < 2; ++hh) {
      const int t = 2 * pr + hh;
      f32x4 a = __builtin_amdgcn_mfma_f32_16x16x32_bf16(B8(vf[t]), B8(p0), z, 0, 0, 0), b = __builtin_amdgcn_mfma_f32_16x16x32_bf16(B8(vf[t]), B8(p1), z, 0, 0, 0);
      a = __builtin_amdgcn_mfma_f32_16x16x16bf16_1k(B4(vm[t]), B4(q0), a, 0, 0, 0); b = __builtin_amdgcn_mfma_f32_16x16x16bf16_1k(B4(vm[t]), B4(q1), b, 0, 0, 0);
      o0[2 * hh] = pk(a[0] * r0, a[1] * r0); o0[2 * hh + 1] = pk(a[2] * r0, a[3] * r0); o1[2 * hh] = pk(b[0] * r1, b[1] * r1); o1[2 * hh + 1] = pk(b[2] * r1, b[3] * r1);
    }
#pragma unroll
    for (int d = 0; d < 2; ++d) {
      auto x = __builtin_amdgcn_permlane16_swap(o0[d], o0[2 + d], false, false); o0[d] = x[0]; o0[2 + d] = x[1];
      auto y = __builtin_amdgcn_permlane16_swap(o1[d], o1[2 + d], false, false); o1[d] = y[0]; o1[2 + d] = y[1];
    }
    __builtin_amdgcn_raw_buffer_store_b128(o0, rs, ob + pr * 1024, OATT, SC1);
    __builtin_amdgcn_raw_buffer_store_b128(o1, rs, ob + 131072 + pr * 1024, OATT, SC1);
  }
}
template <int WHICH, int NSP>
__device__ __forceinline__ void red_row(__amdgpu_buffer_rsrc_t rs, const u16* x, u16* out, int row0, unsigned l) {
  const unsigned row = row0 + ((l >> 2) & 1);
  const unsigned bo = (row >> 4) * 32768 + (row & 15) * 64 + (l >> 3) * 1024 + (l & 3) * 16, ro = row * 2048 + (l >> 3) * 64 + (l & 3) * 16;
  u32x4 p[NSP][4], rv[4];
#pragma unroll
  for (int k = 0; k < 4; ++k) rv[k] = WHICH ? __builtin_amdgcn_raw_buffer_load_b128(rs, bo + k * 8192, HB, SC1) : *(const GAS u32x4*)((const GAS char*)x + ro + k * 512);
#pragma unroll
  for (int sp = 0; sp < NSP; ++sp)
#pragma unroll
    for (int k = 0; k < 4; ++k) p[sp][k] = __builtin_amdgcn_raw_buffer_load_b128(rs, bo + k * 8192, (WHICH ? PART2 : PART) + sp * 4194304, SC1);
  __builtin_amdgcn_sched_barrier(0);
  float v[32];
#pragma unroll
  for (int k = 0; k < 4; ++k) {
    u32x4 hv;
#pragma unroll
    for (int d = 0; d < 4; ++d) {
      float a = bflo(p[0][k][d]), c = bfhi(p[0][k][d]);
#pragma unroll
      for (int sp = 1; sp < NSP; ++sp) { a += bflo(p[sp][k][d]); c += bfhi(p[sp][k][d]); }
      hv[d] = pk(bflo(rv[k][d]) + a, bfhi(rv[k][d]) + c);
      v[8 * k + 2 * d] = bflo(hv[d]); v[8 * k + 2 * d + 1] = bfhi(hv[d]);
    }
    if (WHICH) *(GAS u32x4*)((GAS char*)out + ro + k * 512) = hv;
    else __builtin_amdgcn_raw_buffer_store_b128(hv, rs, bo + k * 8192, HB, SC1);
  }
  if (!WHICH) {
    u32x4 o[4]; ln32(v, o);
#pragma unroll
    for (int k = 0; k < 4; ++k) __builtin_amdgcn_raw_buffer_store_b128(o[k], rs, bo + k * 8192, HN, SC1);
  }
}

__global__ void __launch_bounds__(256) prep_k(const u16* x, const u16* mem0, char* ws) {
  const unsigned l = threadIdx.x & 63, t = blockIdx.x * 256 + threadIdx.x, n = gridDim.x * 256;
  const unsigned row = blockIdx.x * 8 + (threadIdx.x >> 6) * 2 + ((l >> 2) & 1);
  const unsigned bo = (row >> 4) * 32768 + (row & 15) * 64 + (l >> 3) * 1024 + (l & 3) * 16, ro = row * 2048 + (l >> 3) * 64 + (l & 3) * 16;
  u32x4 rv[4], o[4]; float v[32];
#pragma unroll
  for (int k = 0; k < 4; ++k) rv[k] = *(const u32x4*)((const char*)x + ro + k * 512);
  const uint4 s = make_uint4(~0u, ~0u, ~0u, ~0u);
  for (unsigned i = t + 1280; i < (CNT - MEMB) / 16; i += n) ((uint4*)(ws + MEMB))[i] = s;
  if (t < 1024) { unsigned c = t >> 5, rh = (t >> 4) & 1, p = (t >> 2) & 3, mm = t & 3; ((uint4*)(ws + MEMB))[(t >> 8) * 320 + (t & 255)] = *(const uint4*)(mem0 + (4 * rh + mm) * 1024 + 32 * c + 8 * p); }
  if (t < 11328) ((uint4*)(ws + CNT))[t] = make_uint4(0, 0, 0, 0);
#pragma unroll
  for (int k = 0; k < 4; ++k)
#pragma unroll
    for (int d = 0; d < 4; ++d) { v[8 * k + 2 * d] = bflo(rv[k][d]); v[8 * k + 2 * d + 1] = bfhi(rv[k][d]); }
  ln32(v, o);
#pragma unroll
  for (int k = 0; k < 4; ++k) *(u32x4*)(ws + XN + bo + k * 8192) = o[k];
}

struct Args { char* ws; const u16 *x, *Wq, *Wk, *Wv, *Wo, *Wg, *Wu, *Wd; u16* out; int wq, wv, wo, pad; };
typedef const __attribute__((address_space(4))) Args* CArgs;
__global__ void __launch_bounds__(256, 1) mega_k(Args A0) {
  const int b = blockIdx.x, lab = b & 7, o = b >> 3, wave0 = __builtin_amdgcn_readfirstlane(threadIdx.x >> 6);
  const unsigned dbgo = DBG + ((lab * 32 + o) * 4 + wave0) * 2048;
  if (lab == 0) qk_role(A0.ws, A0.Wq, A0.Wk, o, wave0, A0.wq, dbgo);
  else if (lab <= 2) vo_role(A0.ws, A0.Wv, A0.Wo, o, lab - 1, wave0, A0.wv, A0.wo, dbgo);
  unsigned spins = 0;
  const unsigned qe = k_qoff[b + 1];
  for (unsigned idx = k_qoff[b];; ++idx) {
    CArgs ap = (CArgs)__builtin_amdgcn_kernarg_segment_ptr();
    int wave = wave0; asm volatile("" : "+s"(ap), "+s"(wave)); spins = RFL(spins);
    char* ws = ap->ws; const u16 *x = ap->x, *Wq = ap->Wq, *Wk = ap->Wk, *Wv = ap->Wv, *Wo = ap->Wo, *Wg = ap->Wg, *Wu = ap->Wu, *Wd = ap->Wd; u16* out = ap->out;
    const unsigned l = lane_id();
    if (idx >= qe) break;
    const unsigned it = RFL(k_items[idx]);
    const unsigned ty = it & 7, cf = (it >> 3) & 3, ia = (it >> 5) & 511, ib = (it >> 14) & 127, ic = (it >> 21) & 15, id = (it >> 25) & 15;

    if (ty == 1 || ty == 3 || ty == 6) {
      const __amdgpu_buffer_rsrc_t rs = __builtin_amdgcn_make_buffer_rsrc(ws, 0, 251658240, 0x00020000);
      if (ty == 1) {
        const unsigned G = ia >> 2;
        wait_lines(ws, wave, l, 8 + G * 32 + ib * 4, 4, 516, spins);
        for (unsigned k = 0; k < ic; ++k) att_one(rs, ia + k, ib * 4 + wave, l, spins);
        signal(ws, wave, l, 520 + G * 8 + ib, 1, 0, 0, ic);
      } else {
        const unsigned G = ia >> 4;
        wait_lines(ws, wave, l, (ty == 3 ? 648 : 1288) + G * 8, id, 16, spins);

        for (unsigned k = 0; k < ic; ++k) { const int row = (ia + k) * 8 + wave * 2;
          if (ty == 3) { if (id == 4) red_row<0, 4>(rs, x, out, row, l); else red_row<0, 8>(rs, x, out, row, l); }
          else { if (id == 4) red_row<1, 4>(rs, x, out, row, l); else red_row<1, 7>(rs, x, out, row, l); } }
        if (ty == 3) signal(ws, wave, l, 776 + G * 4 + ((ia >> 2) & 3), 1, 0, 0, ic);
      }
    } else {
      const unsigned G = cf == 0 ? ia * 2 : cf == 2 ? ia >> 1 : ia, wt = cf == 2 ? 1 : 2, wide = cf == 3 ? 2 : 1;
      GP P; unsigned sl, sn = 1, sadd = wt;
      if (ty == 0) {
        const u16* W = ic == 0 ? Wq : ic == 1 ? Wk : Wv;
        P = cf == 0 ? item_qkv_L(W, ic, ia, ib, wave) : cf == 1 ? item_qkv_S(W, ic, ia, ib, wave) : cf == 2 ? item_qkv_X(W, ic, ia, ib, wave) : item_qkv_M(W, ic, ia, ib, wave);
        sl = 8 + G * 32 + (cf == 0 || cf == 3 ? ib * 2 : ib); sn = cf == 0 ? 4 : wide; sadd = ic == 0 ? wt * 256 : wt;
      } else if (ty == 4) {
        P = cf == 1 ? item_gu_S(Wg, Wu, ia, ib, wave) : cf == 2 ? item_gu_X(Wg, Wu, ia, ib, wave) : item_gu_M(Wg, Wu, ia, ib, wave);
        sl = 840 + G * 28 + (cf == 3 ? ib : ib >> 1); sadd = wt * wide;
        wait_lines(ws, wave, l, 776 + G * 4, 4, 4, spins);
      } else {
        const bool dn = ty == 5; const int nk = dn ? (id == 4 ? 14 : 8) : (id == 4 ? 16 : 8);
        P = cf == 1 ? item_part_S(dn ? Wd : Wo, dn, ia, ib, ic * nk * 64, nk, ic, wave) : cf == 2 ? item_part_X(dn ? Wd : Wo, dn, ia, ib, ic * nk * 64, nk, ic, wave) : item_part_M(dn ? Wd : Wo, dn, ia, ib, ic * nk * 64, nk, ic, wave);
        sl = (dn ? 1288 : 648) + G * 8 + ic; sadd = wt * wide;
        if (dn) wait_lines(ws, wave, l, 840 + G * 28 + ic * (nk >> 1), nk >> 1, 4, spins);
        else wait_lines(ws, wave, l, 520 + G * 8 + ic * (nk >> 3), nk >> 3, 4, spins);
      }

      if (cf == 0) gemm_L(ws, wave, P); else if (cf == 1) gemm_S(ws, wave, P); else if (cf == 2) gemm_X(ws, wave, P); else gemm_M(ws, wave, P);
      const unsigned l2 = lane_id();
      signal(ws, wave, l2, sl, sn, 1, 32, sadd);
    }

  }
}

extern "C" void kernel_launch(void* const* d_in, const int* in_sizes, int n_in, void* d_out, int out_size, void* d_ws, size_t ws_size, hipStream_t st) {
  prep_k<<<256, 256, 0, st>>>((const u16*)d_in[0], (const u16*)d_in[1], (char*)d_ws);
  (void)hipFuncSetAttribute((const void*)mega_k, hipFuncAttributeMaxDynamicSharedMemorySize, 163840);
  Args A{(char*)d_ws, (const u16*)d_in[0], (const u16*)d_in[2], (const u16*)d_in[3], (const u16*)d_in[4], (const u16*)d_in[5], (const u16*)d_in[8], (const u16*)d_in[9], (const u16*)d_in[10], (u16*)d_out, 40, 500, 32, 0};
  mega_k<<<256, 256, 163840, st>>>(A);
}
```
